```python
import torch

def _softmax(x, axis=-1):
    return torch.softmax(x, dim=axis)
def _gelu(x):
    return torch.nn.functional.gelu(x, approximate="tanh")
def _silu(x):
    return torch.nn.functional.silu(x)
def _sigmoid(x):
    return torch.sigmoid(x)
def _layernorm(x, axis=-1, eps=1e-5):
    m = x.mean(dim=axis, keepdim=True)
    v = x.var(dim=axis, keepdim=True, unbiased=False)
    return (x - m) / torch.sqrt(v + eps)
def _scatter_add(tgt, idx, src, axis=0):
    return tgt.clone().index_add_(axis, idx, src)
def _segment_sum(src, seg, n):
    out = torch.zeros((n,) + src.shape[1:], dtype=src.dtype)
    return out.index_add_(0, seg, src)
def _pad(x, pw):
    flat = tuple(v for pair in reversed(pw) for v in pair)
    return torch.nn.functional.pad(x, flat)

kernel_name = 'recurrent_scan__linear_kernel_hedgehog__dense_activation_silu__prompt'

def setup_inputs():

    _w = (lambda a: (lambda t: t.to(torch.bfloat16) if t.is_floating_point() else t)(torch.as_tensor(a)))
    def _A_setup():
        B, S, D, H = 1, 2048, 512, 32
        Dh = D // H
        return {
            "x": _w((lambda *s: torch.randn(*s, dtype=torch.bfloat16) * 0.05)(B, S, D)),
            "wqkv": _w((lambda *s: torch.randn(*s, dtype=torch.bfloat16) * 0.4)(D, 3 * D)),
            "w_phi": _w((lambda *s: torch.randn(*s, dtype=torch.bfloat16) * 0.4)(H, Dh, Dh) * (Dh ** -0.5)),
            "wo": _w((lambda *s: torch.randn(*s, dtype=torch.bfloat16) * 0.2)(D, D)),
        }
    _A_in = _A_setup()

    def _B_setup():
        B, S, D, F = 1, 2048, 512, 1792
        return {
            "x": _w((lambda *s: torch.randn(*s, dtype=torch.bfloat16) * 0.05)(B, S, D)),
            "w1": _w((lambda *s: torch.randn(*s, dtype=torch.bfloat16) * 0.4)(D, F) * (D ** -0.5)),
            "w2": _w((lambda *s: torch.randn(*s, dtype=torch.bfloat16) * 0.1)(F, D) * (F ** -0.5)),
        }
    _B_in = _B_setup()

    return {
        "A_x": _A_in["x"],
        "A_wqkv": _A_in["wqkv"],
        "A_w_phi": _A_in["w_phi"],
        "A_wo": _A_in["wo"],
        "B_x": _B_in["x"],
        "B_w1": _B_in["w1"],
        "B_w2": _B_in["w2"],
    }

def reference(A_x, A_wqkv, A_w_phi, A_wo, B_x, B_w1, B_w2):
    def _A_ref(x, wqkv, w_phi, wo):
        B, S, D = x.shape
        H = 32; Dh = D // H
        qkv = (lambda x, *d: x.permute(*d))((x @ wqkv).reshape(B, S, 3, H, Dh), 2, 0, 3, 1, 4)
        q, k, v = qkv[0], qkv[1], qkv[2]
        pq = torch.einsum("bhsd,hdf->bhsf", q, w_phi)
        pk = torch.einsum("bhsd,hdf->bhsf", k, w_phi)
        phi_q = torch.concatenate([_softmax(pq, axis=-1), _softmax(-pq, axis=-1)], axis=-1)
        phi_k = torch.concatenate([_softmax(pk, axis=-1), _softmax(-pk, axis=-1)], axis=-1)
        kv = torch.einsum("bhsf,bhsd->bhfd", phi_k, v)
        z  = torch.einsum("bhsf->bhf", phi_k)
        num = torch.einsum("bhqf,bhfd->bhqd", phi_q, kv)
        den = torch.einsum("bhqf,bhf->bhq", phi_q, z)[..., None] + 1e-6
        o = num / den
        return (lambda x, *d: x.permute(*d))(o, 0, 2, 1, 3).reshape(B, S, D) @ wo

    def _B_ref(x, w1, w2):
        h = torch.einsum("bsd,df->bsf", x, w1)
        h = _silu(h)
        out = torch.einsum("bsf,fd->bsd", h, w2)
        return out

    x = A_x
    xn = _layernorm(x, axis=-1)
    s = _A_ref(xn, A_wqkv, A_w_phi, A_wo)
    g = _silu(_B_ref(xn, B_w1, B_w2))
    return x + s * g
```

```cpp
#include <hip/hip_runtime.h>
#include <hip/hip_bf16.h>
typedef unsigned short u16;
__device__ inline float bf2f(u16 v){ return __uint_as_float(((unsigned)v)<<16); }
__device__ inline u16 f2bf(float f){ unsigned u=__float_as_uint(f); u += 0x7fffu + ((u>>16)&1u); return (u16)(u>>16); }
__device__ inline float rb(float f){ return bf2f(f2bf(f)); }
constexpr int S=2048,D=512,H=32,DH=16,F=1792;

__global__ void k_ln(const u16* x, u16* xn){
  int row=blockIdx.x; __shared__ float red[2][4];
  int t=threadIdx.x; float v0=bf2f(x[row*D+t]), v1=bf2f(x[row*D+t+256]);
  float s=v0+v1; for(int m=32;m>=1;m>>=1) s+=__shfl_xor(s,m);
  if((t&63)==0) red[0][t>>6]=s; __syncthreads();
  float mean=(red[0][0]+red[0][1]+red[0][2]+red[0][3])/D;
  float d0=v0-mean,d1=v1-mean; float q=d0*d0+d1*d1; for(int m=32;m>=1;m>>=1) q+=__shfl_xor(q,m);
  if((t&63)==0) red[1][t>>6]=q; __syncthreads();
  float var=(red[1][0]+red[1][1]+red[1][2]+red[1][3])/D;
  float r=rsqrtf(var+1e-5f);
  xn[row*D+t]=f2bf(d0*r); xn[row*D+t+256]=f2bf(d1*r);
}
__global__ void k_gemm(const u16* A,const u16* B,u16* C,int M,int N,int K,int act){
  int n=blockIdx.x*64+(threadIdx.x&63); int m=blockIdx.y*4+(threadIdx.x>>6);
  float acc=0; for(int k=0;k<K;k++) acc+=bf2f(A[m*K+k])*bf2f(B[k*N+n]);
  if(act){ acc=rb(acc); acc=acc/(1.f+__expf(-acc)); }
  C[m*N+n]=f2bf(acc);
}
__global__ void k_phi(const u16* qkv,const u16* wphi,u16* phi){
  int idx=blockIdx.x*256+threadIdx.x;
  int s=idx%S; int h=(idx/S)%H; int w=idx/(S*H);
  float p[16];
  for(int f=0;f<16;f++){ float a=0; for(int d=0;d<16;d++) a+=bf2f(qkv[s*1536+w*512+h*16+d])*bf2f(wphi[h*256+d*16+f]); p[f]=rb(a);}
  float mx=p[0],mn=p[0]; for(int f=1;f<16;f++){mx=fmaxf(mx,p[f]);mn=fminf(mn,p[f]);}
  float e1[16],e2[16],z1=0,z2=0; for(int f=0;f<16;f++){e1[f]=__expf(p[f]-mx);e2[f]=__expf(mn-p[f]);z1+=e1[f];z2+=e2[f];}
  u16* o=phi+(size_t)idx*32; for(int f=0;f<16;f++){o[f]=f2bf(e1[f]/z1);o[16+f]=f2bf(e2[f]/z2);}
}
__global__ void k_kv(const u16* qkv,const u16* phi,float* kv,float* z){
  int h=blockIdx.x; int t=threadIdx.x;
  int f=t/16,d=t%16; const u16* pk=phi+(size_t)(H+h)*S*32; float a=0,zz=0;
  for(int s=0;s<S;s++){ float p=bf2f(pk[s*32+f]); a+=p*bf2f(qkv[s*1536+1024+h*16+d]); zz+=p;}
  kv[h*512+t]=rb(a); if(d==0) z[h*32+f]=rb(zz);
}
__global__ void k_o(const u16* phi,const float* kv,const float* z,u16* o){
  int idx=blockIdx.x*256+threadIdx.x; int c=idx%512,s=idx/512; int h=c/16,d=c%16;
  const u16* pq=phi+((size_t)h*S+s)*32; float n=0,de=0; for(int f=0;f<32;f++){float p=bf2f(pq[f]); n+=p*kv[h*512+f*16+d]; de+=p*z[h*32+f];}
  o[idx]=f2bf(rb(n)/(rb(rb(de)+1e-6f)));
}
__global__ void k_fin(const u16* x,const u16* s,const u16* g,u16* out){
  int idx=blockIdx.x*256+threadIdx.x; float gg=bf2f(g[idx]);
  out[idx]=f2bf(bf2f(x[idx])+rb(bf2f(s[idx])*gg));
}
extern "C" void kernel_launch(void* const* d_in,const int* in_sizes,int n_in,void* d_out,int out_size,void* d_ws,size_t ws_size,hipStream_t st){
  const u16* x=(const u16*)d_in[0]; const u16* wqkv=(const u16*)d_in[1]; const u16* wphi=(const u16*)d_in[2]; const u16* wo=(const u16*)d_in[3];
  const u16* w1=(const u16*)d_in[5]; const u16* w2=(const u16*)d_in[6];
  char* ws=(char*)d_ws; u16* xn=(u16*)ws; u16* qkv=(u16*)(ws+(4<<20)); u16* hh=(u16*)(ws+(16<<20)); u16* phi=(u16*)(ws+(32<<20));
  float* kv=(float*)(ws+(48<<20)); float* z=(float*)(ws+(49<<20)); u16* o=(u16*)(ws+(50<<20)); u16* sb=(u16*)(ws+(54<<20)); u16* g=(u16*)(ws+(58<<20)); u16* g0=(u16*)(ws+(62<<20));
  k_ln<<<S,256,0,st>>>(x,xn);
  k_gemm<<<dim3(1536/64,S/4),256,0,st>>>(xn,wqkv,qkv,S,1536,D,0);
  k_gemm<<<dim3(F/64,S/4),256,0,st>>>(xn,w1,hh,S,F,D,1);
  k_gemm<<<dim3(D/64,S/4),256,0,st>>>(hh,w2,g,S,D,F,1);
  k_phi<<<2*H*S/256,256,0,st>>>(qkv,wphi,phi);
  k_kv<<<H,512,0,st>>>(qkv,phi,kv,z);
  k_o<<<S*D/256,256,0,st>>>(phi,kv,z,o);
  k_gemm<<<dim3(D/64,S/4),256,0,st>>>(o,wo,sb,S,D,D,0);
  k_fin<<<S*D/256,256,0,st>>>(x,sb,g,(u16*)d_out);
}
```

```cpp
#include <hip/hip_runtime.h>

typedef unsigned short u16;
typedef unsigned u32;
typedef unsigned long long u64;
typedef short bf16x8 __attribute__((ext_vector_type(8)));
typedef short s16x4 __attribute__((ext_vector_type(4)));
typedef __bf16 bf2 __attribute__((ext_vector_type(2)));
typedef float f32x16 __attribute__((ext_vector_type(16)));
typedef float f32x4 __attribute__((ext_vector_type(4)));
typedef u32 u32x4 __attribute__((ext_vector_type(4)));
typedef u32 u32x2 __attribute__((ext_vector_type(2)));
#define LAS __attribute__((address_space(3)))
#define GAS __attribute__((address_space(1)))
#define DI __device__ __forceinline__
#define RLX __ATOMIC_RELAXED, __HIP_MEMORY_SCOPE_AGENT
#define RLXW __ATOMIC_RELAXED, __HIP_MEMORY_SCOPE_WORKGROUP

#define TIMING 0

constexpr int S_ = 2048, D_ = 512, F_ = 1792;
constexpr float LOG2E = 1.4426950408889634f;

constexpr u32 WS_E = 0;
constexpr u32 WS_FH = 4096;
constexpr u32 WS_FK = WS_FH + 32768;
constexpr u32 WS_FO = WS_FK + 32768;
constexpr u32 WS_KV = 131072;
constexpr u32 WS_H = 1u << 20;
constexpr u32 WS_O = WS_H + 64u * 112u * 1024u;
constexpr u32 WS_END = WS_O + 64u * 32u * 1024u;
constexpr u32 WS_TM = 16u << 20;

constexpr u32 W1R = 0, NW1 = 5;
constexpr u32 X1R = 81920, NX1 = 4;
constexpr u32 W2R = 0, H2R = 98304;
constexpr u32 L_PHI = 98304, L_V = 131072;
constexpr u32 L_KVA = 147456, L_CSF = 151808;
constexpr u32 MISC = 157696;
constexpr u32 L_SUM = MISC, L_SQ = MISC + 512, L_CSP = MISC + 1024, L_CNT = MISC + 5120, L_TAG = L_CNT + 32, L_ONES = MISC + 5184;
enum { C_H = 0, C_PHI = 1, C_KV = 2, C_CH = 3, C_O = 4, C_HR = 5 };
constexpr u32 SPIN_CAP = 1u << 22;

__shared__ __attribute__((aligned(1024))) u32 lds32[40960];
#define lds ((char*)lds32)

DI f32x16 mfma32(bf16x8 a, bf16x8 b, f32x16 c) { return __builtin_amdgcn_mfma_f32_32x32x16_bf16(a, b, c, 0, 0, 0); }
DI u32 pk2(float a, float b) { bf2 v = {(__bf16)a, (__bf16)b}; return __builtin_bit_cast(u32, v); }
DI float dot2(u32 a, u32 b, float c) { return __builtin_amdgcn_fdot2_f32_bf16(__builtin_bit_cast(bf2, a), __builtin_bit_cast(bf2, b), c, false); }
DI float ex2(float x) { return __builtin_amdgcn_exp2f(x); }
DI float rcp(float x) { return __builtin_amdgcn_rcpf(x); }
DI u32 f2u(float x) { return __builtin_bit_cast(u32, x); }
DI float u2f(u32 x) { return __builtin_bit_cast(float, x); }
DI bf16x8 ld128(u32 off) { return *(LAS bf16x8*)(lds + off); }
DI f32x4 ldf4(u32 off) { return *(LAS f32x4*)(lds + off); }
DI float ldf(u32 off) { return *(LAS float*)(lds + off); }
DI void stf(u32 off, float v) { *(LAS float*)(lds + off) = v; }
DI bf16x8 ldtr(u32 off0, u32 off1) {
  s16x4 a = __builtin_amdgcn_ds_read_tr16_b64_v4i16((LAS s16x4*)(lds + off0));
  s16x4 b = __builtin_amdgcn_ds_read_tr16_b64_v4i16((LAS s16x4*)(lds + off1));
  return __builtin_shufflevector(a, b, 0, 1, 2, 3, 4, 5, 6, 7);
}
DI bf16x8 asfrag(u32 a, u32 b, u32 c, u32 d) { u32x4 v = {a, b, c, d}; return __builtin_bit_cast(bf16x8, v); }
DI void barrier() { asm volatile("s_barrier" ::: "memory"); }
DI void wait_lgkm0() { asm volatile("s_waitcnt lgkmcnt(0)" ::: "memory"); }
DI void wait_vm0() { asm volatile("s_waitcnt vmcnt(0)" ::: "memory"); }
template <int N> DI void wait_vm() { asm volatile("s_waitcnt vmcnt(%0)" ::"n"(N) : "memory"); }
DI float xsum(float x) { u32x2 r = __builtin_amdgcn_permlane32_swap(f2u(x), f2u(x), false, false); return u2f(r[0]) + u2f(r[1]); }
DI void xmaxmin(float& mx, float& mn) {
  u32x2 r = __builtin_amdgcn_permlane32_swap(f2u(mx), f2u(-mn), false, false);
  float m = fmaxf(u2f(r[0]), u2f(r[1]));
  u32x2 q = __builtin_amdgcn_permlane32_swap(f2u(m), f2u(m), false, false);
  mx = u2f(q[0]); mn = -u2f(q[1]);
}
DI void xsum2(float& a, float& b) {
  u32x2 r = __builtin_amdgcn_permlane32_swap(f2u(a), f2u(b), false, false);
  float m = u2f(r[0]) + u2f(r[1]);
  u32x2 q = __builtin_amdgcn_permlane32_swap(f2u(m), f2u(m), false, false);
  a = u2f(q[0]); b = u2f(q[1]);
}
DI u32 lds_add_rtn(u32 off) { return __hip_atomic_fetch_add((LAS u32*)&lds32[off >> 2], 1u, RLXW); }
DI u32 lds_ld(u32 off) { return __hip_atomic_load((LAS u32*)&lds32[off >> 2], RLXW); }
DI u32 arrive(int cnt, int lane) {
  asm volatile("" ::: "memory");
  u32 old = 0;
  if (lane == 0) old = lds_add_rtn(L_CNT + 4 * cnt);
  old = __builtin_amdgcn_readfirstlane(old);
  asm volatile("" ::: "memory");
  return old;
}
DI void lds_spin(int cnt, u32 target) {
  for (u32 it = 0; it < SPIN_CAP; ++it) {
    if (__builtin_amdgcn_readfirstlane(lds_ld(L_CNT + 4 * cnt)) >= target) break;
    __builtin_amdgcn_s_sleep(1);
  }
  asm volatile("" ::: "memory");
}
DI void poll16(const char* ws, u32 base, u32 stride, u64 tag, int lane, int mask) {
  GAS u64* p = (GAS u64*)(ws + base + (u32)(lane & mask) * stride);
  for (u32 it = 0; it < SPIN_CAP; ++it) {
    u64 v = __hip_atomic_load(p, RLX);
    if (__all(v == tag)) break;
    __builtin_amdgcn_s_sleep(1);
  }
  asm volatile("" ::: "memory");
}
DI void set_flag(char* ws, u32 off, u64 tag, int lane) {
  if (lane == 0) __hip_atomic_store((GAS u64*)(ws + off), tag, RLX);
}

#define STAMP(k) do { } while (0)
#define STAMP_DECL do { } while (0)
#define STAMP_DUMP do { } while (0)

struct Params { const u16* x; const u16* wqkv; const u16* wphi; const u16* wo; const u16* w1; const u16* w2; u16* out; char* ws; };

typedef __amdgpu_buffer_rsrc_t rsrc_t;
DI rsrc_t mkrs(const void* p, u32 bytes) { return __builtin_amdgcn_make_buffer_rsrc((void*)p, 0, bytes, 0x00020000); }
template <int AUX> DI void dma16(rsrc_t rs, u32 ldsoff, u32 voff, u32 soff) {
  __builtin_amdgcn_raw_ptr_buffer_load_lds(rs, (LAS void*)(lds + ldsoff), 16, voff, soff, 0, AUX);
}

template <int C> DI void p2_issue_h(rsrc_t rws, int wl, u32 hv, int i2) {
  constexpr u32 slot = H2R + (C % 3) * 16384;
#pragma unroll
  for (int q = 0; q < 4; ++q) {
    const int pc = wl * 4 + q;
    const int rb = pc >> 3, kk = pc & 7;
    u32 src = C < 14 ? WS_H + ((2 * i2 + rb) * 112 + 8 * C + kk) * 1024 : WS_O + ((2 * i2 + rb) * 32 + 8 * (C - 14) + kk) * 1024;
    dma16<16>(rws, slot + pc * 1024, hv, src);
  }
}
template <int C> DI void p2_issue_w(rsrc_t rw2, rsrc_t rwo, int wl, u32 wv, int j2) {
  constexpr u32 slot = W2R + (C % 6) * 16384;
#pragma unroll
  for (int q = 0; q < 4; ++q) {
    const int pc = wl * 4 + q;
    if constexpr (C < 14) dma16<0>(rw2, slot + pc * 1024, wv, (128 * C + 8 * pc) * 1024 + j2 * 128);
    else dma16<0>(rwo, slot + pc * 1024, wv, (128 * (C - 14) + 8 * pc) * 1024 + j2 * 128);
  }
}
template <int C> DI void p2_loader_iter(rsrc_t rws, rsrc_t rw2, rsrc_t rwo, int wl, u32 hv, u32 wv, int i2, int j2) {
  constexpr int N = C == 0 ? 8 : C == 1 ? 4 : C == 2 ? 8 : C <= 13 ? 12 : C == 14 ? 8 : C <= 16 ? 4 : 0;
  wait_vm<N>();
  barrier();
  if constexpr (C >= 1 && C + 2 <= 17) p2_issue_h<C + 2>(rws, wl, hv, i2);
  if constexpr (C >= 1 && C + 5 <= 17) p2_issue_w<C + 5>(rw2, rwo, wl, wv, j2);
  if constexpr (C < 17) p2_loader_iter<C + 1>(rws, rw2, rwo, wl, hv, wv, i2, j2);
}

constexpr int p1_xh(int c) { return (c >= 2 && c <= 9) ? 2 : 0; }
constexpr int p1_w(int c, int nw) { return (c >= 1 && c + 4 <= 15) ? nw : 0; }
constexpr int p1_allow(int c, int nw) {
  if (c == 0) return 12 + 3 * nw;
  if (c == 1) return 8 + 2 * nw;
  if (c == 2) return 8 + nw + p1_w(1, nw);
  if (c == 3) return 4 + p1_w(1, nw) + p1_w(2, nw) + p1_xh(2);
  if (c >= 15) return 0;
  return p1_xh(c - 3) + p1_w(c - 2, nw) + p1_xh(c - 2) + p1_w(c - 1, nw) + p1_xh(c - 1);
}
struct L1 {
  rsrc_t rx, rq, r1;
  u32 xv, qv, hv;
  u32 xs, s0, s1, h0;
  int wl;
};
template <int NW> DI void p1_issue_w(const L1& l, int c) {
  const u32 slot = W1R + (c % NW1) * 16384 + l.wl * 1024;
  dma16<0>(l.rq, slot, l.qv, l.s0 + (32 * c + 8 * l.wl) * 3072);
  if constexpr (NW == 4) dma16<0>(l.rq, slot + 4096, l.qv, l.s1 + (32 * c + 8 * l.wl) * 3072);
  dma16<0>(l.r1, slot + 8192, l.hv, l.h0 + (32 * c + 8 * l.wl) * 3584);
  dma16<0>(l.r1, slot + 12288, l.hv, l.h0 + 128 + (32 * c + 8 * l.wl) * 3584);
}
DI void p1_issue_x(const L1& l, int P, int half) {
  const u32 slot = X1R + (P % NX1) * 16384;
#pragma unroll
  for (int u = 0; u < 2; ++u) {
    const int ix = l.wl + 4 * (2 * half + u);
    dma16<0>(l.rx, slot + ix * 1024, l.xv, l.xs + ix * 8192 + P * 128);
  }
}
template <int NW, int C> DI void p1_loader_iter(const L1& l) {
  wait_vm<p1_allow(C, NW)>();
  barrier();
  if constexpr (p1_w(C, NW) > 0) p1_issue_w<NW>(l, C + 4);
  if constexpr (p1_xh(C) > 0) p1_issue_x(l, (C + 6) >> 1, C & 1);
  if constexpr (C < 15) p1_loader_iter<NW, C + 1>(l);
}
template <int NW> DI void p1_loader(const L1& l) {
  p1_issue_x(l, 0, 0); p1_issue_x(l, 0, 1);
  p1_issue_w<NW>(l, 0); p1_issue_w<NW>(l, 1);
  p1_issue_x(l, 1, 0); p1_issue_x(l, 1, 1);
  p1_issue_w<NW>(l, 2); p1_issue_w<NW>(l, 3);
  p1_issue_x(l, 2, 0); p1_issue_x(l, 2, 1);
  p1_issue_w<NW>(l, 4);
  p1_issue_x(l, 3, 0); p1_issue_x(l, 3, 1);
  p1_loader_iter<NW, 0>(l);
}
DI int h_unit0(int p, int isq) { return isq ? ((p & 1) ? (7 * p + 3) >> 1 : (7 * p) >> 1) : ((p & 1) ? (7 * p - 1) >> 1 : (7 * p + 4) >> 1); }

DI void loader_main(const Params& p, int w, int lane, int i, int j) {
  STAMP_DECL;
  const int wl = w - 8, pr = j >> 1, isq = j & 1;
  L1 l;
  l.rx = mkrs(p.x, S_ * D_ * 2); l.rq = mkrs(p.wqkv, D_ * 1536 * 2); l.r1 = mkrs(p.w1, D_ * F_ * 2);
  l.wl = wl;
  const u32 gsw = ((((lane & 7) >> 2) ^ ((lane >> 4) & 1)) * 64) + (lane & 3) * 16;
  l.xv = (lane >> 3) * 1024 + (((lane & 7) ^ ((4 * (wl & 1) + (lane >> 4)) & 7)) * 16);
  l.qv = (lane >> 3) * 3072 + gsw;
  l.hv = (lane >> 3) * 3584 + gsw;
  l.xs = 128 * i * 1024;
  l.s0 = isq ? pr * 128 : 1024 + pr * 128;
  l.s1 = 2048 + pr * 128;
  l.h0 = h_unit0(pr, isq) * 128;
  STAMP(0);
  if (isq) p1_loader<3>(l); else p1_loader<4>(l);
  STAMP(2);
  barrier();
  STAMP(3);
  const rsrc_t rw2 = mkrs(p.w2, F_ * D_ * 2), rwo = mkrs(p.wo, D_ * D_ * 2), rws = mkrs(p.ws, WS_END);
  const int i2 = 2 * i + (j & 1), j2 = j >> 1;
  const u32 wv = (lane >> 3) * 1024 + ((((lane & 7) >> 2) ^ ((lane >> 4) & 1)) * 64) + (lane & 3) * 16;
  const u32 hv = lane * 16;
  p2_issue_w<0>(rw2, rwo, wl, wv, j2); p2_issue_w<1>(rw2, rwo, wl, wv, j2); p2_issue_w<2>(rw2, rwo, wl, wv, j2);
  p2_issue_w<3>(rw2, rwo, wl, wv, j2); p2_issue_w<4>(rw2, rwo, wl, wv, j2); p2_issue_w<5>(rw2, rwo, wl, wv, j2);
  STAMP(4);
  if (!isq) lds_spin(C_KV, 4);
  if (wl == 0) {
    const u64 tag = *(LAS u64*)(lds + L_TAG);
    poll16(p.ws, WS_FH + i * 16 * 128, 128, tag, lane, 15);
    if (lane == 0) __hip_atomic_store((LAS u32*)&lds32[(L_CNT >> 2) + C_HR], 1u, RLXW);
  } else lds_spin(C_HR, 1);
  STAMP(5);
  p2_issue_h<0>(rws, wl, hv, i2); p2_issue_h<1>(rws, wl, hv, i2); p2_issue_h<2>(rws, wl, hv, i2);
  p2_loader_iter<0>(rws, rw2, rwo, wl, hv, wv, i2, j2);
  STAMP(6);
  STAMP_DUMP;
}

template <int NT> struct Frag { bf16x8 x; bf16x8 w[NT]; };

template <int NT> struct P1 {
  u32 xa;
  u32 wa[NT];
  DI void init(int rg, int lane, const int (&tg)[NT]) {
    const int s = lane & 31, h = lane >> 5, blk = (lane >> 4) & 1, q = (lane & 15) >> 2, pp = lane & 3;
    xa = (32 * rg + s) * 128 + ((h ^ ((s >> 1) & 7)) * 16);
#pragma unroll
    for (int t = 0; t < NT; ++t) wa[t] = (tg[t] >> 1) * 4096 + (8 * h + q) * 128 + (((tg[t] & 1) ^ (q >> 1)) * 64) + 32 * blk + 8 * pp;
  }
  DI void read(Frag<NT>& f, u32 wslot, u32 xslot, int ks, int kx) const {
    f.x = ld128(xslot + (xa ^ (kx << 5)));
#pragma unroll
    for (int t = 0; t < NT; ++t) f.w[t] = ldtr(wslot + wa[t] + ks * 2048, wslot + wa[t] + ks * 2048 + 512);
  }
};

template <int STAT, int NT> DI void p1_step(const Frag<NT>& f, f32x16 (&acc)[NT], float& stat) {
#pragma unroll
  for (int t = 0; t < NT; ++t) acc[t] = mfma32(f.w[t], f.x, acc[t]);
  const u32x4 xv = __builtin_bit_cast(u32x4, f.x);
#pragma unroll
  for (int e = 0; e < 4; ++e) stat = dot2(xv[e], STAT == 0 ? 0x3f803f80u : xv[e], stat);
}
template <int NT> DI void p1_cs(const Frag<NT>& f, float (&cs)[NT]) {
#pragma unroll
  for (int t = 0; t < NT; ++t) {
    const u32x4 wv = __builtin_bit_cast(u32x4, f.w[t]);
#pragma unroll
    for (int e = 0; e < 4; ++e) cs[t] = dot2(wv[e], 0x3f803f80u, cs[t]);
  }
}

template <int STAT, int NT, bool LAST>
DI void p1_pair(const P1<NT>& a, Frag<NT>& f0, f32x16 (&acc)[NT], float& stat, float (&cs)[NT], int rg, int cm, u32 wsl, u32 w1, u32 w2, u32 xsl, u32 xn) {
  Frag<NT> f1;
  a.read(f1, wsl, xsl, 1, 1);
  p1_step<STAT>(f0, acc, stat);
  if (cm == rg) p1_cs(f0, cs);
  a.read(f0, w1, xsl, 0, 2);
  p1_step<STAT>(f1, acc, stat);
  if (cm == rg) p1_cs(f1, cs);
  wait_lgkm0();
  barrier();
  a.read(f1, w1, xsl, 1, 3);
  p1_step<STAT>(f0, acc, stat);
  if (cm + 1 == rg) p1_cs(f0, cs);
  if constexpr (!LAST) a.read(f0, w2, xn, 0, 0);
  p1_step<STAT>(f1, acc, stat);
  if (cm + 1 == rg) p1_cs(f1, cs);
  if constexpr (!LAST) { wait_lgkm0(); barrier(); }
}
template <int STAT, int NT> DI void p1_main(const P1<NT>& a, f32x16 (&acc)[NT], float& stat, float (&cs)[NT], int rg) {
  Frag<NT> f0;
#pragma unroll
  for (int t = 0; t < NT; ++t) asm volatile("" : "+v"(acc[t]));
  barrier();
  u32 wsl = W1R, xsl = X1R;
  a.read(f0, wsl, xsl, 0, 0);
#pragma unroll 1
  for (int cp = 0; cp < 7; ++cp) {
    const u32 w1 = wsl + 16384 == W1R + NW1 * 16384 ? W1R : wsl + 16384;
    const u32 w2 = w1 + 16384 == W1R + NW1 * 16384 ? W1R : w1 + 16384;
    const u32 xn = xsl + 16384 == X1R + NX1 * 16384 ? X1R : xsl + 16384;
    p1_pair<STAT, NT, false>(a, f0, acc, stat, cs, rg, 2 * (cp & 1), wsl, w1, w2, xsl, xn);
    wsl = w2; xsl = xn;
  }
  p1_pair<STAT, NT, true>(a, f0, acc, stat, cs, rg, 2, wsl, W1R + (15 % NW1) * 16384, 0, xsl, 0);
}

template <int STAT, int NT> DI void p1_finish(float stat, const float (&cs)[NT], const int (&tg)[NT], int rg, int lane) {
  const int s = lane & 31;
  stf((STAT == 0 ? L_SUM : L_SQ) + (32 * rg + s) * 4, xsum(stat));
#pragma unroll
  for (int t = 0; t < NT; ++t) stf(L_CSP + (rg * 8 + tg[t]) * 128 + s * 4, xsum(cs[t]));
  wait_lgkm0();
  barrier();
}
template <int NT> DI void cs_reduce(const int (&tg)[NT], int w8, int lane) {
  const int n = lane & 31;
  float v[NT];
#pragma unroll
  for (int t = 0; t < NT; ++t)
    v[t] = (ldf(L_CSP + (0 * 8 + tg[t]) * 128 + n * 4) + ldf(L_CSP + (1 * 8 + tg[t]) * 128 + n * 4)) +
           (ldf(L_CSP + (2 * 8 + tg[t]) * 128 + n * 4) + ldf(L_CSP + (3 * 8 + tg[t]) * 128 + n * 4));
#pragma unroll
  for (int t = 0; t < NT; ++t) stf(L_CSF + (w8 * 4 + t) * 128 + n * 4, v[t]);
  asm volatile("" ::: "memory");
}
DI void cs_get(float (&c)[16], int w8, int t, int lane) {
  const int h = lane >> 5;
#pragma unroll
  for (int g = 0; g < 4; ++g) {
    f32x4 v = ldf4(L_CSF + (w8 * 4 + t) * 128 + (8 * g + 4 * h) * 4);
    c[4 * g] = v[0]; c[4 * g + 1] = v[1]; c[4 * g + 2] = v[2]; c[4 * g + 3] = v[3];
  }
}

DI void silu_tile(const f32x16& acc, const float (&cs)[16], float nmu, float nrl, u32x4 (&o)[2]) {
  float y[16];
#pragma unroll
  for (int e = 0; e < 16; ++e) {
    const float t = fmaf(nmu, cs[e], acc[e]);
    y[e] = t * rcp(1.0f + ex2(t * nrl));
  }
#pragma unroll
  for (int u = 0; u < 2; ++u)
#pragma unroll
    for (int e = 0; e < 4; ++e) o[u][e] = pk2(y[8 * u + 2 * e], y[8 * u + 2 * e + 1]);
}

DI f32x16 phi_logits(const f32x16& acc, const float (&cs)[16], float nmu, bf16x8 a0, bf16x8 a1) {
  float t[16];
#pragma unroll
  for (int e = 0; e < 16; ++e) t[e] = fmaf(nmu, cs[e], acc[e]);
  bf16x8 b0 = asfrag(pk2(t[0], t[1]), pk2(t[2], t[3]), pk2(t[4], t[5]), pk2(t[6], t[7]));
  bf16x8 b1 = asfrag(pk2(t[8], t[9]), pk2(t[10], t[11]), pk2(t[12], t[13]), pk2(t[14], t[15]));
  f32x16 z = {};
  z = mfma32(a0, b0, z);
  z = mfma32(a1, b1, z);
  return z;
}
DI void phi_exp(const f32x16& pl, int hh, float rl, float (&ep)[8], float (&en)[8], float& zp, float& zn) {
  float mx = fmaxf(fmaxf(fmaxf(pl[8 * hh], pl[8 * hh + 1]), fmaxf(pl[8 * hh + 2], pl[8 * hh + 3])), fmaxf(fmaxf(pl[8 * hh + 4], pl[8 * hh + 5]), fmaxf(pl[8 * hh + 6], pl[8 * hh + 7])));
  float mn = fminf(fminf(fminf(pl[8 * hh], pl[8 * hh + 1]), fminf(pl[8 * hh + 2], pl[8 * hh + 3])), fminf(fminf(pl[8 * hh + 4], pl[8 * hh + 5]), fminf(pl[8 * hh + 6], pl[8 * hh + 7])));
  xmaxmin(mx, mn);
  const float a = -rl * mx, b = rl * mn;
  zp = 0.f; zn = 0.f;
#pragma unroll
  for (int e = 0; e < 8; ++e) {
    ep[e] = ex2(fmaf(pl[8 * hh + e], rl, a));
    en[e] = ex2(fmaf(pl[8 * hh + e], -rl, b));
    zp += ep[e]; zn += en[e];
  }
  xsum2(zp, zn);
}

DI void load_wphi(const Params& p, int head0, int lane, bf16x8& a0, bf16x8& a1) {
  const int row = lane & 31, h = lane >> 5;
  const u16* wp = p.wphi + (head0 + (row >> 4)) * 256 + (row & 15);
  u32 v[4];
#pragma unroll
  for (int e = 0; e < 4; ++e) {
    const int j0 = 2 * e, j1 = 2 * e + 1;
    const int d0 = 8 * (j0 >> 2) + 4 * h + (j0 & 3), d1 = 8 * (j1 >> 2) + 4 * h + (j1 & 3);
    v[e] = (u32)wp[d0 * 16] | ((u32)wp[d1 * 16] << 16);
  }
  const bool lo = row < 16;
  a0 = asfrag(lo ? v[0] : 0u, lo ? v[1] : 0u, lo ? v[2] : 0u, lo ? v[3] : 0u);
  a1 = asfrag(lo ? 0u : v[0], lo ? 0u : v[1], lo ? 0u : v[2], lo ? 0u : v[3]);
}

DI void row_stats(int rg, int lane, float& nmu, float& r) {
  const int s = 32 * rg + (lane & 31);
  const float mu = ldf(L_SUM + s * 4) * (1.0f / D_);
  const float var = fmaf(-mu, mu, ldf(L_SQ + s * 4) * (1.0f / D_));
  r = __builtin_amdgcn_rsqf(fmaxf(var, 0.f) + 1e-5f);
  nmu = -mu;
}

DI void store_h(rsrc_t rws, int i, int rg, int tile, int lane, const u32x4 (&o)[2]) {
#pragma unroll
  for (int u = 0; u < 2; ++u)
    __builtin_amdgcn_raw_buffer_store_b128(o[u], rws, lane * 16, WS_H + ((4 * i + rg) * 112 + 2 * tile + u) * 1024, 16);
}

DI void roleA_main(const Params& p, int w, int lane, int i, int j) {
  STAMP_DECL;
  const int rg = w, pr = j >> 1, isq = j & 1;
  const int s = lane & 31, h = lane >> 5;
  u64 ep = 0;
  if (w == 0) ep = __hip_atomic_load((GAS u64*)(p.ws + WS_E), RLX);
  const int t0 = (!isq && (pr & 1)) ? 5 : 4;
  const int tg[3] = {t0, t0 + 1, t0 + 2};
  P1<3> ad; ad.init(rg, lane, tg);
  f32x16 acc[3] = {};
  float stat = 0.f, cs[3] = {0.f, 0.f, 0.f};
  STAMP(0);
  p1_main<0>(ad, acc, stat, cs, rg);
  STAMP(1);
  if (w == 0 && lane == 0) *(LAS u64*)(lds + L_TAG) = ep + 1;
  p1_finish<0>(stat, cs, tg, rg, lane);
  STAMP(2);
  const u64 tag = *(LAS u64*)(lds + L_TAG);
  const rsrc_t rws = mkrs(p.ws, WS_END);
  cs_reduce(tg, w, lane);
  float nmu, r;
  row_stats(rg, lane, nmu, r);
  const float rl = r * LOG2E;
  const int Tb = 2 * h_unit0(pr, isq) + t0 - 4;
#pragma unroll
  for (int t = 0; t < 3; ++t) {
    float c16[16];
    u32x4 o[2];
    cs_get(c16, w, t, lane);
    silu_tile(acc[t], c16, nmu, -rl, o);
    store_h(rws, i, rg, Tb + t, lane, o);
  }
  STAMP(3);
  const int rb = rg >> 1, cb = rg & 1, i2 = 2 * i + (j & 1), j2 = j >> 1;
  const int orow = 64 * i2 + 32 * rb + s;
  const u32 ooff = orow * 1024 + (64 * j2 + 32 * cb) * 2 + h * 16;
  u32x4 xr[2];
  wait_vm0();
  if (arrive(C_H, lane) == (isq ? 7u : 3u)) set_flag(p.ws, WS_FH + (i * 16 + j) * 128, tag, lane);
#pragma unroll
  for (int u = 0; u < 2; ++u) xr[u] = *(const GAS u32x4*)((const char*)p.x + ooff + 32 * u);
  float nmu2, r2;
  row_stats(2 * (j & 1) + rb, lane, nmu2, r2);
  STAMP(6);
  const int blk = (lane >> 4) & 1, q = (lane & 15) >> 2, pp = lane & 3;
  const u32 wa = (4 * h + q) * 128 + ((cb ^ (q >> 1)) * 64) + 32 * blk + 8 * pp;
  const u32 ha = H2R + rb * 8192 + lane * 16;
  f32x16 ag = {}, as = {};
  u32 ws_ = 0, hs = 0;
  auto chunk = [&](f32x16& acc_) {
#pragma unroll
    for (int kk = 0; kk < 8; ++kk) {
      const bf16x8 hb = ld128(ha + hs + kk * 1024);
      const bf16x8 wf = ldtr(W2R + ws_ + wa + kk * 2048, W2R + ws_ + wa + kk * 2048 + 1024);
      acc_ = mfma32(wf, hb, acc_);
    }
    ws_ = ws_ == 5 * 16384 ? 0 : ws_ + 16384;
    hs = hs == 2 * 16384 ? 0 : hs + 16384;
  };
#pragma unroll 1
  for (int c = 0; c < 14; ++c) {
    barrier();
    if (c == 0) STAMP(7);
    chunk(ag);
    wait_lgkm0();
  }
  STAMP(8);
  float G[16];
#pragma unroll
  for (int e = 0; e < 16; ++e) {
    const float gp = ag[e] * r2;
    G[e] = gp * rcp(1.0f + ex2(-LOG2E * gp));
  }
#pragma unroll 1
  for (int c = 14; c < 18; ++c) {
    barrier();
    chunk(as);
    wait_lgkm0();
  }
  STAMP(9);
#pragma unroll
  for (int u = 0; u < 2; ++u) {
    u32x2 ra = __builtin_amdgcn_permlane32_swap(xr[u][0], xr[u][2], false, false);
    u32x2 rb_ = __builtin_amdgcn_permlane32_swap(xr[u][1], xr[u][3], false, false);
    u32 pk[2][2];
#pragma unroll
    for (int g = 0; g < 2; ++g) {
      const u32 x01 = ra[g], x23 = rb_[g];
      const int e = 8 * u + 4 * g;
      const float y0 = fmaf(as[e], G[e], u2f(x01 << 16)), y1 = fmaf(as[e + 1], G[e + 1], u2f(x01 & 0xffff0000u));
      const float y2 = fmaf(as[e + 2], G[e + 2], u2f(x23 << 16)), y3 = fmaf(as[e + 3], G[e + 3], u2f(x23 & 0xffff0000u));
      pk[g][0] = pk2(y0, y1); pk[g][1] = pk2(y2, y3);
    }
    u32x2 s0 = __builtin_amdgcn_permlane32_swap(pk[0][0], pk[1][0], false, false);
    u32x2 s1 = __builtin_amdgcn_permlane32_swap(pk[0][1], pk[1][1], false, false);
    u32x4 v = {s0[0], s1[0], s0[1], s1[1]};
    *(GAS u32x4*)((char*)p.out + ooff + 32 * u) = v;
  }
  STAMP(10);
  STAMP_DUMP;
}

DI void roleB_kv(const Params& p, int w, int lane, int i, int j) {
  STAMP_DECL;
  const int rg = w - 4, pr = j >> 1;
  const int s = lane & 31, h = lane >> 5;
  bf16x8 a0[2], a1[2];
  load_wphi(p, 4 * pr, lane, a0[0], a1[0]);
  load_wphi(p, 4 * pr + 2, lane, a0[1], a1[1]);
  const int tg[4] = {0, 1, 2, 3};
  P1<4> ad; ad.init(rg, lane, tg);
  f32x16 acc[4] = {};
  float stat = 0.f, cs[4] = {0.f, 0.f, 0.f, 0.f};
  STAMP(0);
  p1_main<1>(ad, acc, stat, cs, rg);
  STAMP(1);
  p1_finish<1>(stat, cs, tg, rg, lane);
  STAMP(2);
  const u64 tag = *(LAS u64*)(lds + L_TAG);
  const rsrc_t rws = mkrs(p.ws, WS_END);
  cs_reduce(tg, w, lane);
  float nmu, r;
  row_stats(rg, lane, nmu, r);
  const float rl = r * LOG2E;
  const u32 sw = ((s >> 1) & 7);
#pragma unroll
  for (int t = 0; t < 2; ++t) {
    float c16[16];
    cs_get(c16, w, t, lane);
    const f32x16 pl = phi_logits(acc[t], c16, nmu, a0[t], a1[t]);
#pragma unroll
    for (int hh = 0; hh < 2; ++hh) {
      float ep_[8], en_[8], zp, zn;
      phi_exp(pl, hh, rl, ep_, en_, zp, zn);
      const float ip = rcp(zp), in = rcp(zn);
      const u32 base = L_PHI + (2 * t + hh) * 8192 + (32 * rg + s) * 64;
#pragma unroll
      for (int g = 0; g < 2; ++g) {
        u32x2 vp = {pk2(ep_[4 * g] * ip, ep_[4 * g + 1] * ip), pk2(ep_[4 * g + 2] * ip, ep_[4 * g + 3] * ip)};
        u32x2 vn = {pk2(en_[4 * g] * in, en_[4 * g + 1] * in), pk2(en_[4 * g + 2] * in, en_[4 * g + 3] * in)};
        *(LAS u32x2*)(lds + base + (((2 * g + h) ^ sw) * 8)) = vp;
        *(LAS u32x2*)(lds + base + (((4 + 2 * g + h) ^ sw) * 8)) = vn;
      }
    }
  }
#pragma unroll
  for (int t = 0; t < 2; ++t) {
    float c16[16];
    cs_get(c16, w, 2 + t, lane);
    const u32 base = L_V + t * 8192 + (32 * rg + s) * 64;
#pragma unroll
    for (int g = 0; g < 4; ++g) {
      float v[4];
#pragma unroll
      for (int e = 0; e < 4; ++e) v[e] = fmaf(nmu, c16[4 * g + e], acc[2 + t][4 * g + e]) * r;
      u32x2 vv = {pk2(v[0], v[1]), pk2(v[2], v[3])};
      *(LAS u32x2*)(lds + base + (((2 * g + h) ^ sw) * 8)) = vv;
    }
  }
  arrive(C_PHI, lane);
  STAMP(3);
  lds_spin(C_PHI, 4);
  {
    const int blk = (lane >> 4) & 1, q = (lane & 15) >> 2, pp = lane & 3;
    f32x16 d = {};
#pragma unroll
    for (int u = 0; u < 8; ++u) {
      const int r0 = 16 * u + 8 * h + q, r1 = r0 + 4;
      const u32 pb = L_PHI + rg * 8192, vb = L_V + (rg >> 1) * 8192;
      const u32 pa0 = pb + r0 * 64 + (((4 * blk + pp) ^ ((r0 >> 1) & 7)) * 8);
      const u32 pa1 = pb + r1 * 64 + (((4 * blk + pp) ^ ((r1 >> 1) & 7)) * 8);
      const u32 pv0 = blk ? L_ONES : vb + r0 * 64 + (((4 * (rg & 1) + pp) ^ ((r0 >> 1) & 7)) * 8);
      const u32 pv1 = blk ? L_ONES : vb + r1 * 64 + (((4 * (rg & 1) + pp) ^ ((r1 >> 1) & 7)) * 8);
      d = mfma32(ldtr(pa0, pa1), ldtr(pv0, pv1), d);
    }
    if (s <= 16) {
#pragma unroll
      for (int u = 0; u < 2; ++u) {
        u32x4 v = {pk2(d[8 * u], d[8 * u + 1]), pk2(d[8 * u + 2], d[8 * u + 3]), pk2(d[8 * u + 4], d[8 * u + 5]), pk2(d[8 * u + 6], d[8 * u + 7])};
        __builtin_amdgcn_raw_buffer_store_b128(v, rws, (2 * s + h) * 16, WS_KV + (((pr * 16 + i) * 4 + rg) * 2 + u) * 544, 16);
      }
    }
  }
  STAMP(4);
  wait_vm0();
  if (arrive(C_KV, lane) == 3) set_flag(p.ws, WS_FK + (pr * 16 + i) * 128, tag, lane);
  STAMP(5);
  for (int c = 0; c < 12; ++c) barrier();
  if (rg == 0) {
    poll16(p.ws, WS_FO + i * 8 * 128, 128, tag, lane, 7);
  }
  STAMP(9);
  for (int c = 12; c < 18; ++c) barrier();
  if (blockIdx.x == 0 && rg == 0) {
    poll16(p.ws, WS_FO, 128, tag, lane, 63);
    poll16(p.ws, WS_FO + 64 * 128, 128, tag, lane, 63);
    if (lane == 0) __hip_atomic_store((GAS u64*)(p.ws + WS_E), tag, RLX);
  }
  STAMP(10);
  STAMP_DUMP;
}

DI void roleB_q(const Params& p, int w, int lane, int i, int j) {
  STAMP_DECL;
  const int rg = w - 4, pr = j >> 1;
  const int s = lane & 31, h = lane >> 5;
  bf16x8 a0[2], a1[2];
  load_wphi(p, 4 * pr, lane, a0[0], a1[0]);
  load_wphi(p, 4 * pr + 2, lane, a0[1], a1[1]);
  const int tg[3] = {7, 0, 1};
  P1<3> ad; ad.init(rg, lane, tg);
  f32x16 acc[3] = {};
  float stat = 0.f, cs[3] = {0.f, 0.f, 0.f};
  STAMP(0);
  p1_main<1>(ad, acc, stat, cs, rg);
  STAMP(1);
  p1_finish<1>(stat, cs, tg, rg, lane);
  STAMP(2);
  const u64 tag = *(LAS u64*)(lds + L_TAG);
  const rsrc_t rws = mkrs(p.ws, WS_END);
  cs_reduce(tg, w, lane);
  float nmu, r;
  row_stats(rg, lane, nmu, r);
  const float rl = r * LOG2E;
  {
    float c16[16];
    u32x4 o[2];
    cs_get(c16, w, 0, lane);
    silu_tile(acc[0], c16, nmu, -rl, o);
    store_h(rws, i, rg, 2 * h_unit0(pr, 1) + 3, lane, o);
  }
  STAMP(3);
  bf16x8 bq[4][2];
  float zeps[4];
#pragma unroll
  for (int t = 0; t < 2; ++t) {
    float c16[16];
    cs_get(c16, w, 1 + t, lane);
    const f32x16 pl = phi_logits(acc[1 + t], c16, nmu, a0[t], a1[t]);
#pragma unroll
    for (int hh = 0; hh < 2; ++hh) {
      float ep_[8], en_[8], zp, zn;
      phi_exp(pl, hh, rl, ep_, en_, zp, zn);
      const float rho = zp * rcp(zn);
      bq[2 * t + hh][0] = asfrag(pk2(ep_[0], ep_[1]), pk2(ep_[2], ep_[3]), pk2(ep_[4], ep_[5]), pk2(ep_[6], ep_[7]));
      bq[2 * t + hh][1] = asfrag(pk2(en_[0] * rho, en_[1] * rho), pk2(en_[2] * rho, en_[3] * rho), pk2(en_[4] * rho, en_[5] * rho), pk2(en_[6] * rho, en_[7] * rho));
      zeps[2 * t + hh] = zp * 1e-6f;
    }
    if (t == 0) {
      wait_vm0();
      if (arrive(C_H, lane) == 7) set_flag(p.ws, WS_FH + (i * 16 + j) * 128, tag, lane);
    }
  }
  STAMP(4);
  poll16(p.ws, WS_FK + pr * 16 * 128, 128, tag, lane, 15);
  STAMP(5);
  {
    u32 clo = 0x00003f80u, chi = 0x3f800000u;
    asm volatile("" : "+s"(clo), "+s"(chi));
    u32x4 pv[2][8];
    u32x2 pz[2];
#pragma unroll
    for (int u = 0; u < 2; ++u) {
      const u32 pbase = WS_KV + ((pr * 16 * 4 + rg) * 2 + u) * 544;
#pragma unroll
      for (int m = 0; m < 8; ++m)
        pv[u][m] = __builtin_amdgcn_raw_buffer_load_b128(rws, ((lane >> 5) * 8 * 544) + (lane & 31) * 16, pbase + 2 * m * 8 * 544, 16);
      pz[u] = __builtin_amdgcn_raw_buffer_load_b64(rws, (lane >> 2) * 8 * 544 + 512 + (lane & 3) * 8, pbase, 16);
    }
#pragma unroll
    for (int u = 0; u < 2; ++u) {
      float sm[8] = {0.f, 0.f, 0.f, 0.f, 0.f, 0.f, 0.f, 0.f};
#pragma unroll
      for (int m = 0; m < 8; ++m)
#pragma unroll
        for (int e = 0; e < 4; ++e) {
          sm[2 * e] = dot2(pv[u][m][e], clo, sm[2 * e]);
          sm[2 * e + 1] = dot2(pv[u][m][e], chi, sm[2 * e + 1]);
        }
#pragma unroll
      for (int e = 0; e < 4; ++e) xsum2(sm[2 * e], sm[2 * e + 1]);
      float zz[4] = {u2f(pz[u][0] << 16), u2f(pz[u][0] & 0xffff0000u), u2f(pz[u][1] << 16), u2f(pz[u][1] & 0xffff0000u)};
#pragma unroll
      for (int e = 0; e < 4; ++e) {
        float v = zz[e];
        v += u2f(__builtin_amdgcn_update_dpp(0, (int)f2u(v), 0x128, 0xf, 0xf, false));
        v += u2f(__builtin_amdgcn_update_dpp(0, (int)f2u(v), 0x124, 0xf, 0xf, false));
        u32x2 t = __builtin_amdgcn_permlane16_swap(f2u(v), f2u(v), false, false);
        v = u2f(t[0]) + u2f(t[1]);
        zz[e] = xsum(v);
      }
      const u32 kb = L_KVA + (rg * 2 + u) * 544;
      if (lane < 32) { u32x4 v = {pk2(sm[0], sm[1]), pk2(sm[2], sm[3]), pk2(sm[4], sm[5]), pk2(sm[6], sm[7])}; *(LAS u32x4*)(lds + kb + lane * 16) = v; }
      if (lane < 4) { u32x2 v = {pk2(zz[0], zz[1]), pk2(zz[2], zz[3])}; *(LAS u32x2*)(lds + kb + 512 + lane * 8) = v; }
    }
  }
  arrive(C_CH, lane);
  lds_spin(C_CH, 4);
  STAMP(6);
#pragma unroll
  for (int hh = 0; hh < 4; ++hh) {
    const u32 ao = (s < 16 ? s : 16) * 32 + h * 16;
    f32x16 d = {};
    d = mfma32(ld128(L_KVA + (hh * 2) * 544 + ao), bq[hh][0], d);
    d = mfma32(ld128(L_KVA + (hh * 2 + 1) * 544 + ao), bq[hh][1], d);
    const float inv = rcp(d[8] + zeps[hh]);
    u32x4 v = {pk2(d[0] * inv, d[1] * inv), pk2(d[2] * inv, d[3] * inv), pk2(d[4] * inv, d[5] * inv), pk2(d[6] * inv, d[7] * inv)};
    __builtin_amdgcn_raw_buffer_store_b128(v, rws, lane * 16, WS_O + ((4 * i + rg) * 32 + 4 * pr + hh) * 1024, 16);
  }
  STAMP(7);
  barrier();
  barrier();
  wait_vm0();
  if (arrive(C_O, lane) == 3) set_flag(p.ws, WS_FO + (i * 8 + pr) * 128, tag, lane);
  STAMP(8);
  for (int c = 2; c < 12; ++c) barrier();
  if (rg == 0) poll16(p.ws, WS_FO + i * 8 * 128, 128, tag, lane, 7);
  STAMP(9);
  for (int c = 12; c < 18; ++c) barrier();
  STAMP(10);
  STAMP_DUMP;
}

__global__ void __launch_bounds__(768, 3) fused_hedgehog_ffn(Params p) {
  const int tid = threadIdx.x, lane = tid & 63;
  const int w = __builtin_amdgcn_readfirstlane(tid >> 6);
  const int b = blockIdx.x, g = b & 7, m = b >> 3;
  const int i = 4 * (g >> 1) + (m & 3), j = 8 * (g & 1) + (m >> 2);
  if (w == 4) {
    if (lane < 8) *(LAS u32*)(lds + L_CNT + 4 * lane) = 0;
    if (lane < 4) *(LAS u32*)(lds + L_ONES + 4 * lane) = 0x3f803f80u;
    wait_lgkm0();
  }
  if (w >= 8) loader_main(p, w, lane, i, j);
  else if (w < 4) roleA_main(p, w, lane, i, j);
  else if (j & 1) roleB_q(p, w, lane, i, j);
  else roleB_kv(p, w, lane, i, j);
}

extern "C" void kernel_launch(void* const* d_in, const int* in_sizes, int n_in, void* d_out, int out_size, void* d_ws, size_t ws_size,
                              hipStream_t stream) {
  Params p;
  p.x = (const u16*)d_in[0]; p.wqkv = (const u16*)d_in[1]; p.wphi = (const u16*)d_in[2]; p.wo = (const u16*)d_in[3];
  p.w1 = (const u16*)d_in[5]; p.w2 = (const u16*)d_in[6]; p.out = (u16*)d_out; p.ws = (char*)d_ws;
  fused_hedgehog_ffn<<<256, 768, 0, stream>>>(p);
}
```

```cpp
#include <hip/hip_runtime.h>

typedef unsigned short u16;
typedef unsigned u32;
typedef unsigned long long u64;
typedef short bf16x8 __attribute__((ext_vector_type(8)));
typedef short s16x4 __attribute__((ext_vector_type(4)));
typedef __bf16 bf2 __attribute__((ext_vector_type(2)));
typedef float f32x16 __attribute__((ext_vector_type(16)));
typedef float f32x4 __attribute__((ext_vector_type(4)));
typedef u32 u32x4 __attribute__((ext_vector_type(4)));
typedef u32 u32x2 __attribute__((ext_vector_type(2)));
#define LAS __attribute__((address_space(3)))
#define GAS __attribute__((address_space(1)))
#define DI __device__ __forceinline__
#define RLX __ATOMIC_RELAXED, __HIP_MEMORY_SCOPE_AGENT
#define RLXW __ATOMIC_RELAXED, __HIP_MEMORY_SCOPE_WORKGROUP

#define PRIO 1
#define TIMING 0

constexpr int S_ = 2048, D_ = 512, F_ = 1792;
constexpr float LOG2E = 1.4426950408889634f;

constexpr u32 WS_E = 0;
constexpr u32 WS_FH = 4096;
constexpr u32 WS_FK = WS_FH + 32768;
constexpr u32 WS_FO = WS_FK + 32768;
constexpr u32 WS_ST = 102400;
constexpr u32 WS_KV = 262144;
constexpr u32 WS_H = 1u << 20;
constexpr u32 WS_O = WS_H + 64u * 112u * 1024u;
constexpr u32 WS_END = WS_O + 64u * 32u * 1024u;
constexpr u32 WS_TM = 16u << 20;

constexpr u32 W1R = 0, NW1 = 5;
constexpr u32 X1R = 81920, NX1 = 4;
constexpr u32 L_PHI = 98304, L_V = 131072;
constexpr u32 L_KVA = 147456;
constexpr u32 MISC = 157696;
constexpr u32 L_CS = MISC + 1024;
constexpr u32 L_CNT = MISC + 2048, L_TAG = MISC;
constexpr u32 L_PF = L_CNT + 64;
enum { C_H = 0, C_PHI = 1, C_KV = 2, C_CH = 3, C_O = 4, C_HR = 5, C_OR = 6, C_IMG = 7, C_RG = 8, C_RS = 9 };
constexpr u32 SPIN_CAP = 1u << 22;

__shared__ __attribute__((aligned(1024))) u32 lds32[40960];
#define lds ((char*)lds32)

DI f32x16 mfma32(bf16x8 a, bf16x8 b, f32x16 c) { return __builtin_amdgcn_mfma_f32_32x32x16_bf16(a, b, c, 0, 0, 0); }
DI u32 pk2(float a, float b) { bf2 v = {(__bf16)a, (__bf16)b}; return __builtin_bit_cast(u32, v); }
DI float dot2(u32 a, u32 b, float c) { return __builtin_amdgcn_fdot2_f32_bf16(__builtin_bit_cast(bf2, a), __builtin_bit_cast(bf2, b), c, false); }
DI float ex2(float x) { return __builtin_amdgcn_exp2f(x); }
DI float rcp(float x) { return __builtin_amdgcn_rcpf(x); }
DI float fmax2(float a, float b) { return __builtin_elementwise_maximum(a, b); }
DI float fmin2(float a, float b) { return __builtin_elementwise_minimum(a, b); }
DI u32 f2u(float x) { return __builtin_bit_cast(u32, x); }
DI float u2f(u32 x) { return __builtin_bit_cast(float, x); }
DI bf16x8 ld128(u32 off) { return *(LAS bf16x8*)(lds + off); }
DI f32x4 ldf4(u32 off) { return *(LAS f32x4*)(lds + off); }
DI float ldf(u32 off) { return *(LAS float*)(lds + off); }
DI void stf(u32 off, float v) { *(LAS float*)(lds + off) = v; }
DI bf16x8 ldtr(u32 off0, u32 off1) {
  s16x4 a = __builtin_amdgcn_ds_read_tr16_b64_v4i16((LAS s16x4*)(lds + off0));
  s16x4 b = __builtin_amdgcn_ds_read_tr16_b64_v4i16((LAS s16x4*)(lds + off1));
  return __builtin_shufflevector(a, b, 0, 1, 2, 3, 4, 5, 6, 7);
}
DI bf16x8 asfrag(u32 a, u32 b, u32 c, u32 d) { u32x4 v = {a, b, c, d}; return __builtin_bit_cast(bf16x8, v); }
DI void barrier() { asm volatile("s_barrier" ::: "memory"); }
DI void wait_lgkm0() { asm volatile("s_waitcnt lgkmcnt(0)" ::: "memory"); }
DI void wait_vm0() { asm volatile("s_waitcnt vmcnt(0)" ::: "memory"); }
template <int N> DI void wait_vm() { asm volatile("s_waitcnt vmcnt(%0)" ::"n"(N) : "memory"); }
DI float xsum(float x) { u32x2 r = __builtin_amdgcn_permlane32_swap(f2u(x), f2u(x), false, false); return u2f(r[0]) + u2f(r[1]); }
DI void xmaxmin(float& mx, float& mn) {
  u32x2 r = __builtin_amdgcn_permlane32_swap(f2u(mx), f2u(-mn), false, false);
  float m = fmax2(u2f(r[0]), u2f(r[1]));
  u32x2 q = __builtin_amdgcn_permlane32_swap(f2u(m), f2u(m), false, false);
  mx = u2f(q[0]); mn = -u2f(q[1]);
}
DI void xsum2(float& a, float& b) {
  u32x2 r = __builtin_amdgcn_permlane32_swap(f2u(a), f2u(b), false, false);
  float m = u2f(r[0]) + u2f(r[1]);
  u32x2 q = __builtin_amdgcn_permlane32_swap(f2u(m), f2u(m), false, false);
  a = u2f(q[0]); b = u2f(q[1]);
}
DI void lds_st(u32 off, u32 v) { __hip_atomic_store((LAS u32*)&lds32[off >> 2], v, RLXW); }
DI u32 ldsaddr(u32 off) { return (u32)(size_t)(LAS char*)lds + off; }
DI u32x4 asm_ld128(u32 off) {
  u32x4 v;
  asm volatile("ds_read_b128 %0, %1\n\ts_waitcnt lgkmcnt(0)" : "=&v"(v) : "v"(ldsaddr(off)) : "memory");
  return v;
}
DI void asm_st32(u32 off, u32 val) { asm volatile("ds_write_b32 %0, %1" ::"v"(ldsaddr(off)), "v"(val) : "memory"); }
DI u32 asm_ld32(u32 off) {
  u32 v;
  asm volatile("ds_read_b32 %0, %1\n\ts_waitcnt lgkmcnt(0)" : "=&v"(v) : "v"(ldsaddr(off)) : "memory");
  return v;
}
DI void lds_spin(u32 off, u32 target) {
#pragma unroll 1
  for (u32 it = 0; it < SPIN_CAP; ++it) {
    if (__builtin_amdgcn_readfirstlane(asm_ld32(off)) >= target) break;
    __builtin_amdgcn_s_sleep(1);
  }
}
DI u32 lds_add_rtn(u32 off) { return __hip_atomic_fetch_add((LAS u32*)&lds32[off >> 2], 1u, RLXW); }
DI u32 lds_ld(u32 off) { return __hip_atomic_load((LAS u32*)&lds32[off >> 2], RLXW); }
DI u32 arrive(int cnt, int lane) {
  asm volatile("" ::: "memory");
  u32 old = 0;
  if (lane == 0) old = lds_add_rtn(L_CNT + 4 * cnt);
  old = __builtin_amdgcn_readfirstlane(old);
  asm volatile("" ::: "memory");
  return old;
}
DI void poll16(const char* ws, u32 base, u32 stride, u64 tag, int lane, int mask) {
  GAS u64* p = (GAS u64*)(ws + base + (u32)(lane & mask) * stride);
#pragma unroll 1
  for (u32 it = 0; it < SPIN_CAP; ++it) {
    u64 v = __hip_atomic_load(p, RLX);
    if (__all(v == tag)) break;
    __builtin_amdgcn_s_sleep(1);
  }
  asm volatile("" ::: "memory");
}
DI void set_flag(char* ws, u32 off, u64 tag, int lane) {
  if (lane == 0) __hip_atomic_store((GAS u64*)(ws + off), tag, RLX);
}

#define STAMP(k) do { } while (0)
#define STAMP_DECL do { } while (0)
#define STAMP_DUMP do { } while (0)

struct Params { const u16* x; const u16* wqkv; const u16* wphi; const u16* wo; const u16* w1; const u16* w2; u16* out; char* ws; };

typedef __amdgpu_buffer_rsrc_t rsrc_t;
DI rsrc_t mkrs(const void* p, u32 bytes) { return __builtin_amdgcn_make_buffer_rsrc((void*)p, 0, bytes, 0x00020000); }
DI void st128_sc1(rsrc_t rs, u32x4 v, u32 voff, u32 soff) { __builtin_amdgcn_raw_buffer_store_b128(v, rs, voff + soff, 0, 16); }
template <int AUX> DI void dma16(rsrc_t rs, u32 ldsoff, u32 voff, u32 soff) {
  __builtin_amdgcn_raw_ptr_buffer_load_lds(rs, (LAS void*)(lds + ldsoff), 16, voff, soff, 0, AUX);
}

DI int h_unit0(int p, int isq) { return isq ? ((p & 1) ? (7 * p + 3) >> 1 : (7 * p) >> 1) : ((p & 1) ? (7 * p - 1) >> 1 : (7 * p + 4) >> 1); }

DI void loader_main(const Params& p, int w, int lane, int i, int j, int i2, int j2) {
  STAMP_DECL;
  const int wl = w - 8, pr = j >> 1, isq = j & 1;
  const rsrc_t rx = mkrs(p.x, S_ * D_ * 2), rq = mkrs(p.wqkv, D_ * 1536 * 2), r1 = mkrs(p.w1, D_ * F_ * 2);
  const u32 gsw = ((((lane & 7) >> 2) ^ ((lane >> 4) & 1)) * 64) + (lane & 3) * 16;
  const u32 xv = (lane >> 3) * 1024 + (((lane & 7) ^ ((4 * (wl & 1) + (lane >> 4)) & 7)) * 16);
  const u32 qv = (lane >> 3) * 3072 + gsw, hv1 = (lane >> 3) * 3584 + gsw;
  const u32 xs = (128 * i + 8 * wl) * 1024;
  const u32 s0 = (isq ? pr * 128 : 1024 + pr * 128) + 8 * wl * 3072, s1 = 2048 + pr * 128 + 8 * wl * 3072;
  const u32 h0 = h_unit0(pr, isq) * 128 + 8 * wl * 3584;
  auto issue_w = [&](int c, u32 slot) {
    const u32 d = slot + wl * 1024;
    dma16<0>(rq, d, qv, s0 + c * (32 * 3072));
    if (!isq) dma16<0>(rq, d + 4096, qv, s1 + c * (32 * 3072));
    dma16<0>(r1, d + 8192, hv1, h0 + c * (32 * 3584));
    dma16<0>(r1, d + 12288, hv1, h0 + 128 + c * (32 * 3584));
  };
  auto issue_x = [&](int P, int half) {
    const u32 d = X1R + (P & 3) * 16384 + (wl + 8 * half) * 1024;
    dma16<0>(rx, d, xv, xs + half * 65536 + P * 128);
    dma16<0>(rx, d + 4096, xv, xs + half * 65536 + 32768 + P * 128);
  };
  STAMP(0);
  issue_x(0, 0); issue_x(0, 1);
  issue_w(0, 0); issue_w(1, 16384);
  issue_x(1, 0); issue_x(1, 1);
  issue_w(2, 2 * 16384);
  STAMP(1);
  if (isq) wait_vm<7>(); else wait_vm<8>();
  barrier();
  issue_w(3, 3 * 16384);
  issue_x(2, 0); issue_x(2, 1);
  issue_w(4, 4 * 16384);
  if (isq) wait_vm<10>(); else wait_vm<12>();
  barrier();
  issue_w(5, 0);
  issue_x(3, 0); issue_x(3, 1);
  u32 wslot = 16384;
#pragma unroll 1
  for (int c = 2; c < 16; ++c) {
    if (isq) {
      if (c <= 11) wait_vm<10>(); else if (c == 12) wait_vm<6>(); else if (c == 13) wait_vm<3>(); else wait_vm<0>();
    } else {
      if (c <= 11) wait_vm<12>(); else if (c == 12) wait_vm<8>(); else if (c == 13) wait_vm<4>(); else wait_vm<0>();
    }
    barrier();
    if (c <= 11) issue_w(c + 4, wslot);
    if (c <= 9) issue_x((c + 6) >> 1, c & 1);
    wslot = wslot == 4 * 16384 ? 0 : wslot + 16384;
  }
  STAMP(2);
  barrier();
  STAMP(3);
  const rsrc_t rw2 = mkrs(p.w2, F_ * D_ * 2);
  const u32 wv = (lane >> 3) * 1024 + gsw, wsrc = 16 * 28 * wl * 1024 + j2 * 128;
  auto issue_w2 = [&](int n) {
    dma16<0>(rw2, n * 8192 + wl * 2048, wv, wsrc + n * 16384);
    dma16<0>(rw2, n * 8192 + wl * 2048 + 1024, wv, wsrc + n * 16384 + 8192);
  };
#pragma unroll 1
  for (int n = 0; n < 12; ++n) issue_w2(n);
  wait_vm0();
  asm_st32(L_PF + 4 * wl, 12);
  if (!isq) lds_spin(L_CNT + 4 * C_IMG, 4);
#pragma unroll 1
  for (int n = 12; n < 18; ++n) issue_w2(n);
  wait_vm0();
  asm_st32(L_PF + 4 * wl, 18);
  STAMP(4);
  if (wl == 0) {
    const u32x4 tg = asm_ld128(L_TAG);
    const u64 tag = (u64)tg[0] | ((u64)tg[1] << 32);
    poll16(p.ws, WS_FH + (i2 >> 1) * 16 * 128, 128, tag, lane, 15);
    asm_st32(L_CNT + 4 * C_HR, 1);
    STAMP(5);
    poll16(p.ws, WS_FO + (i2 >> 1) * 8 * 128, 128, tag, lane, 7);
    asm_st32(L_CNT + 4 * C_OR, 1);
    STAMP(6);
    if (blockIdx.x == 0) {
      poll16(p.ws, WS_FO, 128, tag, lane, 63);
      poll16(p.ws, WS_FO + 64 * 128, 128, tag, lane, 63);
      if (lane == 0) __hip_atomic_store((GAS u64*)(p.ws + WS_E), tag, RLX);
    }
  }
  STAMP_DUMP;
}

template <int NT> struct Frag { bf16x8 x; bf16x8 w[NT]; };

template <int NT> struct P1 {
  u32 xa[4];
  u32 wa[NT];
  DI void init(int rg, int lane, const int (&tg)[NT]) {
    const int s = lane & 31, h = lane >> 5, blk = (lane >> 4) & 1, q = (lane & 15) >> 2, pp = lane & 3, hi = rg >> 1;
#pragma unroll
    for (int k = 0; k < 4; ++k) xa[k] = X1R + (((32 * rg + s) * 128 + ((h ^ ((s >> 1) & 7)) * 16)) ^ ((k ^ hi) << 5));
#pragma unroll
    for (int t = 0; t < NT; ++t)
      wa[t] = W1R + (tg[t] >> 1) * 4096 + hi * 2048 + (8 * h + q) * 128 + (((tg[t] & 1) ^ (q >> 1)) * 64) + 32 * blk + 8 * pp;
  }
};
#define SB() __builtin_amdgcn_sched_barrier(0)
template <int NT> struct Addr { u32 w[NT]; };
template <int NT> DI Addr<NT> waddr(const P1<NT>& a, u32 slot) {
  Addr<NT> r;
#pragma unroll
  for (int t = 0; t < NT; ++t) r.w[t] = a.wa[t] + slot;
  return r;
}

template <int NT, bool CS, bool RELOAD>
DI void p1_step(Frag<NT>& f, f32x16 (&acc)[NT], bf16x8 sel, f32x4& cs0, f32x4& cs2, const Addr<NT>& wa, u32 xa, int ks) {
#pragma unroll
  for (int t = 0; t < NT; ++t) {
    acc[t] = mfma32(f.w[t], f.x, acc[t]);
    if constexpr (CS) {
      if (t == 0) cs0 = __builtin_amdgcn_mfma_f32_16x16x32_bf16(sel, f.w[0], cs0, 0, 0, 0);
      if (t == 2) cs2 = __builtin_amdgcn_mfma_f32_16x16x32_bf16(sel, f.w[2], cs2, 0, 0, 0);
    }
    SB();
    if constexpr (RELOAD) {
      const u32 ad = wa.w[t] ^ (ks << 11);
      f.w[t] = ldtr(ad, ad + 512);
      if (t == NT - 1) f.x = ld128(xa);
    }
    SB();
  }
}
template <int NT, bool LAST>
DI void p1_pair(const P1<NT>& a, Frag<NT>& f0, Frag<NT>& f1, f32x16 (&acc)[NT], bf16x8 sel, f32x4& cs0, f32x4& cs2, u32 w1, u32 w2, u32 xsl, u32 xn) {
  {
    const Addr<NT> wa = waddr(a, w1);
    p1_step<NT, true, true>(f0, acc, sel, cs0, cs2, wa, a.xa[2] + xsl, 0);
    p1_step<NT, false, true>(f1, acc, sel, cs0, cs2, wa, a.xa[3] + xsl, 1);
  }
  barrier();
  {
    const Addr<NT> wa = waddr(a, w2);
    p1_step<NT, true, !LAST>(f0, acc, sel, cs0, cs2, wa, a.xa[0] + xn, 0);
    p1_step<NT, false, !LAST>(f1, acc, sel, cs0, cs2, wa, a.xa[1] + xn, 1);
  }
  if constexpr (!LAST) barrier();
}
struct Stat { u32x4 g; };
DI u32x4 stat_load(rsrc_t rws, int row0, int lane) { return __builtin_amdgcn_raw_buffer_load_b128(rws, (lane & 31) * 16, WS_ST + row0 * 16, 16); }
DI void stat_get(rsrc_t rws, int row0, int lane, u64 tag, u32x4 g, float& nmu, float& r) {
#pragma unroll 1
  for (u32 it = 0; it < SPIN_CAP; ++it) {
    if (__all(g[1] == (u32)tag && g[3] == (u32)(tag >> 32))) break;
    __builtin_amdgcn_s_sleep(1);
    g = stat_load(rws, row0, lane);
  }
  nmu = -u2f(g[0]); r = u2f(g[2]);
}
DI void stat_publish(const Params& p, rsrc_t rws, int row, int lane, u64 tag, u32x4 xv) {
  float s0 = 0.f, s1 = 0.f, q0 = 0.f, q1 = 0.f;
#pragma unroll
  for (int e = 0; e < 4; ++e) {
    const float lo = u2f(xv[e] << 16), hi = u2f(xv[e] & 0xffff0000u);
    s0 += lo; s1 += hi; q0 = fmaf(lo, lo, q0); q1 = fmaf(hi, hi, q1);
  }
  float s = s0 + s1, q = q0 + q1;
#define DPP_ADD(v, ctrl) v += u2f(__builtin_amdgcn_update_dpp(0, (int)f2u(v), ctrl, 0xf, 0xf, false))
  DPP_ADD(s, 0x128); DPP_ADD(q, 0x128); DPP_ADD(s, 0x124); DPP_ADD(q, 0x124);
  DPP_ADD(s, 0x122); DPP_ADD(q, 0x122); DPP_ADD(s, 0x121); DPP_ADD(q, 0x121);
#undef DPP_ADD
  { u32x2 a = __builtin_amdgcn_permlane16_swap(f2u(s), f2u(q), false, false);
    float m = u2f(a[0]) + u2f(a[1]);
    u32x2 b = __builtin_amdgcn_permlane16_swap(f2u(m), f2u(m), false, false);
    s = u2f(b[0]); q = u2f(b[1]); }
  xsum2(s, q);
  const float mu = s * (1.0f / D_);
  const float var = fmaf(-mu, mu, q * (1.0f / D_));
  const float r = __builtin_amdgcn_rsqf(fmax2(var, 0.f) + 1e-5f);
  if (lane == 0) {
    const u32x4 g = {f2u(mu), (u32)tag, f2u(r), (u32)(tag >> 32)};
    st128_sc1(rws, g, 0, WS_ST + row * 16);
  }
}
template <int NT> DI void p1_main(const P1<NT>& a, f32x16 (&acc)[NT], f32x4& cs0, f32x4& cs2, int lane, rsrc_t rws, int row0, u32x4& sg) {
  Frag<NT> f0, f1;
  const u32 one = ((lane & 15) == ((lane >> 4) & 1)) ? 0x3f803f80u : 0u;
  const bf16x8 sel = asfrag(one, one, one, one);
#pragma unroll
  for (int t = 0; t < NT; ++t) asm volatile("" : "+v"(acc[t]));
  barrier();
#pragma unroll
  for (int t = 0; t < NT; ++t) f0.w[t] = ldtr(a.wa[t], a.wa[t] + 512);
  f0.x = ld128(a.xa[0]);
  SB();
#pragma unroll
  for (int t = 0; t < NT; ++t) f1.w[t] = ldtr(a.wa[t] ^ 2048, (a.wa[t] ^ 2048) + 512);
  f1.x = ld128(a.xa[1]);
  SB();
  u32 wsl = 0, xsl = 0;
#pragma unroll 1
  for (int cp = 0; cp < 7; ++cp) {
    const u32 w1 = wsl == 4 * 16384 ? 0 : wsl + 16384;
    const u32 w2 = w1 == 4 * 16384 ? 0 : w1 + 16384;
    const u32 xn = (xsl + 16384) & 65535;
    p1_pair<NT, false>(a, f0, f1, acc, sel, cs0, cs2, w1, w2, xsl, xn);
    wsl = w2; xsl = xn;
  }
  sg = stat_load(rws, row0, lane);
  p1_pair<NT, true>(a, f0, f1, acc, sel, cs0, cs2, (15 % NW1) * 16384, 0, xsl, 0);
}

DI void p1_finish(f32x4 cs0, f32x4 cs2, int g0, int g2, bool add2, int lane) {
  if (lane < 16) {
    __builtin_amdgcn_ds_faddf((LAS float*)&lds32[(L_CS + g0 * 128 + lane * 4) >> 2], cs0[0], 0, 0, false);
    __builtin_amdgcn_ds_faddf((LAS float*)&lds32[(L_CS + g0 * 128 + 64 + lane * 4) >> 2], cs0[1], 0, 0, false);
    if (add2) {
      __builtin_amdgcn_ds_faddf((LAS float*)&lds32[(L_CS + g2 * 128 + lane * 4) >> 2], cs2[0], 0, 0, false);
      __builtin_amdgcn_ds_faddf((LAS float*)&lds32[(L_CS + g2 * 128 + 64 + lane * 4) >> 2], cs2[1], 0, 0, false);
    }
  }
  wait_lgkm0();
  barrier();
}
DI void cs_get(float (&c)[16], int gt, int lane) {
  const int h = lane >> 5;
#pragma unroll
  for (int g = 0; g < 4; ++g) {
    f32x4 v = ldf4(L_CS + gt * 128 + (8 * g + 4 * h) * 4);
    c[4 * g] = v[0]; c[4 * g + 1] = v[1]; c[4 * g + 2] = v[2]; c[4 * g + 3] = v[3];
  }
}

DI void silu_tile(const f32x16& acc, const float (&cs)[16], float nmu, float nrl, u32x4 (&o)[2]) {
  float y[16];
#pragma unroll
  for (int e = 0; e < 16; ++e) {
    const float t = fmaf(nmu, cs[e], acc[e]);
    y[e] = t * rcp(1.0f + ex2(t * nrl));
  }
#pragma unroll
  for (int u = 0; u < 2; ++u)
#pragma unroll
    for (int e = 0; e < 4; ++e) o[u][e] = pk2(y[8 * u + 2 * e], y[8 * u + 2 * e + 1]);
}

DI f32x16 phi_logits(const f32x16& acc, const float (&cs)[16], float nmu, bf16x8 a0, bf16x8 a1) {
  float t[16];
#pragma unroll
  for (int e = 0; e < 16; ++e) t[e] = fmaf(nmu, cs[e], acc[e]);
  bf16x8 b0 = asfrag(pk2(t[0], t[1]), pk2(t[2], t[3]), pk2(t[4], t[5]), pk2(t[6], t[7]));
  bf16x8 b1 = asfrag(pk2(t[8], t[9]), pk2(t[10], t[11]), pk2(t[12], t[13]), pk2(t[14], t[15]));
  f32x16 z = {};
  z = mfma32(a0, b0, z);
  z = mfma32(a1, b1, z);
  return z;
}
DI void phi_exp(const f32x16& pl, int hh, float rl, float (&ep)[8], float (&en)[8], float& zp, float& zn) {
  const int o = 8 * hh;
  float mx = fmax2(fmax2(fmax2(fmax2(fmax2(pl[o], pl[o + 1]), pl[o + 2]), fmax2(fmax2(pl[o + 3], pl[o + 4]), pl[o + 5])), pl[o + 6]), pl[o + 7]);
  float mn = fmin2(fmin2(fmin2(fmin2(fmin2(pl[o], pl[o + 1]), pl[o + 2]), fmin2(fmin2(pl[o + 3], pl[o + 4]), pl[o + 5])), pl[o + 6]), pl[o + 7]);
  xmaxmin(mx, mn);
  const float a = -rl * mx, b = rl * mn;
  zp = 0.f; zn = 0.f;
#pragma unroll
  for (int e = 0; e < 8; ++e) {
    ep[e] = ex2(fmaf(pl[8 * hh + e], rl, a));
    en[e] = ex2(fmaf(pl[8 * hh + e], -rl, b));
    zp += ep[e]; zn += en[e];
  }
  xsum2(zp, zn);
}

DI void load_wphi(const Params& p, int head0, int lane, bf16x8& a0, bf16x8& a1) {
  const int row = lane & 31, h = lane >> 5;
  const u16* wp = p.wphi + (head0 + (row >> 4)) * 256 + (row & 15);
  u32 v[4];
#pragma unroll
  for (int e = 0; e < 4; ++e) {
    const int j0 = 2 * e, j1 = 2 * e + 1;
    const int d0 = 8 * (j0 >> 2) + 4 * h + (j0 & 3), d1 = 8 * (j1 >> 2) + 4 * h + (j1 & 3);
    v[e] = (u32)wp[d0 * 16] | ((u32)wp[d1 * 16] << 16);
  }
  const bool lo = row < 16;
  a0 = asfrag(lo ? v[0] : 0u, lo ? v[1] : 0u, lo ? v[2] : 0u, lo ? v[3] : 0u);
  a1 = asfrag(lo ? 0u : v[0], lo ? 0u : v[1], lo ? 0u : v[2], lo ? 0u : v[3]);
}

DI void store_h(rsrc_t rws, int i, int rg, int tile, int lane, const u32x4 (&o)[2]) {
#pragma unroll
  for (int u = 0; u < 2; ++u)
    st128_sc1(rws, o[u], lane * 16, WS_H + ((4 * i + rg) * 112 + 2 * tile + u) * 1024);
}

#define HWIN 7
constexpr int HW = HWIN;
DI u32x4 srd(const void* ptr, u32 bytes) {
  const u64 a = (u64)ptr;
  u32x4 d = {(u32)a, (u32)(a >> 32) & 0xffffu, bytes, 0x00020000u};
  d[0] = __builtin_amdgcn_readfirstlane(d[0]); d[1] = __builtin_amdgcn_readfirstlane(d[1]);
  return d;
}
DI bf16x8 asm_gload(u32x4 rs, u32 voff, u32 soff) {
  bf16x8 v;
  asm volatile("buffer_load_dwordx4 %0, %1, %2, %3 offen sc1" : "=v"(v) : "v"(voff), "s"(rs), "s"(soff) : "memory");
  return v;
}
DI void asm_dma(u32x4 rs, u32 ldsa, u32 voff, u32 soff) {
  asm volatile("s_mov_b32 m0, %0\n\ts_nop 0\n\tbuffer_load_dwordx4 %1, %2, %3 offen lds" ::"s"(ldsa), "v"(voff), "s"(rs), "s"(soff) : "memory");
}
struct P2 { u32 wa[2], hv, wv, hs[2], w2s, wos, ld; u32x4 rws, rw2, rwo; };
constexpr int p2_ops(int m) { return (m + HW < 28 ? 2 : 0) + (m < 18 ? 2 : 0); }
constexpr int p2_younger(int n) {
  int c = 0;
  if (n < HW) { c = 2 * (HW - 1 - n); for (int m = 0; m < n; ++m) c += p2_ops(m); }
  else { c = (n - HW < 18) ? 2 : 0; for (int m = n - HW + 1; m < n; ++m) c += p2_ops(m); }
  return c;
}
DI u32 opq(u32 x) { asm volatile("" : "+v"(x)); return x; }
template <int N> DI void p2a_step(const P2& c, f32x16 (&ac)[4], bf16x8 (&hw)[HW][2], bf16x8 (&wf)[2][2], int q, u32 b0, u32 b1) {
  asm volatile("s_waitcnt vmcnt(%2)" : "+v"(hw[N % HW][0]), "+v"(hw[N % HW][1]) : "n"(p2_younger(N)) : "memory");
  if constexpr (N == 11) lds_spin(L_PF + 4 * q, 18);
  if constexpr (N + 1 < 28) {
    constexpr u32 sl = (N + 1) % 18, o = (sl & 7) * 8192;
    if constexpr ((sl & 7) == 0 || sl == 0) { b0 = opq(c.wa[0] + (sl >> 3) * 65536); b1 = opq(c.wa[1] + (sl >> 3) * 65536); }
    wf[(N + 1) & 1][0] = ldtr(b0 + o, b0 + o + 1024);
    wf[(N + 1) & 1][1] = ldtr(b1 + o, b1 + o + 1024);
  }
  SB();
  ac[0] = mfma32(wf[N & 1][0], hw[N % HW][0], ac[0]);
  ac[1] = mfma32(wf[N & 1][1], hw[N % HW][0], ac[1]);
  ac[2] = mfma32(wf[N & 1][0], hw[N % HW][1], ac[2]);
  ac[3] = mfma32(wf[N & 1][1], hw[N % HW][1], ac[3]);
  SB();
  if constexpr (N + HW < 28) {
    hw[N % HW][0] = asm_gload(c.rws, c.hv, c.hs[0] + (N + HW) * 1024);
    hw[N % HW][1] = asm_gload(c.rws, c.hv, c.hs[1] + (N + HW) * 1024);
  }
  if constexpr (N < 10) {
    asm_dma(c.rw2, c.ld + N * 8192, c.wv, c.w2s + (N + 18) * 16384);
    asm_dma(c.rw2, c.ld + N * 8192 + 1024, c.wv, c.w2s + (N + 18) * 16384 + 8192);
  } else if constexpr (N < 18) {
    asm_dma(c.rwo, c.ld + N * 8192, c.wv, c.wos + (N - 10) * 16384);
    asm_dma(c.rwo, c.ld + N * 8192 + 1024, c.wv, c.wos + (N - 10) * 16384 + 8192);
  }
  SB();
  if constexpr (N + 1 < 28) p2a_step<N + 1>(c, ac, hw, wf, q, b0, b1);
}
template <int N> DI void p2b_step(f32x16 (&ac)[4], bf16x8 (&ow)[8][2], u32 b0, u32 b1) {
  constexpr u32 o = N * 8192;
  asm volatile("s_waitcnt vmcnt(%2)" : "+v"(ow[N][0]), "+v"(ow[N][1]) : "n"(2 * (7 - N)) : "memory");
  const bf16x8 w0 = ldtr(b0 + o, b0 + o + 1024), w1 = ldtr(b1 + o, b1 + o + 1024);
  SB();
  ac[0] = mfma32(w0, ow[N][0], ac[0]);
  ac[1] = mfma32(w1, ow[N][0], ac[1]);
  ac[2] = mfma32(w0, ow[N][1], ac[2]);
  ac[3] = mfma32(w1, ow[N][1], ac[3]);
  SB();
  if constexpr (N < 7) p2b_step<N + 1>(ac, ow, b0, b1);
}
DI void red_send(const f32x16 (&ac)[4], int q, int lane, int slot0) {
  const u32 b = opq(slot0 * 8192 + q * 2048 + lane * 16);
#pragma unroll
  for (int l = 1; l < 4; ++l)
#pragma unroll
    for (int g = 0; g < 4; ++g) {
      const f32x4 v = {ac[l][4 * g], ac[l][4 * g + 1], ac[l][4 * g + 2], ac[l][4 * g + 3]};
      *(LAS f32x4*)(lds + b + (2 * (l - 1) + (g >> 1)) * 8192 + (g & 1) * 1024) = v;
    }
}
DI void red_recv(f32x16& a, int q, int lane, int slot0) {
#pragma unroll
  for (int l = 1; l < 4; ++l) {
    const u32 b = opq(slot0 * 8192 + (q ^ l) * 2048 + lane * 16);
#pragma unroll
    for (int g = 0; g < 4; ++g) {
      const f32x4 v = ldf4(b + (2 * (l - 1) + (g >> 1)) * 8192 + (g & 1) * 1024);
#pragma unroll
      for (int e = 0; e < 4; ++e) a[4 * g + e] += v[e];
    }
  }
}

DI u64 compute_prologue(const Params& p, rsrc_t rws, int w, int lane, int i, int j) {
  const int row = 128 * i + 8 * j + w;
  const u32x4 xv = *(const GAS u32x4*)((const char*)p.x + row * 1024 + lane * 16);
  const u64 tag = __hip_atomic_load((GAS u64*)(p.ws + WS_E), RLX) + 1;
  stat_publish(p, rws, row, lane, tag, xv);
  return tag;
}

DI void roleA_main(const Params& p, int w, int lane, int i, int j, int i2, int j2) {
  STAMP_DECL;
  const int rg = w, pr = j >> 1, isq = j & 1, odd = rg & 1;
  const int s = lane & 31, h = lane >> 5;
  const rsrc_t rws = mkrs(p.ws, WS_END);
  const u64 tag = compute_prologue(p, rws, w, lane, i, j);
  if (w == 0 && lane == 0) *(LAS u64*)(lds + L_TAG) = tag;
  const int t0 = (!isq && (pr & 1)) ? 5 : 4;
  const int tg[3] = {t0 + odd, t0 + 1 - odd, t0 + 2};
  P1<3> ad; ad.init(rg, lane, tg);
  f32x16 acc[3] = {};
  f32x4 cs0 = {}, cs2 = {};
  u32x4 sg;
  STAMP(0);
  p1_main(ad, acc, cs0, cs2, lane, rws, 128 * i + 32 * rg, sg);
  STAMP(1);
  p1_finish(cs0, cs2, tg[0], tg[2], !odd, lane);
  STAMP(2);
  if (PRIO == 0 || (PRIO == 1 && isq)) __builtin_amdgcn_s_setprio(1);
  float nmu, r;
  stat_get(rws, 128 * i + 32 * rg, lane, tag, sg, nmu, r);
  const float rl = r * LOG2E;
  const int Tb = 2 * h_unit0(pr, isq) - 4;
#pragma unroll
  for (int t = 0; t < 3; ++t) {
    float c16[16];
    u32x4 o[2];
    cs_get(c16, tg[t], lane);
    silu_tile(acc[t], c16, nmu, -rl, o);
    store_h(rws, i, rg, Tb + tg[t], lane, o);
  }
  STAMP(3);
  wait_vm0();
  if (arrive(C_H, lane) == (isq ? 7u : 3u)) set_flag(p.ws, WS_FH + (i * 16 + j) * 128, tag, lane);
  __builtin_amdgcn_s_setprio(0);
  const int q = rg, rb = q >> 1, cb = q & 1;
  const int orow = 64 * i2 + 32 * rb + s;
  const u32 ooff = orow * 1024 + (64 * j2 + 32 * cb) * 2 + h * 16;
  float nmu2, r2;
  stat_get(rws, 64 * i2 + 32 * rb, lane, tag, stat_load(rws, 64 * i2 + 32 * rb, lane), nmu2, r2);
  P2 c2;
  {
    const int blk = (lane >> 4) & 1, qq = (lane & 15) >> 2, pp = lane & 3;
    c2.wa[0] = q * 2048 + (4 * h + qq) * 128 + ((cb ^ (qq >> 1)) * 64) + 32 * blk + 8 * pp;
    c2.wa[1] = c2.wa[0] ^ 64;
    c2.hv = lane * 16;
    c2.wv = (lane >> 3) * 1024 + ((((lane & 7) >> 2) ^ ((lane >> 4) & 1)) * 64) + (lane & 3) * 16;
    c2.rws = srd(p.ws, WS_END); c2.rw2 = srd(p.w2, F_ * D_ * 2); c2.rwo = srd(p.wo, D_ * D_ * 2);
    c2.hs[0] = WS_H + ((2 * i2 + rb) * 112 + 28 * q) * 1024; c2.hs[1] = WS_H + ((2 * i2 + (rb ^ 1)) * 112 + 28 * q) * 1024;
    c2.w2s = 16 * 28 * q * 1024 + j2 * 128;
    c2.wos = 128 * q * 1024 + j2 * 128;
    c2.ld = ldsaddr(q * 2048);
  }
  f32x16 ac[4] = {};
  bf16x8 hw[HW][2], wf[2][2];
  STAMP(6);
  lds_spin(L_PF + 4 * q, 12);
  lds_spin(L_CNT + 4 * C_HR, 1);
  STAMP(7);
#pragma unroll
  for (int n = 0; n < HW; ++n) { hw[n][0] = asm_gload(c2.rws, c2.hv, c2.hs[0] + n * 1024); hw[n][1] = asm_gload(c2.rws, c2.hv, c2.hs[1] + n * 1024); }
  wf[0][0] = ldtr(c2.wa[0], c2.wa[0] + 1024); wf[0][1] = ldtr(c2.wa[1], c2.wa[1] + 1024);
  p2a_step<0>(c2, ac, hw, wf, q, c2.wa[0], c2.wa[1]);
  STAMP(8);
  red_send(ac, q, lane, 0);
  arrive(C_RG, lane);
  u32x4 xr[2];
#pragma unroll
  for (int u = 0; u < 2; ++u) xr[u] = *(const GAS u32x4*)((const char*)p.x + ooff + 32 * u);
  lds_spin(L_CNT + 4 * C_RG, 4);
  red_recv(ac[0], q, lane, 0);
#pragma unroll
  for (int g = 0; g < 4; ++g) {
    f32x4 v;
#pragma unroll
    for (int e = 0; e < 4; ++e) { const float gp = ac[0][4 * g + e] * r2; v[e] = gp * rcp(1.0f + ex2(-LOG2E * gp)); }
    *(LAS f32x4*)(lds + (6 + (g >> 1)) * 8192 + q * 2048 + (g & 1) * 1024 + lane * 16) = v;
  }
#pragma unroll
  for (int u = 0; u < 2; ++u) *(LAS u32x4*)(lds + 8 * 8192 + q * 2048 + u * 1024 + lane * 16) = xr[u];
  const u32 os0 = WS_O + ((2 * i2 + rb) * 32 + 8 * q) * 1024, os1 = WS_O + ((2 * i2 + (rb ^ 1)) * 32 + 8 * q) * 1024;
  lds_spin(L_CNT + 4 * C_OR, 1);
  STAMP(9);
  bf16x8 ow[8][2];
#pragma unroll
  for (int n = 0; n < 8; ++n) { ow[n][0] = asm_gload(c2.rws, c2.hv, os0 + n * 1024); ow[n][1] = asm_gload(c2.rws, c2.hv, os1 + n * 1024); }
#pragma unroll
  for (int l = 0; l < 4; ++l) ac[l] = f32x16{};
  p2b_step<0>(ac, ow, opq(c2.wa[0] + 10 * 8192), opq(c2.wa[1] + 10 * 8192));
  red_send(ac, q, lane, 10);
  arrive(C_RS, lane);
  lds_spin(L_CNT + 4 * C_RS, 4);
  red_recv(ac[0], q, lane, 10);
  const f32x16 as = ac[0];
  float G[16];
#pragma unroll
  for (int g = 0; g < 4; ++g) {
    const f32x4 v = ldf4((6 + (g >> 1)) * 8192 + q * 2048 + (g & 1) * 1024 + lane * 16);
    G[4 * g] = v[0]; G[4 * g + 1] = v[1]; G[4 * g + 2] = v[2]; G[4 * g + 3] = v[3];
  }
#pragma unroll
  for (int u = 0; u < 2; ++u) xr[u] = *(LAS u32x4*)(lds + 8 * 8192 + q * 2048 + u * 1024 + lane * 16);
  STAMP(10);
#pragma unroll
  for (int u = 0; u < 2; ++u) {
    u32x2 ra = __builtin_amdgcn_permlane32_swap(xr[u][0], xr[u][2], false, false);
    u32x2 rb_ = __builtin_amdgcn_permlane32_swap(xr[u][1], xr[u][3], false, false);
    u32 pk[2][2];
#pragma unroll
    for (int g = 0; g < 2; ++g) {
      const u32 x01 = ra[g], x23 = rb_[g];
      const int e = 8 * u + 4 * g;
      const float y0 = fmaf(as[e], G[e], u2f(x01 << 16)), y1 = fmaf(as[e + 1], G[e + 1], u2f(x01 & 0xffff0000u));
      const float y2 = fmaf(as[e + 2], G[e + 2], u2f(x23 << 16)), y3 = fmaf(as[e + 3], G[e + 3], u2f(x23 & 0xffff0000u));
      pk[g][0] = pk2(y0, y1); pk[g][1] = pk2(y2, y3);
    }
    u32x2 s0 = __builtin_amdgcn_permlane32_swap(pk[0][0], pk[1][0], false, false);
    u32x2 s1 = __builtin_amdgcn_permlane32_swap(pk[0][1], pk[1][1], false, false);
    u32x4 v = {s0[0], s1[0], s0[1], s1[1]};
    *(GAS u32x4*)((char*)p.out + ooff + 32 * u) = v;
  }
  STAMP(11);
  STAMP_DUMP;
}

DI void roleB_kv(const Params& p, int w, int lane, int i, int j) {
  STAMP_DECL;
  const int rg = w - 4, pr = j >> 1, odd = rg & 1;
  const int s = lane & 31, h = lane >> 5;
  bf16x8 a0[2], a1[2];
  load_wphi(p, 4 * pr + 2 * odd, lane, a0[0], a1[0]);
  load_wphi(p, 4 * pr + 2 - 2 * odd, lane, a0[1], a1[1]);
  const int tg[4] = {odd, 1 - odd, 2 + odd, 3 - odd};
  const rsrc_t rws = mkrs(p.ws, WS_END);
  const u64 tag = compute_prologue(p, rws, w, lane, i, j);
  P1<4> ad; ad.init(rg, lane, tg);
  f32x16 acc[4] = {};
  f32x4 cs0 = {}, cs2 = {};
  u32x4 sg;
  STAMP(0);
  p1_main(ad, acc, cs0, cs2, lane, rws, 128 * i + 32 * rg, sg);
  STAMP(1);
  p1_finish(cs0, cs2, tg[0], tg[2], true, lane);
  STAMP(2);
  if (PRIO >= 1) __builtin_amdgcn_s_setprio(1);
  float nmu, r;
  stat_get(rws, 128 * i + 32 * rg, lane, tag, sg, nmu, r);
  const float rl = r * LOG2E;
  const u32 ib = (32 * rg + s) * 64 + ((h ^ ((s >> 1) & 7)) * 8);
  const u32 ia[4] = {ib, ib ^ 16, ib ^ 32, ib ^ 48};
#pragma unroll
  for (int t = 0; t < 2; ++t) {
    float c16[16];
    cs_get(c16, tg[t], lane);
    const f32x16 pl = phi_logits(acc[t], c16, nmu, a0[t], a1[t]);
#pragma unroll
    for (int hh = 0; hh < 2; ++hh) {
      float ep_[8], en_[8], zp, zn;
      phi_exp(pl, hh, rl, ep_, en_, zp, zn);
      const float ip = rcp(zp), in = rcp(zn);
      const u32 base = L_PHI + (2 * tg[t] + hh) * 8192;
#pragma unroll
      for (int g = 0; g < 2; ++g) {
        u32x2 vp = {pk2(ep_[4 * g] * ip, ep_[4 * g + 1] * ip), pk2(ep_[4 * g + 2] * ip, ep_[4 * g + 3] * ip)};
        u32x2 vn = {pk2(en_[4 * g] * in, en_[4 * g + 1] * in), pk2(en_[4 * g + 2] * in, en_[4 * g + 3] * in)};
        *(LAS u32x2*)(lds + base + ia[g]) = vp;
        *(LAS u32x2*)(lds + base + ia[2 + g]) = vn;
      }
    }
  }
#pragma unroll
  for (int t = 0; t < 2; ++t) {
    float c16[16];
    cs_get(c16, tg[2 + t], lane);
    const u32 base = L_V + (tg[2 + t] - 2) * 8192;
#pragma unroll
    for (int g = 0; g < 4; ++g) {
      float v[4];
#pragma unroll
      for (int e = 0; e < 4; ++e) v[e] = fmaf(nmu, c16[4 * g + e], acc[2 + t][4 * g + e]) * r;
      u32x2 vv = {pk2(v[0], v[1]), pk2(v[2], v[3])};
      *(LAS u32x2*)(lds + base + ia[g]) = vv;
    }
  }
  arrive(C_PHI, lane);
  STAMP(3);
  lds_spin(L_CNT + 4 * C_PHI, 4);
  {
    const int blk = (lane >> 4) & 1, q = (lane & 15) >> 2, pp = lane & 3;
    const int r0 = 8 * h + q, r1 = r0 + 4;
    const u32 pa0 = L_PHI + rg * 8192 + r0 * 64 + (((4 * blk + pp) ^ ((r0 >> 1) & 7)) * 8);
    const u32 pa1 = L_PHI + rg * 8192 + r1 * 64 + (((4 * blk + pp) ^ ((r1 >> 1) & 7)) * 8);
    const u32 vb = L_V + (rg >> 1) * 8192;
    const u32 pv0 = vb + r0 * 64 + (((4 * (rg & 1) + pp) ^ ((r0 >> 1) & 7)) * 8);
    const u32 pv1 = vb + r1 * 64 + (((4 * (rg & 1) + pp) ^ ((r1 >> 1) & 7)) * 8);
    const bf16x8 ones = asfrag(0x3f803f80u, 0x3f803f80u, 0x3f803f80u, 0x3f803f80u);
    f32x16 d = {};
#pragma unroll
    for (int u = 0; u < 8; ++u) {
      const bf16x8 vv = ldtr(pv0 + u * 1024, pv1 + u * 1024);
      d = mfma32(ldtr(pa0 + u * 1024, pa1 + u * 1024), blk ? ones : vv, d);
    }
    asm volatile("" :: "v"(d));
    arrive(C_IMG, lane);
    if (s <= 16) {
#pragma unroll
      for (int u = 0; u < 2; ++u) {
        u32x4 v = {pk2(d[8 * u], d[8 * u + 1]), pk2(d[8 * u + 2], d[8 * u + 3]), pk2(d[8 * u + 4], d[8 * u + 5]), pk2(d[8 * u + 6], d[8 * u + 7])};
        st128_sc1(rws, v, (2 * s + h) * 16, WS_KV + (((pr * 16 + i) * 4 + rg) * 2 + u) * 544);
      }
    }
  }
  STAMP(4);
  wait_vm0();
  if (arrive(C_KV, lane) == 3) set_flag(p.ws, WS_FK + (pr * 16 + i) * 128, tag, lane);
  STAMP(5);
  STAMP_DUMP;
}

DI void roleB_q(const Params& p, int w, int lane, int i, int j) {
  STAMP_DECL;
  const int rg = w - 4, pr = j >> 1, odd = rg & 1;
  const int s = lane & 31, h = lane >> 5;
  bf16x8 a0[2], a1[2];
  load_wphi(p, 4 * pr + 2 * odd, lane, a0[0], a1[0]);
  load_wphi(p, 4 * pr + 2 - 2 * odd, lane, a0[1], a1[1]);
  const int tg[3] = {odd, 1 - odd, 7};
  const rsrc_t rws = mkrs(p.ws, WS_END);
  const u64 tag = compute_prologue(p, rws, w, lane, i, j);
  P1<3> ad; ad.init(rg, lane, tg);
  f32x16 acc[3] = {};
  f32x4 cs0 = {}, cs2 = {};
  u32x4 sg;
  STAMP(0);
  p1_main(ad, acc, cs0, cs2, lane, rws, 128 * i + 32 * rg, sg);
  STAMP(1);
  p1_finish(cs0, cs2, tg[0], tg[2], !odd, lane);
  STAMP(2);
  float nmu, r;
  stat_get(rws, 128 * i + 32 * rg, lane, tag, sg, nmu, r);
  const float rl = r * LOG2E;
  {
    float c16[16];
    u32x4 o[2];
    cs_get(c16, 7, lane);
    silu_tile(acc[2], c16, nmu, -rl, o);
    store_h(rws, i, rg, 2 * h_unit0(pr, 1) + 3, lane, o);
  }
  STAMP(3);
  bf16x8 bq[4][2];
  float zeps[4];
#pragma unroll
  for (int t = 0; t < 2; ++t) {
    float c16[16];
    cs_get(c16, tg[t], lane);
    const f32x16 pl = phi_logits(acc[t], c16, nmu, a0[t], a1[t]);
#pragma unroll
    for (int hh = 0; hh < 2; ++hh) {
      float ep_[8], en_[8], zp, zn;
      phi_exp(pl, hh, rl, ep_, en_, zp, zn);
      const float rho = zp * rcp(zn);
      bq[2 * t + hh][0] = asfrag(pk2(ep_[0], ep_[1]), pk2(ep_[2], ep_[3]), pk2(ep_[4], ep_[5]), pk2(ep_[6], ep_[7]));
      bq[2 * t + hh][1] = asfrag(pk2(en_[0] * rho, en_[1] * rho), pk2(en_[2] * rho, en_[3] * rho), pk2(en_[4] * rho, en_[5] * rho), pk2(en_[6] * rho, en_[7] * rho));
      zeps[2 * t + hh] = zp * 1e-6f;
    }
    if (t == 0) {
      wait_vm0();
      if (arrive(C_H, lane) == 7) set_flag(p.ws, WS_FH + (i * 16 + j) * 128, tag, lane);
    }
  }
  STAMP(4);
  poll16(p.ws, WS_FK + pr * 16 * 128, 128, tag, lane, 15);
  STAMP(5);
  {
    u32x4 pv[2][8];
    u32x2 pz[2];
#pragma unroll
    for (int u = 0; u < 2; ++u) {
      const u32 pbase = WS_KV + ((pr * 16 * 4 + rg) * 2 + u) * 544;
#pragma unroll
      for (int m = 0; m < 8; ++m)
        pv[u][m] = __builtin_amdgcn_raw_buffer_load_b128(rws, ((lane >> 5) * 8 * 544) + (lane & 31) * 16, pbase + 2 * m * 8 * 544, 16);
      pz[u] = __builtin_amdgcn_raw_buffer_load_b64(rws, (lane >> 2) * 8 * 544 + 512 + (lane & 3) * 8, pbase, 16);
    }
    u32 ps[4];
#pragma unroll
    for (int e = 0; e < 4; ++e) ps[e] = (s == 2 * e ? 0x00003f80u : 0u) | (s == 2 * e + 1 ? 0x3f800000u : 0u);
    const bf16x8 psel = asfrag(ps[0], ps[1], ps[2], ps[3]);
#pragma unroll
    for (int u = 0; u < 2; ++u) {
      f32x16 sm = {};
#pragma unroll
      for (int m = 0; m < 8; ++m) sm = mfma32(psel, __builtin_bit_cast(bf16x8, pv[u][m]), sm);
      float zz[4] = {u2f(pz[u][0] << 16), u2f(pz[u][0] & 0xffff0000u), u2f(pz[u][1] << 16), u2f(pz[u][1] & 0xffff0000u)};
#pragma unroll
      for (int e = 0; e < 4; ++e) {
        float v = zz[e];
        v += u2f(__builtin_amdgcn_update_dpp(0, (int)f2u(v), 0x128, 0xf, 0xf, false));
        v += u2f(__builtin_amdgcn_update_dpp(0, (int)f2u(v), 0x124, 0xf, 0xf, false));
        u32x2 t = __builtin_amdgcn_permlane16_swap(f2u(v), f2u(v), false, false);
        v = u2f(t[0]) + u2f(t[1]);
        zz[e] = xsum(v);
      }
      const u32 kb = L_KVA + (rg * 2 + u) * 544;
      { u32x2 v = {pk2(sm[0], sm[1]), pk2(sm[2], sm[3])}; *(LAS u32x2*)(lds + kb + s * 16 + h * 8) = v; }
      if (lane < 4) { u32x2 v = {pk2(zz[0], zz[1]), pk2(zz[2], zz[3])}; *(LAS u32x2*)(lds + kb + 512 + lane * 8) = v; }
    }
  }
  arrive(C_CH, lane);
  lds_spin(L_CNT + 4 * C_CH, 4);
  STAMP(6);
#pragma unroll
  for (int hh = 0; hh < 4; ++hh) {
    const u32 ao = (s < 16 ? s : 16) * 32 + h * 16;
    f32x16 d = {};
    const int ah = hh ^ (2 * odd);
    d = mfma32(ld128(L_KVA + (ah * 2) * 544 + ao), bq[hh][0], d);
    d = mfma32(ld128(L_KVA + (ah * 2 + 1) * 544 + ao), bq[hh][1], d);
    const float inv = rcp(d[8] + zeps[hh]);
    u32x4 v = {pk2(d[0] * inv, d[1] * inv), pk2(d[2] * inv, d[3] * inv), pk2(d[4] * inv, d[5] * inv), pk2(d[6] * inv, d[7] * inv)};
    st128_sc1(rws, v, lane * 16, WS_O + ((4 * i + rg) * 32 + 4 * pr + ah) * 1024);
  }
  STAMP(7);
  wait_vm0();
  if (arrive(C_O, lane) == 3) set_flag(p.ws, WS_FO + (i * 8 + pr) * 128, tag, lane);
  STAMP(8);
  STAMP_DUMP;
}

__global__ void __launch_bounds__(768, 3) fused_hedgehog_ffn(Params p) {
  const int tid = threadIdx.x, lane = tid & 63;
  const int w = __builtin_amdgcn_readfirstlane(tid >> 6);
  const int b = blockIdx.x, g = b & 7, m = b >> 3;
#define MAP 2
  const int i = MAP == 0 ? 4 * (g >> 1) + (m & 3) : MAP == 1 ? 2 * g + (m & 1) : MAP == 2 ? 8 * (g >> 2) + (m & 7) : (b >> 4);
  const int j = MAP == 0 ? 8 * (g & 1) + (m >> 2) : MAP == 1 ? (m >> 1) : MAP == 2 ? 4 * (g & 3) + (m >> 3) : (b & 15);
  if (w == 4) {
    *(LAS u32*)(lds + L_CNT + 4 * (lane & 31)) = 0;
#pragma unroll
    for (int q = 0; q < 4; ++q) *(LAS u32*)(lds + L_CS + 4 * (lane + 64 * q)) = 0;
    wait_lgkm0();
  }
#define MAP2 1
  const int i2 = MAP2 == 0 ? 2 * i + (j & 1) : 8 * (g >> 1) + (m & 7), j2 = MAP2 == 0 ? j >> 1 : 4 * (g & 1) + (m >> 3);
  if (w >= 8) loader_main(p, w, lane, i, j, i2, j2);
  else if (w < 4) roleA_main(p, w, lane, i, j, i2, j2);
  else if (j & 1) roleB_q(p, w, lane, i, j);
  else roleB_kv(p, w, lane, i, j);
}

extern "C" void kernel_launch(void* const* d_in, const int* in_sizes, int n_in, void* d_out, int out_size, void* d_ws, size_t ws_size,
                              hipStream_t stream) {
  Params p;
  p.x = (const u16*)d_in[0]; p.wqkv = (const u16*)d_in[1]; p.wphi = (const u16*)d_in[2]; p.wo = (const u16*)d_in[3];
  p.w1 = (const u16*)d_in[5]; p.w2 = (const u16*)d_in[6]; p.out = (u16*)d_out; p.ws = (char*)d_ws;
  fused_hedgehog_ffn<<<256, 768, 0, stream>>>(p);
}
```

```cpp
#include <hip/hip_runtime.h>

typedef unsigned short u16;
typedef unsigned u32;
typedef unsigned long long u64;
typedef short bf16x8 __attribute__((ext_vector_type(8)));
typedef short s16x4 __attribute__((ext_vector_type(4)));
typedef __bf16 bf2 __attribute__((ext_vector_type(2)));
typedef float f32x16 __attribute__((ext_vector_type(16)));
typedef float f32x4 __attribute__((ext_vector_type(4)));
typedef u32 u32x4 __attribute__((ext_vector_type(4)));
typedef u32 u32x2 __attribute__((ext_vector_type(2)));
#define LAS __attribute__((address_space(3)))
#define GAS __attribute__((address_space(1)))
#define DI __device__ __forceinline__
#define RLX __ATOMIC_RELAXED, __HIP_MEMORY_SCOPE_AGENT
#define RLXW __ATOMIC_RELAXED, __HIP_MEMORY_SCOPE_WORKGROUP

#define PRIO 1
#define TIMING 0

constexpr int S_ = 2048, D_ = 512, F_ = 1792;
constexpr float LOG2E = 1.4426950408889634f;

constexpr u32 WS_E = 0;
constexpr u32 WS_FH = 4096;
constexpr u32 WS_FK = WS_FH + 32768;
constexpr u32 WS_FO = WS_FK + 32768;
constexpr u32 WS_ST = 102400;
constexpr u32 WS_KV = 262144;
constexpr u32 WS_H = 1u << 20;
constexpr u32 WS_O = WS_H + 64u * 112u * 1024u;
constexpr u32 WS_END = WS_O + 64u * 32u * 1024u;
constexpr u32 WS_TM = 16u << 20;

constexpr u32 W1R = 0, NW1 = 5;
constexpr u32 X1R = 81920, NX1 = 4;
constexpr u32 L_PHI = 98304, L_V = 131072;
constexpr u32 L_KVA = 147456;
constexpr u32 MISC = 157696;
constexpr u32 L_CS = MISC + 1024;
constexpr u32 L_CNT = MISC + 2048, L_TAG = MISC;
constexpr u32 L_PF = L_CNT + 64;
enum { C_H = 0, C_PHI = 1, C_KV = 2, C_CH = 3, C_O = 4, C_HR = 5, C_OR = 6, C_IMG = 7, C_RG = 8, C_RS = 9 };
constexpr u32 SPIN_CAP = 1u << 22;

__shared__ __attribute__((aligned(1024))) u32 lds32[40960];
#define lds ((char*)lds32)

DI f32x16 mfma32(bf16x8 a, bf16x8 b, f32x16 c) { return __builtin_amdgcn_mfma_f32_32x32x16_bf16(a, b, c, 0, 0, 0); }
DI u32 pk2(float a, float b) { bf2 v = {(__bf16)a, (__bf16)b}; return __builtin_bit_cast(u32, v); }
DI float dot2(u32 a, u32 b, float c) { return __builtin_amdgcn_fdot2_f32_bf16(__builtin_bit_cast(bf2, a), __builtin_bit_cast(bf2, b), c, false); }
DI float ex2(float x) { return __builtin_amdgcn_exp2f(x); }
DI float rcp(float x) { return __builtin_amdgcn_rcpf(x); }
DI float fmax2(float a, float b) { return __builtin_elementwise_maximum(a, b); }
DI float fmin2(float a, float b) { return __builtin_elementwise_minimum(a, b); }
DI u32 f2u(float x) { return __builtin_bit_cast(u32, x); }
DI float u2f(u32 x) { return __builtin_bit_cast(float, x); }
DI bf16x8 ld128(u32 off) { return *(LAS bf16x8*)(lds + off); }
DI f32x4 ldf4(u32 off) { return *(LAS f32x4*)(lds + off); }
DI float ldf(u32 off) { return *(LAS float*)(lds + off); }
DI void stf(u32 off, float v) { *(LAS float*)(lds + off) = v; }
DI bf16x8 ldtr(u32 off0, u32 off1) {
  s16x4 a = __builtin_amdgcn_ds_read_tr16_b64_v4i16((LAS s16x4*)(lds + off0));
  s16x4 b = __builtin_amdgcn_ds_read_tr16_b64_v4i16((LAS s16x4*)(lds + off1));
  return __builtin_shufflevector(a, b, 0, 1, 2, 3, 4, 5, 6, 7);
}
DI bf16x8 asfrag(u32 a, u32 b, u32 c, u32 d) { u32x4 v = {a, b, c, d}; return __builtin_bit_cast(bf16x8, v); }
DI void barrier() { asm volatile("s_barrier" ::: "memory"); }
DI void wait_lgkm0() { asm volatile("s_waitcnt lgkmcnt(0)" ::: "memory"); }
DI void wait_vm0() { asm volatile("s_waitcnt vmcnt(0)" ::: "memory"); }
template <int N> DI void wait_vm() { asm volatile("s_waitcnt vmcnt(%0)" ::"n"(N) : "memory"); }
DI float xsum(float x) { u32x2 r = __builtin_amdgcn_permlane32_swap(f2u(x), f2u(x), false, false); return u2f(r[0]) + u2f(r[1]); }
DI void xmaxmin(float& mx, float& mn) {
  u32x2 r = __builtin_amdgcn_permlane32_swap(f2u(mx), f2u(-mn), false, false);
  float m = fmax2(u2f(r[0]), u2f(r[1]));
  u32x2 q = __builtin_amdgcn_permlane32_swap(f2u(m), f2u(m), false, false);
  mx = u2f(q[0]); mn = -u2f(q[1]);
}
DI void xsum2(float& a, float& b) {
  u32x2 r = __builtin_amdgcn_permlane32_swap(f2u(a), f2u(b), false, false);
  float m = u2f(r[0]) + u2f(r[1]);
  u32x2 q = __builtin_amdgcn_permlane32_swap(f2u(m), f2u(m), false, false);
  a = u2f(q[0]); b = u2f(q[1]);
}
DI void lds_st(u32 off, u32 v) { __hip_atomic_store((LAS u32*)&lds32[off >> 2], v, RLXW); }
DI u32 ldsaddr(u32 off) { return (u32)(size_t)(LAS char*)lds + off; }
DI u32x4 asm_ld128(u32 off) {
  u32x4 v;
  asm volatile("ds_read_b128 %0, %1\n\ts_waitcnt lgkmcnt(0)" : "=&v"(v) : "v"(ldsaddr(off)) : "memory");
  return v;
}
DI void asm_st32(u32 off, u32 val) { asm volatile("ds_write_b32 %0, %1" ::"v"(ldsaddr(off)), "v"(val) : "memory"); }
DI u32 asm_ld32(u32 off) {
  u32 v;
  asm volatile("ds_read_b32 %0, %1\n\ts_waitcnt lgkmcnt(0)" : "=&v"(v) : "v"(ldsaddr(off)) : "memory");
  return v;
}
DI void lds_spin(u32 off, u32 target) {
#pragma unroll 1
  for (u32 it = 0; it < SPIN_CAP; ++it) {
    if (__builtin_amdgcn_readfirstlane(asm_ld32(off)) >= target) break;
    __builtin_amdgcn_s_sleep(1);
  }
}
DI u32 lds_add_rtn(u32 off) { return __hip_atomic_fetch_add((LAS u32*)&lds32[off >> 2], 1u, RLXW); }
DI u32 lds_ld(u32 off) { return __hip_atomic_load((LAS u32*)&lds32[off >> 2], RLXW); }
DI u32 arrive(int cnt, int lane) {
  asm volatile("" ::: "memory");
  u32 old = 0;
  if (lane == 0) old = lds_add_rtn(L_CNT + 4 * cnt);
  old = __builtin_amdgcn_readfirstlane(old);
  asm volatile("" ::: "memory");
  return old;
}
DI void poll16(const char* ws, u32 base, u32 stride, u64 tag, int lane, int mask) {
  GAS u64* p = (GAS u64*)(ws + base + (u32)(lane & mask) * stride);
#pragma unroll 1
  for (u32 it = 0; it < SPIN_CAP; ++it) {
    u64 v = __hip_atomic_load(p, RLX);
    if (__all(v == tag)) break;
    __builtin_amdgcn_s_sleep(1);
  }
  asm volatile("" ::: "memory");
}
DI void set_flag(char* ws, u32 off, u64 tag, int lane) {
  if (lane == 0) __hip_atomic_store((GAS u64*)(ws + off), tag, RLX);
}

#define STAMP(k) do { } while (0)
#define STAMP_DECL do { } while (0)
#define STAMP_DUMP do { } while (0)

struct Params { const u16* x; const u16* wqkv; const u16* wphi; const u16* wo; const u16* w1; const u16* w2; u16* out; char* ws; };

typedef __amdgpu_buffer_rsrc_t rsrc_t;
DI rsrc_t mkrs(const void* p, u32 bytes) { return __builtin_amdgcn_make_buffer_rsrc((void*)p, 0, bytes, 0x00020000); }
DI void st128_sc1(rsrc_t rs, u32x4 v, u32 voff, u32 soff) { __builtin_amdgcn_raw_buffer_store_b128(v, rs, voff + soff, 0, 16); }
template <int AUX> DI void dma16(rsrc_t rs, u32 ldsoff, u32 voff, u32 soff) {
  __builtin_amdgcn_raw_ptr_buffer_load_lds(rs, (LAS void*)(lds + ldsoff), 16, voff, soff, 0, AUX);
}

DI int h_unit0(int p, int isq) { return isq ? ((p & 1) ? (7 * p + 3) >> 1 : (7 * p) >> 1) : ((p & 1) ? (7 * p - 1) >> 1 : (7 * p + 4) >> 1); }

DI void loader_main(const Params& p, int w, int lane, int i, int j, int i2, int j2) {
  STAMP_DECL;
  const int wl = w - 8, pr = j >> 1, isq = j & 1;
  const rsrc_t rx = mkrs(p.x, S_ * D_ * 2), rq = mkrs(p.wqkv, D_ * 1536 * 2), r1 = mkrs(p.w1, D_ * F_ * 2);
  const u32 gsw = ((((lane & 7) >> 2) ^ ((lane >> 4) & 1)) * 64) + (lane & 3) * 16;
  const u32 xv = (lane >> 3) * 1024 + (((lane & 7) ^ ((4 * (wl & 1) + (lane >> 4)) & 7)) * 16);
  const u32 qv = (lane >> 3) * 3072 + gsw, hv1 = (lane >> 3) * 3584 + gsw;
  const u32 xs = (128 * i + 8 * wl) * 1024;
  const u32 s0 = (isq ? pr * 128 : 1024 + pr * 128) + 8 * wl * 3072, s1 = 2048 + pr * 128 + 8 * wl * 3072;
  const u32 h0 = h_unit0(pr, isq) * 128 + 8 * wl * 3584;
  auto issue_w = [&](int c, u32 slot) {
    const u32 d = slot + wl * 1024;
    dma16<0>(rq, d, qv, s0 + c * (32 * 3072));
    if (!isq) dma16<0>(rq, d + 4096, qv, s1 + c * (32 * 3072));
    dma16<0>(r1, d + 8192, hv1, h0 + c * (32 * 3584));
    dma16<0>(r1, d + 12288, hv1, h0 + 128 + c * (32 * 3584));
  };
  auto issue_x = [&](int P, int half) {
    const u32 d = X1R + (P & 3) * 16384 + (wl + 8 * half) * 1024;
    dma16<0>(rx, d, xv, xs + half * 65536 + P * 128);
    dma16<0>(rx, d + 4096, xv, xs + half * 65536 + 32768 + P * 128);
  };
  STAMP(0);
  issue_x(0, 0); issue_x(0, 1);
  issue_w(0, 0); issue_w(1, 16384);
  issue_x(1, 0); issue_x(1, 1);
  issue_w(2, 2 * 16384);
  STAMP(1);
  if (isq) wait_vm<7>(); else wait_vm<8>();
  barrier();
  issue_w(3, 3 * 16384);
  issue_x(2, 0); issue_x(2, 1);
  issue_w(4, 4 * 16384);
  if (isq) wait_vm<10>(); else wait_vm<12>();
  barrier();
  issue_w(5, 0);
  issue_x(3, 0); issue_x(3, 1);
  u32 wslot = 16384;
#pragma unroll 1
  for (int c = 2; c < 16; ++c) {
    if (isq) {
      if (c <= 11) wait_vm<10>(); else if (c == 12) wait_vm<6>(); else if (c == 13) wait_vm<3>(); else wait_vm<0>();
    } else {
      if (c <= 11) wait_vm<12>(); else if (c == 12) wait_vm<8>(); else if (c == 13) wait_vm<4>(); else wait_vm<0>();
    }
    barrier();
    if (c <= 11) issue_w(c + 4, wslot);
    if (c <= 9) issue_x((c + 6) >> 1, c & 1);
    wslot = wslot == 4 * 16384 ? 0 : wslot + 16384;
  }
  STAMP(2);
  barrier();
  STAMP(3);
  const rsrc_t rw2 = mkrs(p.w2, F_ * D_ * 2);
  const u32 wv = (lane >> 3) * 1024 + gsw, wsrc = 16 * 28 * wl * 1024 + j2 * 128;
  auto issue_w2 = [&](int n) {
    dma16<0>(rw2, n * 8192 + wl * 2048, wv, wsrc + n * 16384);
    dma16<0>(rw2, n * 8192 + wl * 2048 + 1024, wv, wsrc + n * 16384 + 8192);
  };
#pragma unroll 1
  for (int n = 0; n < 12; ++n) issue_w2(n);
  wait_vm0();
  asm_st32(L_PF + 4 * wl, 12);
  if (!isq) lds_spin(L_CNT + 4 * C_IMG, 4);
#pragma unroll 1
  for (int n = 12; n < 18; ++n) issue_w2(n);
  wait_vm0();
  asm_st32(L_PF + 4 * wl, 18);
  STAMP(4);
  if (wl == 0) {
    const u32x4 tg = asm_ld128(L_TAG);
    const u64 tag = (u64)tg[0] | ((u64)tg[1] << 32);
    poll16(p.ws, WS_FH + (i2 >> 1) * 16 * 128, 128, tag, lane, 15);
    asm_st32(L_CNT + 4 * C_HR, 1);
    STAMP(5);
    poll16(p.ws, WS_FO + (i2 >> 1) * 8 * 128, 128, tag, lane, 7);
    asm_st32(L_CNT + 4 * C_OR, 1);
    STAMP(6);
    if (blockIdx.x == 0) {
      poll16(p.ws, WS_FO, 128, tag, lane, 63);
      poll16(p.ws, WS_FO + 64 * 128, 128, tag, lane, 63);
      if (lane == 0) __hip_atomic_store((GAS u64*)(p.ws + WS_E), tag, RLX);
    }
  }
  STAMP_DUMP;
}

template <int NT> struct Frag { bf16x8 x; bf16x8 w[NT]; };

template <int NT> struct P1 {
  u32 xa[4];
  u32 wa[NT];
  DI void init(int rg, int lane, const int (&tg)[NT]) {
    const int s = lane & 31, h = lane >> 5, blk = (lane >> 4) & 1, q = (lane & 15) >> 2, pp = lane & 3, hi = rg >> 1;
#pragma unroll
    for (int k = 0; k < 4; ++k) xa[k] = X1R + (((32 * rg + s) * 128 + ((h ^ ((s >> 1) & 7)) * 16)) ^ ((k ^ hi) << 5));
#pragma unroll
    for (int t = 0; t < NT; ++t)
      wa[t] = W1R + (tg[t] >> 1) * 4096 + hi * 2048 + (8 * h + q) * 128 + (((tg[t] & 1) ^ (q >> 1)) * 64) + 32 * blk + 8 * pp;
  }
};
#define SB() __builtin_amdgcn_sched_barrier(0)
template <int NT> struct Addr { u32 w[NT]; };
template <int NT> DI Addr<NT> waddr(const P1<NT>& a, u32 slot) {
  Addr<NT> r;
#pragma unroll
  for (int t = 0; t < NT; ++t) r.w[t] = a.wa[t] + slot;
  return r;
}

template <int NT, bool CS, bool RELOAD>
DI void p1_step(Frag<NT>& f, f32x16 (&acc)[NT], bf16x8 sel, f32x4& cs0, f32x4& cs2, const Addr<NT>& wa, u32 xa, int ks) {
#pragma unroll
  for (int t = 0; t < NT; ++t) {
    acc[t] = mfma32(f.w[t], f.x, acc[t]);
    if constexpr (CS) {
      if (t == 0) cs0 = __builtin_amdgcn_mfma_f32_16x16x32_bf16(sel, f.w[0], cs0, 0, 0, 0);
      if (t == 2) cs2 = __builtin_amdgcn_mfma_f32_16x16x32_bf16(sel, f.w[2], cs2, 0, 0, 0);
    }
    SB();
    if constexpr (RELOAD) {
      const u32 ad = wa.w[t] ^ (ks << 11);
      f.w[t] = ldtr(ad, ad + 512);
      if (t == NT - 1) f.x = ld128(xa);
    }
    SB();
  }
}
template <int NT, bool LAST>
DI void p1_pair(const P1<NT>& a, Frag<NT>& f0, Frag<NT>& f1, f32x16 (&acc)[NT], bf16x8 sel, f32x4& cs0, f32x4& cs2, u32 w1, u32 w2, u32 xsl, u32 xn) {
  {
    const Addr<NT> wa = waddr(a, w1);
    p1_step<NT, true, true>(f0, acc, sel, cs0, cs2, wa, a.xa[2] + xsl, 0);
    p1_step<NT, false, true>(f1, acc, sel, cs0, cs2, wa, a.xa[3] + xsl, 1);
  }
  barrier();
  {
    const Addr<NT> wa = waddr(a, w2);
    p1_step<NT, true, !LAST>(f0, acc, sel, cs0, cs2, wa, a.xa[0] + xn, 0);
    p1_step<NT, false, !LAST>(f1, acc, sel, cs0, cs2, wa, a.xa[1] + xn, 1);
  }
  if constexpr (!LAST) barrier();
}
struct Stat { u32x4 g; };
DI u32x4 stat_load(rsrc_t rws, int row0, int lane) { return __builtin_amdgcn_raw_buffer_load_b128(rws, (lane & 31) * 16, WS_ST + row0 * 16, 16); }
DI void stat_get(rsrc_t rws, int row0, int lane, u64 tag, u32x4 g, float& nmu, float& r) {
#pragma unroll 1
  for (u32 it = 0; it < SPIN_CAP; ++it) {
    if (__all(g[1] == (u32)tag && g[3] == (u32)(tag >> 32))) break;
    __builtin_amdgcn_s_sleep(1);
    g = stat_load(rws, row0, lane);
  }
  nmu = -u2f(g[0]); r = u2f(g[2]);
}
DI void stat_publish(const Params& p, rsrc_t rws, int row, int lane, u64 tag, u32x4 xv) {
  float s0 = 0.f, s1 = 0.f, q0 = 0.f, q1 = 0.f;
#pragma unroll
  for (int e = 0; e < 4; ++e) {
    const float lo = u2f(xv[e] << 16), hi = u2f(xv[e] & 0xffff0000u);
    s0 += lo; s1 += hi; q0 = fmaf(lo, lo, q0); q1 = fmaf(hi, hi, q1);
  }
  float s = s0 + s1, q = q0 + q1;
#define DPP_ADD(v, ctrl) v += u2f(__builtin_amdgcn_update_dpp(0, (int)f2u(v), ctrl, 0xf, 0xf, false))
  DPP_ADD(s, 0x128); DPP_ADD(q, 0x128); DPP_ADD(s, 0x124); DPP_ADD(q, 0x124);
  DPP_ADD(s, 0x122); DPP_ADD(q, 0x122); DPP_ADD(s, 0x121); DPP_ADD(q, 0x121);
#undef DPP_ADD
  { u32x2 a = __builtin_amdgcn_permlane16_swap(f2u(s), f2u(q), false, false);
    float m = u2f(a[0]) + u2f(a[1]);
    u32x2 b = __builtin_amdgcn_permlane16_swap(f2u(m), f2u(m), false, false);
    s = u2f(b[0]); q = u2f(b[1]); }
  xsum2(s, q);
  const float mu = s * (1.0f / D_);
  const float var = fmaf(-mu, mu, q * (1.0f / D_));
  const float r = __builtin_amdgcn_rsqf(fmax2(var, 0.f) + 1e-5f);
  if (lane == 0) {
    const u32x4 g = {f2u(mu), (u32)tag, f2u(r), (u32)(tag >> 32)};
    st128_sc1(rws, g, 0, WS_ST + row * 16);
  }
}
template <int NT> DI void p1_main(const P1<NT>& a, f32x16 (&acc)[NT], f32x4& cs0, f32x4& cs2, int lane, rsrc_t rws, int row0, u32x4& sg) {
  Frag<NT> f0, f1;
  const u32 one = ((lane & 15) == ((lane >> 4) & 1)) ? 0x3f803f80u : 0u;
  const bf16x8 sel = asfrag(one, one, one, one);
#pragma unroll
  for (int t = 0; t < NT; ++t) asm volatile("" : "+v"(acc[t]));
  barrier();
#pragma unroll
  for (int t = 0; t < NT; ++t) f0.w[t] = ldtr(a.wa[t], a.wa[t] + 512);
  f0.x = ld128(a.xa[0]);
  SB();
#pragma unroll
  for (int t = 0; t < NT; ++t) f1.w[t] = ldtr(a.wa[t] ^ 2048, (a.wa[t] ^ 2048) + 512);
  f1.x = ld128(a.xa[1]);
  SB();
  u32 wsl = 0, xsl = 0;
#pragma unroll 1
  for (int cp = 0; cp < 7; ++cp) {
    const u32 w1 = wsl == 4 * 16384 ? 0 : wsl + 16384;
    const u32 w2 = w1 == 4 * 16384 ? 0 : w1 + 16384;
    const u32 xn = (xsl + 16384) & 65535;
    p1_pair<NT, false>(a, f0, f1, acc, sel, cs0, cs2, w1, w2, xsl, xn);
    wsl = w2; xsl = xn;
  }
  sg = stat_load(rws, row0, lane);
  p1_pair<NT, true>(a, f0, f1, acc, sel, cs0, cs2, (15 % NW1) * 16384, 0, xsl, 0);
}

DI void p1_finish(f32x4 cs0, f32x4 cs2, int g0, int g2, bool add2, int lane) {
  if (lane < 16) {
    __builtin_amdgcn_ds_faddf((LAS float*)&lds32[(L_CS + g0 * 128 + lane * 4) >> 2], cs0[0], 0, 0, false);
    __builtin_amdgcn_ds_faddf((LAS float*)&lds32[(L_CS + g0 * 128 + 64 + lane * 4) >> 2], cs0[1], 0, 0, false);
    if (add2) {
      __builtin_amdgcn_ds_faddf((LAS float*)&lds32[(L_CS + g2 * 128 + lane * 4) >> 2], cs2[0], 0, 0, false);
      __builtin_amdgcn_ds_faddf((LAS float*)&lds32[(L_CS + g2 * 128 + 64 + lane * 4) >> 2], cs2[1], 0, 0, false);
    }
  }
  wait_lgkm0();
  barrier();
}
DI void cs_get(float (&c)[16], int gt, int lane) {
  const int h = lane >> 5;
#pragma unroll
  for (int g = 0; g < 4; ++g) {
    f32x4 v = ldf4(L_CS + gt * 128 + (8 * g + 4 * h) * 4);
    c[4 * g] = v[0]; c[4 * g + 1] = v[1]; c[4 * g + 2] = v[2]; c[4 * g + 3] = v[3];
  }
}

DI void silu_tile(const f32x16& acc, const float (&cs)[16], float nmu, float nrl, u32x4 (&o)[2]) {
  float y[16];
#pragma unroll
  for (int e = 0; e < 16; ++e) {
    const float t = fmaf(nmu, cs[e], acc[e]);
    y[e] = t * rcp(1.0f + ex2(t * nrl));
  }
#pragma unroll
  for (int u = 0; u < 2; ++u)
#pragma unroll
    for (int e = 0; e < 4; ++e) o[u][e] = pk2(y[8 * u + 2 * e], y[8 * u + 2 * e + 1]);
}

DI f32x16 phi_logits(const f32x16& acc, const float (&cs)[16], float nmu, bf16x8 a0, bf16x8 a1) {
  float t[16];
#pragma unroll
  for (int e = 0; e < 16; ++e) t[e] = fmaf(nmu, cs[e], acc[e]);
  bf16x8 b0 = asfrag(pk2(t[0], t[1]), pk2(t[2], t[3]), pk2(t[4], t[5]), pk2(t[6], t[7]));
  bf16x8 b1 = asfrag(pk2(t[8], t[9]), pk2(t[10], t[11]), pk2(t[12], t[13]), pk2(t[14], t[15]));
  f32x16 z = {};
  z = mfma32(a0, b0, z);
  z = mfma32(a1, b1, z);
  return z;
}
DI void phi_exp(const f32x16& pl, int hh, float rl, float (&ep)[8], float (&en)[8], float& zp, float& zn) {
  const int o = 8 * hh;
  float mx = fmax2(fmax2(fmax2(fmax2(fmax2(pl[o], pl[o + 1]), pl[o + 2]), fmax2(fmax2(pl[o + 3], pl[o + 4]), pl[o + 5])), pl[o + 6]), pl[o + 7]);
  float mn = fmin2(fmin2(fmin2(fmin2(fmin2(pl[o], pl[o + 1]), pl[o + 2]), fmin2(fmin2(pl[o + 3], pl[o + 4]), pl[o + 5])), pl[o + 6]), pl[o + 7]);
  xmaxmin(mx, mn);
  const float a = -rl * mx, b = rl * mn;
  zp = 0.f; zn = 0.f;
#pragma unroll
  for (int e = 0; e < 8; ++e) {
    ep[e] = ex2(fmaf(pl[8 * hh + e], rl, a));
    en[e] = ex2(fmaf(pl[8 * hh + e], -rl, b));
    zp += ep[e]; zn += en[e];
  }
  xsum2(zp, zn);
}

DI void load_wphi(const Params& p, int head0, int lane, bf16x8& a0, bf16x8& a1) {
  const int row = lane & 31, h = lane >> 5;
  const u16* wp = p.wphi + (head0 + (row >> 4)) * 256 + (row & 15);
  u32 v[4];
#pragma unroll
  for (int e = 0; e < 4; ++e) {
    const int j0 = 2 * e, j1 = 2 * e + 1;
    const int d0 = 8 * (j0 >> 2) + 4 * h + (j0 & 3), d1 = 8 * (j1 >> 2) + 4 * h + (j1 & 3);
    v[e] = (u32)wp[d0 * 16] | ((u32)wp[d1 * 16] << 16);
  }
  const bool lo = row < 16;
  a0 = asfrag(lo ? v[0] : 0u, lo ? v[1] : 0u, lo ? v[2] : 0u, lo ? v[3] : 0u);
  a1 = asfrag(lo ? 0u : v[0], lo ? 0u : v[1], lo ? 0u : v[2], lo ? 0u : v[3]);
}

DI void store_h(rsrc_t rws, int i, int rg, int tile, int lane, const u32x4 (&o)[2]) {
#pragma unroll
  for (int u = 0; u < 2; ++u)
    st128_sc1(rws, o[u], lane * 16, WS_H + ((4 * i + rg) * 112 + 2 * tile + u) * 1024);
}

#define HWIN 7
constexpr int HW = HWIN;
DI u32x4 srd(const void* ptr, u32 bytes) {
  const u64 a = (u64)ptr;
  u32x4 d = {(u32)a, (u32)(a >> 32) & 0xffffu, bytes, 0x00020000u};
  d[0] = __builtin_amdgcn_readfirstlane(d[0]); d[1] = __builtin_amdgcn_readfirstlane(d[1]);
  return d;
}
DI bf16x8 asm_gload(u32x4 rs, u32 voff, u32 soff) {
  bf16x8 v;
  asm volatile("buffer_load_dwordx4 %0, %1, %2, %3 offen sc1" : "=v"(v) : "v"(voff), "s"(rs), "s"(soff) : "memory");
  return v;
}
DI void asm_dma(u32x4 rs, u32 ldsa, u32 voff, u32 soff) {
  asm volatile("s_mov_b32 m0, %0\n\ts_nop 0\n\tbuffer_load_dwordx4 %1, %2, %3 offen lds" ::"s"(ldsa), "v"(voff), "s"(rs), "s"(soff) : "memory");
}
struct P2 { u32 wa[2], hv, wv, hs[2], w2s, wos, ld; u32x4 rws, rw2, rwo; };
constexpr int p2_ops(int m) { return (m + HW < 28 ? 2 : 0) + (m < 18 ? 2 : 0); }
constexpr int p2_younger(int n) {
  int c = 0;
  if (n < HW) { c = 2 * (HW - 1 - n); for (int m = 0; m < n; ++m) c += p2_ops(m); }
  else { c = (n - HW < 18) ? 2 : 0; for (int m = n - HW + 1; m < n; ++m) c += p2_ops(m); }
  return c;
}
DI u32 opq(u32 x) { asm volatile("" : "+v"(x)); return x; }
template <int N> DI void p2a_step(const P2& c, f32x16 (&ac)[4], bf16x8 (&hw)[HW][2], bf16x8 (&wf)[2][2], int q, u32 b0, u32 b1) {
  asm volatile("s_waitcnt vmcnt(%2)" : "+v"(hw[N % HW][0]), "+v"(hw[N % HW][1]) : "n"(p2_younger(N)) : "memory");
  if constexpr (N == 11) lds_spin(L_PF + 4 * q, 18);
  if constexpr (N + 1 < 28) {
    constexpr u32 sl = (N + 1) % 18, o = (sl & 7) * 8192;
    if constexpr ((sl & 7) == 0 || sl == 0) { b0 = opq(c.wa[0] + (sl >> 3) * 65536); b1 = opq(c.wa[1] + (sl >> 3) * 65536); }
    wf[(N + 1) & 1][0] = ldtr(b0 + o, b0 + o + 1024);
    wf[(N + 1) & 1][1] = ldtr(b1 + o, b1 + o + 1024);
  }
  SB();
  ac[0] = mfma32(wf[N & 1][0], hw[N % HW][0], ac[0]);
  ac[1] = mfma32(wf[N & 1][1], hw[N % HW][0], ac[1]);
  ac[2] = mfma32(wf[N & 1][0], hw[N % HW][1], ac[2]);
  ac[3] = mfma32(wf[N & 1][1], hw[N % HW][1], ac[3]);
  SB();
  if constexpr (N + HW < 28) {
    hw[N % HW][0] = asm_gload(c.rws, c.hv, c.hs[0] + (N + HW) * 1024);
    hw[N % HW][1] = asm_gload(c.rws, c.hv, c.hs[1] + (N + HW) * 1024);
  }
  if constexpr (N < 10) {
    asm_dma(c.rw2, c.ld + N * 8192, c.wv, c.w2s + (N + 18) * 16384);
    asm_dma(c.rw2, c.ld + N * 8192 + 1024, c.wv, c.w2s + (N + 18) * 16384 + 8192);
  } else if constexpr (N < 18) {
    asm_dma(c.rwo, c.ld + N * 8192, c.wv, c.wos + (N - 10) * 16384);
    asm_dma(c.rwo, c.ld + N * 8192 + 1024, c.wv, c.wos + (N - 10) * 16384 + 8192);
  }
  SB();
  if constexpr (N + 1 < 28) p2a_step<N + 1>(c, ac, hw, wf, q, b0, b1);
}
template <int N> DI void p2b_step(f32x16 (&ac)[4], bf16x8 (&ow)[8][2], u32 b0, u32 b1) {
  constexpr u32 o = N * 8192;
  asm volatile("s_waitcnt vmcnt(%2)" : "+v"(ow[N][0]), "+v"(ow[N][1]) : "n"(2 * (7 - N)) : "memory");
  const bf16x8 w0 = ldtr(b0 + o, b0 + o + 1024), w1 = ldtr(b1 + o, b1 + o + 1024);
  SB();
  ac[0] = mfma32(w0, ow[N][0], ac[0]);
  ac[1] = mfma32(w1, ow[N][0], ac[1]);
  ac[2] = mfma32(w0, ow[N][1], ac[2]);
  ac[3] = mfma32(w1, ow[N][1], ac[3]);
  SB();
  if constexpr (N < 7) p2b_step<N + 1>(ac, ow, b0, b1);
}
DI void red_send(const f32x16 (&ac)[4], int q, int lane, int slot0) {
  const u32 b = opq(slot0 * 8192 + q * 2048 + lane * 16);
#pragma unroll
  for (int l = 1; l < 4; ++l)
#pragma unroll
    for (int g = 0; g < 4; ++g) {
      const f32x4 v = {ac[l][4 * g], ac[l][4 * g + 1], ac[l][4 * g + 2], ac[l][4 * g + 3]};
      *(LAS f32x4*)(lds + b + (2 * (l - 1) + (g >> 1)) * 8192 + (g & 1) * 1024) = v;
    }
}
DI void red_recv(f32x16& a, int q, int lane, int slot0) {
#pragma unroll
  for (int l = 1; l < 4; ++l) {
    const u32 b = opq(slot0 * 8192 + (q ^ l) * 2048 + lane * 16);
#pragma unroll
    for (int g = 0; g < 4; ++g) {
      const f32x4 v = ldf4(b + (2 * (l - 1) + (g >> 1)) * 8192 + (g & 1) * 1024);
#pragma unroll
      for (int e = 0; e < 4; ++e) a[4 * g + e] += v[e];
    }
  }
}

#define PF_CODE 250
#define PF_DATA 2
DI u64 compute_prologue(const Params& p, rsrc_t rws, int w, int lane, int i, int j, u64 pc) {
  const int row = 128 * i + 8 * j + w;
  const u32x4 xv = *(const GAS u32x4*)((const char*)p.x + row * 1024 + lane * 16);
  const u64 tag = __hip_atomic_load((GAS u64*)(p.ws + WS_E), RLX) + 1;
  u32 pf0 = 0, pf1 = 0;
  const int b = blockIdx.x;
  if (w == 5) {
    const u32 line = (b >> 3) * 8 + (lane & 7);
    if (lane < 8 && line < PF_CODE) pf0 = *(const volatile GAS u32*)((pc & ~127ull) + line * 128);
  }
  if (w >= 4) {
    const u32 t = (b * 4 + (w - 4)) * 64 + lane;
    const char* a = nullptr;
    if (PF_DATA == 1) {
      const u32 t1 = (b * 4 + (w - 4)) * 18 + lane;
      if (lane < 18) { if (t1 < 14336) a = (const char*)p.w2 + t1 * 128; else a = (const char*)p.wo + (t1 - 14336) * 128; }
    } else {
      if (t < 14336) a = (const char*)p.w2 + t * 128; else if (t < 18432) a = (const char*)p.wo + (t - 14336) * 128;
      else if (t < 32768) a = (const char*)p.w1 + (t - 18432) * 128; else if (t < 45056) a = (const char*)p.wqkv + (t - 32768) * 128;
      else if (t < 61440) a = (const char*)p.x + (t - 45056) * 128;
    }
    if (a) pf1 = *(const volatile GAS u32*)a;
  }
  stat_publish(p, rws, row, lane, tag, xv);
  asm volatile("" :: "v"(pf0), "v"(pf1));
  return tag;
}

DI void roleA_main(const Params& p, int w, int lane, int i, int j, int i2, int j2, u64 pc) {
  STAMP_DECL;
  const int rg = w, pr = j >> 1, isq = j & 1, odd = rg & 1;
  const int s = lane & 31, h = lane >> 5;
  const rsrc_t rws = mkrs(p.ws, WS_END);
  const u64 tag = compute_prologue(p, rws, w, lane, i, j, pc);
  if (w == 0 && lane == 0) *(LAS u64*)(lds + L_TAG) = tag;
  const int t0 = (!isq && (pr & 1)) ? 5 : 4;
  const int tg[3] = {t0 + odd, t0 + 1 - odd, t0 + 2};
  P1<3> ad; ad.init(rg, lane, tg);
  f32x16 acc[3] = {};
  f32x4 cs0 = {}, cs2 = {};
  u32x4 sg;
  STAMP(0);
  p1_main(ad, acc, cs0, cs2, lane, rws, 128 * i + 32 * rg, sg);
  STAMP(1);
  p1_finish(cs0, cs2, tg[0], tg[2], !odd, lane);
  STAMP(2);
  if (PRIO == 0 || (PRIO == 1 && isq)) __builtin_amdgcn_s_setprio(1);
  float nmu, r;
  stat_get(rws, 128 * i + 32 * rg, lane, tag, sg, nmu, r);
  const float rl = r * LOG2E;
  const int Tb = 2 * h_unit0(pr, isq) - 4;
#pragma unroll
  for (int t = 0; t < 3; ++t) {
    float c16[16];
    u32x4 o[2];
    cs_get(c16, tg[t], lane);
    silu_tile(acc[t], c16, nmu, -rl, o);
    store_h(rws, i, rg, Tb + tg[t], lane, o);
  }
  STAMP(3);
  wait_vm0();
  if (arrive(C_H, lane) == (isq ? 7u : 3u)) set_flag(p.ws, WS_FH + (i * 16 + j) * 128, tag, lane);
  __builtin_amdgcn_s_setprio(0);
  const int q = rg, rb = q >> 1, cb = q & 1;
  const int orow = 64 * i2 + 32 * rb + s;
  const u32 ooff = orow * 1024 + (64 * j2 + 32 * cb) * 2 + h * 16;
  float nmu2, r2;
  stat_get(rws, 64 * i2 + 32 * rb, lane, tag, stat_load(rws, 64 * i2 + 32 * rb, lane), nmu2, r2);
  P2 c2;
  {
    const int blk = (lane >> 4) & 1, qq = (lane & 15) >> 2, pp = lane & 3;
    c2.wa[0] = q * 2048 + (4 * h + qq) * 128 + ((cb ^ (qq >> 1)) * 64) + 32 * blk + 8 * pp;
    c2.wa[1] = c2.wa[0] ^ 64;
    c2.hv = lane * 16;
    c2.wv = (lane >> 3) * 1024 + ((((lane & 7) >> 2) ^ ((lane >> 4) & 1)) * 64) + (lane & 3) * 16;
    c2.rws = srd(p.ws, WS_END); c2.rw2 = srd(p.w2, F_ * D_ * 2); c2.rwo = srd(p.wo, D_ * D_ * 2);
    c2.hs[0] = WS_H + ((2 * i2 + rb) * 112 + 28 * q) * 1024; c2.hs[1] = WS_H + ((2 * i2 + (rb ^ 1)) * 112 + 28 * q) * 1024;
    c2.w2s = 16 * 28 * q * 1024 + j2 * 128;
    c2.wos = 128 * q * 1024 + j2 * 128;
    c2.ld = ldsaddr(q * 2048);
  }
  f32x16 ac[4] = {};
  bf16x8 hw[HW][2], wf[2][2];
  STAMP(6);
  lds_spin(L_PF + 4 * q, 12);
  lds_spin(L_CNT + 4 * C_HR, 1);
  STAMP(7);
#pragma unroll
  for (int n = 0; n < HW; ++n) { hw[n][0] = asm_gload(c2.rws, c2.hv, c2.hs[0] + n * 1024); hw[n][1] = asm_gload(c2.rws, c2.hv, c2.hs[1] + n * 1024); }
  wf[0][0] = ldtr(c2.wa[0], c2.wa[0] + 1024); wf[0][1] = ldtr(c2.wa[1], c2.wa[1] + 1024);
  p2a_step<0>(c2, ac, hw, wf, q, c2.wa[0], c2.wa[1]);
  STAMP(8);
  red_send(ac, q, lane, 0);
  arrive(C_RG, lane);
  u32x4 xr[2];
#pragma unroll
  for (int u = 0; u < 2; ++u) xr[u] = *(const GAS u32x4*)((const char*)p.x + ooff + 32 * u);
  lds_spin(L_CNT + 4 * C_RG, 4);
  red_recv(ac[0], q, lane, 0);
#pragma unroll
  for (int g = 0; g < 4; ++g) {
    f32x4 v;
#pragma unroll
    for (int e = 0; e < 4; ++e) { const float gp = ac[0][4 * g + e] * r2; v[e] = gp * rcp(1.0f + ex2(-LOG2E * gp)); }
    *(LAS f32x4*)(lds + (6 + (g >> 1)) * 8192 + q * 2048 + (g & 1) * 1024 + lane * 16) = v;
  }
#pragma unroll
  for (int u = 0; u < 2; ++u) *(LAS u32x4*)(lds + 8 * 8192 + q * 2048 + u * 1024 + lane * 16) = xr[u];
  const u32 os0 = WS_O + ((2 * i2 + rb) * 32 + 8 * q) * 1024, os1 = WS_O + ((2 * i2 + (rb ^ 1)) * 32 + 8 * q) * 1024;
  lds_spin(L_CNT + 4 * C_OR, 1);
  STAMP(9);
  bf16x8 ow[8][2];
#pragma unroll
  for (int n = 0; n < 8; ++n) { ow[n][0] = asm_gload(c2.rws, c2.hv, os0 + n * 1024); ow[n][1] = asm_gload(c2.rws, c2.hv, os1 + n * 1024); }
#pragma unroll
  for (int l = 0; l < 4; ++l) ac[l] = f32x16{};
  p2b_step<0>(ac, ow, opq(c2.wa[0] + 10 * 8192), opq(c2.wa[1] + 10 * 8192));
  red_send(ac, q, lane, 10);
  arrive(C_RS, lane);
  lds_spin(L_CNT + 4 * C_RS, 4);
  red_recv(ac[0], q, lane, 10);
  const f32x16 as = ac[0];
  float G[16];
#pragma unroll
  for (int g = 0; g < 4; ++g) {
    const f32x4 v = ldf4((6 + (g >> 1)) * 8192 + q * 2048 + (g & 1) * 1024 + lane * 16);
    G[4 * g] = v[0]; G[4 * g + 1] = v[1]; G[4 * g + 2] = v[2]; G[4 * g + 3] = v[3];
  }
#pragma unroll
  for (int u = 0; u < 2; ++u) xr[u] = *(LAS u32x4*)(lds + 8 * 8192 + q * 2048 + u * 1024 + lane * 16);
  STAMP(10);
#pragma unroll
  for (int u = 0; u < 2; ++u) {
    u32x2 ra = __builtin_amdgcn_permlane32_swap(xr[u][0], xr[u][2], false, false);
    u32x2 rb_ = __builtin_amdgcn_permlane32_swap(xr[u][1], xr[u][3], false, false);
    u32 pk[2][2];
#pragma unroll
    for (int g = 0; g < 2; ++g) {
      const u32 x01 = ra[g], x23 = rb_[g];
      const int e = 8 * u + 4 * g;
      const float y0 = fmaf(as[e], G[e], u2f(x01 << 16)), y1 = fmaf(as[e + 1], G[e + 1], u2f(x01 & 0xffff0000u));
      const float y2 = fmaf(as[e + 2], G[e + 2], u2f(x23 << 16)), y3 = fmaf(as[e + 3], G[e + 3], u2f(x23 & 0xffff0000u));
      pk[g][0] = pk2(y0, y1); pk[g][1] = pk2(y2, y3);
    }
    u32x2 s0 = __builtin_amdgcn_permlane32_swap(pk[0][0], pk[1][0], false, false);
    u32x2 s1 = __builtin_amdgcn_permlane32_swap(pk[0][1], pk[1][1], false, false);
    u32x4 v = {s0[0], s1[0], s0[1], s1[1]};
    *(GAS u32x4*)((char*)p.out + ooff + 32 * u) = v;
  }
  STAMP(11);
  STAMP_DUMP;
}

DI void roleB_kv(const Params& p, int w, int lane, int i, int j, u64 pc) {
  STAMP_DECL;
  const int rg = w - 4, pr = j >> 1, odd = rg & 1;
  const int s = lane & 31, h = lane >> 5;
  bf16x8 a0[2], a1[2];
  load_wphi(p, 4 * pr + 2 * odd, lane, a0[0], a1[0]);
  load_wphi(p, 4 * pr + 2 - 2 * odd, lane, a0[1], a1[1]);
  const int tg[4] = {odd, 1 - odd, 2 + odd, 3 - odd};
  const rsrc_t rws = mkrs(p.ws, WS_END);
  const u64 tag = compute_prologue(p, rws, w, lane, i, j, pc);
  P1<4> ad; ad.init(rg, lane, tg);
  f32x16 acc[4] = {};
  f32x4 cs0 = {}, cs2 = {};
  u32x4 sg;
  STAMP(0);
  p1_main(ad, acc, cs0, cs2, lane, rws, 128 * i + 32 * rg, sg);
  STAMP(1);
  p1_finish(cs0, cs2, tg[0], tg[2], true, lane);
  STAMP(2);
  if (PRIO >= 1) __builtin_amdgcn_s_setprio(1);
  float nmu, r;
  stat_get(rws, 128 * i + 32 * rg, lane, tag, sg, nmu, r);
  const float rl = r * LOG2E;
  const u32 ib = (32 * rg + s) * 64 + ((h ^ ((s >> 1) & 7)) * 8);
  const u32 ia[4] = {ib, ib ^ 16, ib ^ 32, ib ^ 48};
#pragma unroll
  for (int t = 0; t < 2; ++t) {
    float c16[16];
    cs_get(c16, tg[t], lane);
    const f32x16 pl = phi_logits(acc[t], c16, nmu, a0[t], a1[t]);
#pragma unroll
    for (int hh = 0; hh < 2; ++hh) {
      float ep_[8], en_[8], zp, zn;
      phi_exp(pl, hh, rl, ep_, en_, zp, zn);
      const float ip = rcp(zp), in = rcp(zn);
      const u32 base = L_PHI + (2 * tg[t] + hh) * 8192;
#pragma unroll
      for (int g = 0; g < 2; ++g) {
        u32x2 vp = {pk2(ep_[4 * g] * ip, ep_[4 * g + 1] * ip), pk2(ep_[4 * g + 2] * ip, ep_[4 * g + 3] * ip)};
        u32x2 vn = {pk2(en_[4 * g] * in, en_[4 * g + 1] * in), pk2(en_[4 * g + 2] * in, en_[4 * g + 3] * in)};
        *(LAS u32x2*)(lds + base + ia[g]) = vp;
        *(LAS u32x2*)(lds + base + ia[2 + g]) = vn;
      }
    }
  }
#pragma unroll
  for (int t = 0; t < 2; ++t) {
    float c16[16];
    cs_get(c16, tg[2 + t], lane);
    const u32 base = L_V + (tg[2 + t] - 2) * 8192;
#pragma unroll
    for (int g = 0; g < 4; ++g) {
      float v[4];
#pragma unroll
      for (int e = 0; e < 4; ++e) v[e] = fmaf(nmu, c16[4 * g + e], acc[2 + t][4 * g + e]) * r;
      u32x2 vv = {pk2(v[0], v[1]), pk2(v[2], v[3])};
      *(LAS u32x2*)(lds + base + ia[g]) = vv;
    }
  }
  arrive(C_PHI, lane);
  STAMP(3);
  lds_spin(L_CNT + 4 * C_PHI, 4);
  {
    const int blk = (lane >> 4) & 1, q = (lane & 15) >> 2, pp = lane & 3;
    const int r0 = 8 * h + q, r1 = r0 + 4;
    const u32 pa0 = L_PHI + rg * 8192 + r0 * 64 + (((4 * blk + pp) ^ ((r0 >> 1) & 7)) * 8);
    const u32 pa1 = L_PHI + rg * 8192 + r1 * 64 + (((4 * blk + pp) ^ ((r1 >> 1) & 7)) * 8);
    const u32 vb = L_V + (rg >> 1) * 8192;
    const u32 pv0 = vb + r0 * 64 + (((4 * (rg & 1) + pp) ^ ((r0 >> 1) & 7)) * 8);
    const u32 pv1 = vb + r1 * 64 + (((4 * (rg & 1) + pp) ^ ((r1 >> 1) & 7)) * 8);
    const bf16x8 ones = asfrag(0x3f803f80u, 0x3f803f80u, 0x3f803f80u, 0x3f803f80u);
    f32x16 d = {};
#pragma unroll
    for (int u = 0; u < 8; ++u) {
      const bf16x8 vv = ldtr(pv0 + u * 1024, pv1 + u * 1024);
      d = mfma32(ldtr(pa0 + u * 1024, pa1 + u * 1024), blk ? ones : vv, d);
    }
    asm volatile("" :: "v"(d));
    arrive(C_IMG, lane);
    if (s <= 16) {
#pragma unroll
      for (int u = 0; u < 2; ++u) {
        u32x4 v = {pk2(d[8 * u], d[8 * u + 1]), pk2(d[8 * u + 2], d[8 * u + 3]), pk2(d[8 * u + 4], d[8 * u + 5]), pk2(d[8 * u + 6], d[8 * u + 7])};
        st128_sc1(rws, v, (2 * s + h) * 16, WS_KV + (((pr * 16 + i) * 4 + rg) * 2 + u) * 544);
      }
    }
  }
  STAMP(4);
  wait_vm0();
  if (arrive(C_KV, lane) == 3) set_flag(p.ws, WS_FK + (pr * 16 + i) * 128, tag, lane);
  STAMP(5);
  STAMP_DUMP;
}

DI void roleB_q(const Params& p, int w, int lane, int i, int j, u64 pc) {
  STAMP_DECL;
  const int rg = w - 4, pr = j >> 1, odd = rg & 1;
  const int s = lane & 31, h = lane >> 5;
  bf16x8 a0[2], a1[2];
  load_wphi(p, 4 * pr + 2 * odd, lane, a0[0], a1[0]);
  load_wphi(p, 4 * pr + 2 - 2 * odd, lane, a0[1], a1[1]);
  const int tg[3] = {odd, 1 - odd, 7};
  const rsrc_t rws = mkrs(p.ws, WS_END);
  const u64 tag = compute_prologue(p, rws, w, lane, i, j, pc);
  P1<3> ad; ad.init(rg, lane, tg);
  f32x16 acc[3] = {};
  f32x4 cs0 = {}, cs2 = {};
  u32x4 sg;
  STAMP(0);
  p1_main(ad, acc, cs0, cs2, lane, rws, 128 * i + 32 * rg, sg);
  STAMP(1);
  p1_finish(cs0, cs2, tg[0], tg[2], !odd, lane);
  STAMP(2);
  float nmu, r;
  stat_get(rws, 128 * i + 32 * rg, lane, tag, sg, nmu, r);
  const float rl = r * LOG2E;
  {
    float c16[16];
    u32x4 o[2];
    cs_get(c16, 7, lane);
    silu_tile(acc[2], c16, nmu, -rl, o);
    store_h(rws, i, rg, 2 * h_unit0(pr, 1) + 3, lane, o);
  }
  STAMP(3);
  bf16x8 bq[4][2];
  float zeps[4];
#pragma unroll
  for (int t = 0; t < 2; ++t) {
    float c16[16];
    cs_get(c16, tg[t], lane);
    const f32x16 pl = phi_logits(acc[t], c16, nmu, a0[t], a1[t]);
#pragma unroll
    for (int hh = 0; hh < 2; ++hh) {
      float ep_[8], en_[8], zp, zn;
      phi_exp(pl, hh, rl, ep_, en_, zp, zn);
      const float rho = zp * rcp(zn);
      bq[2 * t + hh][0] = asfrag(pk2(ep_[0], ep_[1]), pk2(ep_[2], ep_[3]), pk2(ep_[4], ep_[5]), pk2(ep_[6], ep_[7]));
      bq[2 * t + hh][1] = asfrag(pk2(en_[0] * rho, en_[1] * rho), pk2(en_[2] * rho, en_[3] * rho), pk2(en_[4] * rho, en_[5] * rho), pk2(en_[6] * rho, en_[7] * rho));
      zeps[2 * t + hh] = zp * 1e-6f;
    }
    if (t == 0) {
      wait_vm0();
      if (arrive(C_H, lane) == 7) set_flag(p.ws, WS_FH + (i * 16 + j) * 128, tag, lane);
    }
  }
  STAMP(4);
  poll16(p.ws, WS_FK + pr * 16 * 128, 128, tag, lane, 15);
  STAMP(5);
  {
    u32x4 pv[2][8];
    u32x2 pz[2];
#pragma unroll
    for (int u = 0; u < 2; ++u) {
      const u32 pbase = WS_KV + ((pr * 16 * 4 + rg) * 2 + u) * 544;
#pragma unroll
      for (int m = 0; m < 8; ++m)
        pv[u][m] = __builtin_amdgcn_raw_buffer_load_b128(rws, ((lane >> 5) * 8 * 544) + (lane & 31) * 16, pbase + 2 * m * 8 * 544, 16);
      pz[u] = __builtin_amdgcn_raw_buffer_load_b64(rws, (lane >> 2) * 8 * 544 + 512 + (lane & 3) * 8, pbase, 16);
    }
    u32 ps[4];
#pragma unroll
    for (int e = 0; e < 4; ++e) ps[e] = (s == 2 * e ? 0x00003f80u : 0u) | (s == 2 * e + 1 ? 0x3f800000u : 0u);
    const bf16x8 psel = asfrag(ps[0], ps[1], ps[2], ps[3]);
#pragma unroll
    for (int u = 0; u < 2; ++u) {
      f32x16 sm = {};
#pragma unroll
      for (int m = 0; m < 8; ++m) sm = mfma32(psel, __builtin_bit_cast(bf16x8, pv[u][m]), sm);
      float zz[4] = {u2f(pz[u][0] << 16), u2f(pz[u][0] & 0xffff0000u), u2f(pz[u][1] << 16), u2f(pz[u][1] & 0xffff0000u)};
#pragma unroll
      for (int e = 0; e < 4; ++e) {
        float v = zz[e];
        v += u2f(__builtin_amdgcn_update_dpp(0, (int)f2u(v), 0x128, 0xf, 0xf, false));
        v += u2f(__builtin_amdgcn_update_dpp(0, (int)f2u(v), 0x124, 0xf, 0xf, false));
        u32x2 t = __builtin_amdgcn_permlane16_swap(f2u(v), f2u(v), false, false);
        v = u2f(t[0]) + u2f(t[1]);
        zz[e] = xsum(v);
      }
      const u32 kb = L_KVA + (rg * 2 + u) * 544;
      { u32x2 v = {pk2(sm[0], sm[1]), pk2(sm[2], sm[3])}; *(LAS u32x2*)(lds + kb + s * 16 + h * 8) = v; }
      if (lane < 4) { u32x2 v = {pk2(zz[0], zz[1]), pk2(zz[2], zz[3])}; *(LAS u32x2*)(lds + kb + 512 + lane * 8) = v; }
    }
  }
  arrive(C_CH, lane);
  lds_spin(L_CNT + 4 * C_CH, 4);
  STAMP(6);
#pragma unroll
  for (int hh = 0; hh < 4; ++hh) {
    const u32 ao = (s < 16 ? s : 16) * 32 + h * 16;
    f32x16 d = {};
    const int ah = hh ^ (2 * odd);
    d = mfma32(ld128(L_KVA + (ah * 2) * 544 + ao), bq[hh][0], d);
    d = mfma32(ld128(L_KVA + (ah * 2 + 1) * 544 + ao), bq[hh][1], d);
    const float inv = rcp(d[8] + zeps[hh]);
    u32x4 v = {pk2(d[0] * inv, d[1] * inv), pk2(d[2] * inv, d[3] * inv), pk2(d[4] * inv, d[5] * inv), pk2(d[6] * inv, d[7] * inv)};
    st128_sc1(rws, v, lane * 16, WS_O + ((4 * i + rg) * 32 + 4 * pr + ah) * 1024);
  }
  STAMP(7);
  wait_vm0();
  if (arrive(C_O, lane) == 3) set_flag(p.ws, WS_FO + (i * 8 + pr) * 128, tag, lane);
  STAMP(8);
  STAMP_DUMP;
}

__global__ void __launch_bounds__(768, 3) fused_hedgehog_ffn(Params p) {
  u64 pc;
  asm volatile("s_getpc_b64 %0" : "=s"(pc));
  const int tid = threadIdx.x, lane = tid & 63;
  const int w = __builtin_amdgcn_readfirstlane(tid >> 6);
  const int b = blockIdx.x, g = b & 7, m = b >> 3;
#define MAP 2
  const int i = MAP == 0 ? 4 * (g >> 1) + (m & 3) : MAP == 1 ? 2 * g + (m & 1) : MAP == 2 ? 8 * (g >> 2) + (m & 7) : (b >> 4);
  const int j = MAP == 0 ? 8 * (g & 1) + (m >> 2) : MAP == 1 ? (m >> 1) : MAP == 2 ? 4 * (g & 3) + (m >> 3) : (b & 15);
  if (w == 4) {
    *(LAS u32*)(lds + L_CNT + 4 * (lane & 31)) = 0;
#pragma unroll
    for (int q = 0; q < 4; ++q) *(LAS u32*)(lds + L_CS + 4 * (lane + 64 * q)) = 0;
    wait_lgkm0();
  }
#define MAP2 1
  const int i2 = MAP2 == 0 ? 2 * i + (j & 1) : 8 * (g >> 1) + (m & 7), j2 = MAP2 == 0 ? j >> 1 : 4 * (g & 1) + (m >> 3);
  if (w >= 8) loader_main(p, w, lane, i, j, i2, j2);
  else if (w < 4) roleA_main(p, w, lane, i, j, i2, j2, pc);
  else if (j & 1) roleB_q(p, w, lane, i, j, pc);
  else roleB_kv(p, w, lane, i, j, pc);
}

extern "C" void kernel_launch(void* const* d_in, const int* in_sizes, int n_in, void* d_out, int out_size, void* d_ws, size_t ws_size,
                              hipStream_t stream) {
  Params p;
  p.x = (const u16*)d_in[0]; p.wqkv = (const u16*)d_in[1]; p.wphi = (const u16*)d_in[2]; p.wo = (const u16*)d_in[3];
  p.w1 = (const u16*)d_in[5]; p.w2 = (const u16*)d_in[6]; p.out = (u16*)d_out; p.ws = (char*)d_ws;
  fused_hedgehog_ffn<<<256, 768, 0, stream>>>(p);
}
```

```cpp
#include <hip/hip_runtime.h>

typedef unsigned short u16;
typedef unsigned u32;
typedef unsigned long long u64;
typedef short bf16x8 __attribute__((ext_vector_type(8)));
typedef short s16x4 __attribute__((ext_vector_type(4)));
typedef __bf16 bf2 __attribute__((ext_vector_type(2)));
typedef float f32x16 __attribute__((ext_vector_type(16)));
typedef float f32x4 __attribute__((ext_vector_type(4)));
typedef u32 u32x4 __attribute__((ext_vector_type(4)));
typedef u32 u32x2 __attribute__((ext_vector_type(2)));
#define LAS __attribute__((address_space(3)))
#define GAS __attribute__((address_space(1)))
#define DI __device__ __forceinline__
#define RLX __ATOMIC_RELAXED, __HIP_MEMORY_SCOPE_AGENT
#define RLXW __ATOMIC_RELAXED, __HIP_MEMORY_SCOPE_WORKGROUP

#define PRIO 1
#define TIMING 0

constexpr int S_ = 2048, D_ = 512, F_ = 1792;
constexpr float LOG2E = 1.4426950408889634f;

constexpr u32 WS_E = 0;
constexpr u32 WS_FH = 4096;
constexpr u32 WS_FK = WS_FH + 32768;
constexpr u32 WS_FO = WS_FK + 32768;
constexpr u32 WS_ST = 102400;
constexpr u32 WS_KV = 262144;
constexpr u32 WS_H = 1u << 20;
constexpr u32 WS_O = WS_H + 64u * 112u * 1024u;
constexpr u32 WS_END = WS_O + 64u * 32u * 1024u;
constexpr u32 WS_TM = 16u << 20;

constexpr u32 W1R = 0, NW1 = 5;
constexpr u32 X1R = 81920, NX1 = 4;
constexpr u32 L_PHI = 98304, L_V = 131072;
constexpr u32 L_KVA = 147456;
constexpr u32 MISC = 157696;
constexpr u32 L_CS = MISC + 1024;
constexpr u32 L_CNT = MISC + 2048, L_TAG = MISC;
constexpr u32 L_PF = L_CNT + 64;
enum { C_H = 0, C_PHI = 1, C_KV = 2, C_CH = 3, C_O = 4, C_HR = 5, C_OR = 6, C_IMG = 7, C_RG = 8, C_RS = 9 };
constexpr u32 SPIN_CAP = 1u << 22;

__shared__ __attribute__((aligned(1024))) u32 lds32[40960];
#define lds ((char*)lds32)

DI f32x16 mfma32(bf16x8 a, bf16x8 b, f32x16 c) { return __builtin_amdgcn_mfma_f32_32x32x16_bf16(a, b, c, 0, 0, 0); }
DI u32 pk2(float a, float b) { bf2 v = {(__bf16)a, (__bf16)b}; return __builtin_bit_cast(u32, v); }
DI float dot2(u32 a, u32 b, float c) { return __builtin_amdgcn_fdot2_f32_bf16(__builtin_bit_cast(bf2, a), __builtin_bit_cast(bf2, b), c, false); }
DI float ex2(float x) { return __builtin_amdgcn_exp2f(x); }
DI float rcp(float x) { return __builtin_amdgcn_rcpf(x); }
DI float fmax2(float a, float b) { return __builtin_elementwise_maximum(a, b); }
DI float fmin2(float a, float b) { return __builtin_elementwise_minimum(a, b); }
DI u32 f2u(float x) { return __builtin_bit_cast(u32, x); }
DI float u2f(u32 x) { return __builtin_bit_cast(float, x); }
DI bf16x8 ld128(u32 off) { return *(LAS bf16x8*)(lds + off); }
DI f32x4 ldf4(u32 off) { return *(LAS f32x4*)(lds + off); }
DI float ldf(u32 off) { return *(LAS float*)(lds + off); }
DI void stf(u32 off, float v) { *(LAS float*)(lds + off) = v; }
DI bf16x8 ldtr(u32 off0, u32 off1) {
  s16x4 a = __builtin_amdgcn_ds_read_tr16_b64_v4i16((LAS s16x4*)(lds + off0));
  s16x4 b = __builtin_amdgcn_ds_read_tr16_b64_v4i16((LAS s16x4*)(lds + off1));
  return __builtin_shufflevector(a, b, 0, 1, 2, 3, 4, 5, 6, 7);
}
DI bf16x8 asfrag(u32 a, u32 b, u32 c, u32 d) { u32x4 v = {a, b, c, d}; return __builtin_bit_cast(bf16x8, v); }
DI void barrier() { asm volatile("s_barrier" ::: "memory"); }
DI void wait_lgkm0() { asm volatile("s_waitcnt lgkmcnt(0)" ::: "memory"); }
DI void wait_vm0() { asm volatile("s_waitcnt vmcnt(0)" ::: "memory"); }
template <int N> DI void wait_vm() { asm volatile("s_waitcnt vmcnt(%0)" ::"n"(N) : "memory"); }
DI float xsum(float x) { u32x2 r = __builtin_amdgcn_permlane32_swap(f2u(x), f2u(x), false, false); return u2f(r[0]) + u2f(r[1]); }
DI void xmaxmin(float& mx, float& mn) {
  u32x2 r = __builtin_amdgcn_permlane32_swap(f2u(mx), f2u(-mn), false, false);
  float m = fmax2(u2f(r[0]), u2f(r[1]));
  u32x2 q = __builtin_amdgcn_permlane32_swap(f2u(m), f2u(m), false, false);
  mx = u2f(q[0]); mn = -u2f(q[1]);
}
DI void xsum2(float& a, float& b) {
  u32x2 r = __builtin_amdgcn_permlane32_swap(f2u(a), f2u(b), false, false);
  float m = u2f(r[0]) + u2f(r[1]);
  u32x2 q = __builtin_amdgcn_permlane32_swap(f2u(m), f2u(m), false, false);
  a = u2f(q[0]); b = u2f(q[1]);
}
DI void lds_st(u32 off, u32 v) { __hip_atomic_store((LAS u32*)&lds32[off >> 2], v, RLXW); }
DI u32 ldsaddr(u32 off) { return (u32)(size_t)(LAS char*)lds + off; }
DI u32x4 asm_ld128(u32 off) {
  u32x4 v;
  asm volatile("ds_read_b128 %0, %1\n\ts_waitcnt lgkmcnt(0)" : "=&v"(v) : "v"(ldsaddr(off)) : "memory");
  return v;
}
DI void asm_st32(u32 off, u32 val) { asm volatile("ds_write_b32 %0, %1" ::"v"(ldsaddr(off)), "v"(val) : "memory"); }
DI u32 asm_ld32(u32 off) {
  u32 v;
  asm volatile("ds_read_b32 %0, %1\n\ts_waitcnt lgkmcnt(0)" : "=&v"(v) : "v"(ldsaddr(off)) : "memory");
  return v;
}
DI void lds_spin(u32 off, u32 target) {
#pragma unroll 1
  for (u32 it = 0; it < SPIN_CAP; ++it) {
    if (__builtin_amdgcn_readfirstlane(asm_ld32(off)) >= target) break;
    __builtin_amdgcn_s_sleep(1);
  }
}
DI u32 lds_add_rtn(u32 off) { return __hip_atomic_fetch_add((LAS u32*)&lds32[off >> 2], 1u, RLXW); }
DI u32 lds_ld(u32 off) { return __hip_atomic_load((LAS u32*)&lds32[off >> 2], RLXW); }
DI u32 arrive(int cnt, int lane) {
  asm volatile("" ::: "memory");
  u32 old = 0;
  if (lane == 0) old = lds_add_rtn(L_CNT + 4 * cnt);
  old = __builtin_amdgcn_readfirstlane(old);
  asm volatile("" ::: "memory");
  return old;
}
DI void poll16(const char* ws, u32 base, u32 stride, u64 tag, int lane, int mask) {
  GAS u64* p = (GAS u64*)(ws + base + (u32)(lane & mask) * stride);
#pragma unroll 1
  for (u32 it = 0; it < SPIN_CAP; ++it) {
    u64 v = __hip_atomic_load(p, RLX);
    if (__all(v == tag)) break;
    __builtin_amdgcn_s_sleep(1);
  }
  asm volatile("" ::: "memory");
}
DI void set_flag(char* ws, u32 off, u64 tag, int lane) {
  if (lane == 0) __hip_atomic_store((GAS u64*)(ws + off), tag, RLX);
}

#define STAMP(k) do { } while (0)
#define STAMP_DECL do { } while (0)
#define STAMP_DUMP do { } while (0)

struct Params { const u16* x; const u16* wqkv; const u16* wphi; const u16* wo; const u16* w1; const u16* w2; u16* out; char* ws; };

typedef __amdgpu_buffer_rsrc_t rsrc_t;
DI rsrc_t mkrs(const void* p, u32 bytes) { return __builtin_amdgcn_make_buffer_rsrc((void*)p, 0, bytes, 0x00020000); }
DI void st128_sc1(rsrc_t rs, u32x4 v, u32 voff, u32 soff) { __builtin_amdgcn_raw_buffer_store_b128(v, rs, voff + soff, 0, 16); }
template <int AUX> DI void dma16(rsrc_t rs, u32 ldsoff, u32 voff, u32 soff) {
  __builtin_amdgcn_raw_ptr_buffer_load_lds(rs, (LAS void*)(lds + ldsoff), 16, voff, soff, 0, AUX);
}

DI int h_unit0(int p, int isq) { return isq ? ((p & 1) ? (7 * p + 3) >> 1 : (7 * p) >> 1) : ((p & 1) ? (7 * p - 1) >> 1 : (7 * p + 4) >> 1); }

DI void loader_main(const Params& p, int w, int lane, int i, int j, int i2, int j2) {
  STAMP_DECL;
  const int wl = w - 8, pr = j >> 1, isq = j & 1;
  const rsrc_t rx = mkrs(p.x, S_ * D_ * 2), rq = mkrs(p.wqkv, D_ * 1536 * 2), r1 = mkrs(p.w1, D_ * F_ * 2);
  const u32 gsw = ((((lane & 7) >> 2) ^ ((lane >> 4) & 1)) * 64) + (lane & 3) * 16;
  const u32 xv = (lane >> 3) * 1024 + (((lane & 7) ^ ((4 * (wl & 1) + (lane >> 4)) & 7)) * 16);
  const u32 qv = (lane >> 3) * 3072 + gsw, hv1 = (lane >> 3) * 3584 + gsw;
  const u32 xs = (128 * i + 8 * wl) * 1024;
  const u32 s0 = (isq ? pr * 128 : 1024 + pr * 128) + 8 * wl * 3072, s1 = 2048 + pr * 128 + 8 * wl * 3072;
  const u32 h0 = h_unit0(pr, isq) * 128 + 8 * wl * 3584;
  auto issue_w = [&](int c, u32 slot) {
    const u32 d = slot + wl * 1024;
    dma16<0>(rq, d, qv, s0 + c * (32 * 3072));
    if (!isq) dma16<0>(rq, d + 4096, qv, s1 + c * (32 * 3072));
    dma16<0>(r1, d + 8192, hv1, h0 + c * (32 * 3584));
    dma16<0>(r1, d + 12288, hv1, h0 + 128 + c * (32 * 3584));
  };
  auto issue_x = [&](int P, int half) {
    const u32 d = X1R + (P & 3) * 16384 + (wl + 8 * half) * 1024;
    dma16<0>(rx, d, xv, xs + half * 65536 + P * 128);
    dma16<0>(rx, d + 4096, xv, xs + half * 65536 + 32768 + P * 128);
  };
  STAMP(0);
  issue_x(0, 0); issue_x(0, 1);
  issue_w(0, 0); issue_w(1, 16384);
  issue_x(1, 0); issue_x(1, 1);
  issue_w(2, 2 * 16384);
  STAMP(1);
  if (isq) wait_vm<7>(); else wait_vm<8>();
  barrier();
  issue_w(3, 3 * 16384);
  issue_x(2, 0); issue_x(2, 1);
  issue_w(4, 4 * 16384);
  if (isq) wait_vm<10>(); else wait_vm<12>();
  barrier();
  issue_w(5, 0);
  issue_x(3, 0); issue_x(3, 1);
  u32 wslot = 16384;
#pragma unroll 1
  for (int c = 2; c < 16; ++c) {
    if (isq) {
      if (c <= 11) wait_vm<10>(); else if (c == 12) wait_vm<6>(); else if (c == 13) wait_vm<3>(); else wait_vm<0>();
    } else {
      if (c <= 11) wait_vm<12>(); else if (c == 12) wait_vm<8>(); else if (c == 13) wait_vm<4>(); else wait_vm<0>();
    }
    barrier();
    if (c <= 11) issue_w(c + 4, wslot);
    if (c <= 9) issue_x((c + 6) >> 1, c & 1);
    wslot = wslot == 4 * 16384 ? 0 : wslot + 16384;
  }
  STAMP(2);
  barrier();
  STAMP(3);
  const rsrc_t rw2 = mkrs(p.w2, F_ * D_ * 2);
  const u32 wv = (lane >> 3) * 1024 + gsw, wsrc = 16 * 28 * wl * 1024 + j2 * 128;
  auto issue_w2 = [&](int n) {
    dma16<0>(rw2, n * 8192 + wl * 2048, wv, wsrc + n * 16384);
    dma16<0>(rw2, n * 8192 + wl * 2048 + 1024, wv, wsrc + n * 16384 + 8192);
  };
#pragma unroll 1
  for (int n = 0; n < 12; ++n) issue_w2(n);
  wait_vm0();
  asm_st32(L_PF + 4 * wl, 12);
  if (!isq) lds_spin(L_CNT + 4 * C_IMG, 4);
#pragma unroll 1
  for (int n = 12; n < 18; ++n) issue_w2(n);
  wait_vm0();
  asm_st32(L_PF + 4 * wl, 18);
  STAMP(4);
  if (wl == 0) {
    const u32x4 tg = asm_ld128(L_TAG);
    const u64 tag = (u64)tg[0] | ((u64)tg[1] << 32);
    poll16(p.ws, WS_FH + (i2 >> 1) * 16 * 128, 128, tag, lane, 15);
    asm_st32(L_CNT + 4 * C_HR, 1);
    STAMP(5);
    poll16(p.ws, WS_FO + (i2 >> 1) * 8 * 128, 128, tag, lane, 7);
    asm_st32(L_CNT + 4 * C_OR, 1);
    STAMP(6);
    if (blockIdx.x == 0) {
      poll16(p.ws, WS_FO, 128, tag, lane, 63);
      poll16(p.ws, WS_FO + 64 * 128, 128, tag, lane, 63);
      if (lane == 0) __hip_atomic_store((GAS u64*)(p.ws + WS_E), tag, RLX);
    }
  }
  STAMP_DUMP;
}

template <int NT> struct Frag { bf16x8 x; bf16x8 w[NT]; };

template <int NT> struct P1 {
  u32 xa[4];
  u32 wa[NT];
  DI void init(int rg, int lane, const int (&tg)[NT]) {
    const int s = lane & 31, h = lane >> 5, blk = (lane >> 4) & 1, q = (lane & 15) >> 2, pp = lane & 3, hi = rg >> 1;
#pragma unroll
    for (int k = 0; k < 4; ++k) xa[k] = X1R + (((32 * rg + s) * 128 + ((h ^ ((s >> 1) & 7)) * 16)) ^ ((k ^ hi) << 5));
#pragma unroll
    for (int t = 0; t < NT; ++t)
      wa[t] = W1R + (tg[t] >> 1) * 4096 + hi * 2048 + (8 * h + q) * 128 + (((tg[t] & 1) ^ (q >> 1)) * 64) + 32 * blk + 8 * pp;
  }
};
#define SB() __builtin_amdgcn_sched_barrier(0)
template <int NT> struct Addr { u32 w[NT]; };
template <int NT> DI Addr<NT> waddr(const P1<NT>& a, u32 slot) {
  Addr<NT> r;
#pragma unroll
  for (int t = 0; t < NT; ++t) r.w[t] = a.wa[t] + slot;
  return r;
}

template <int NT, bool CS, bool RELOAD>
DI void p1_step(Frag<NT>& f, f32x16 (&acc)[NT], bf16x8 sel, f32x4& cs0, f32x4& cs2, const Addr<NT>& wa, u32 xa, int ks) {
#pragma unroll
  for (int t = 0; t < NT; ++t) {
    acc[t] = mfma32(f.w[t], f.x, acc[t]);
    if constexpr (CS) {
      if (t == 0) cs0 = __builtin_amdgcn_mfma_f32_16x16x32_bf16(sel, f.w[0], cs0, 0, 0, 0);
      if (t == 2) cs2 = __builtin_amdgcn_mfma_f32_16x16x32_bf16(sel, f.w[2], cs2, 0, 0, 0);
    }
    SB();
    if constexpr (RELOAD) {
      const u32 ad = wa.w[t] ^ (ks << 11);
      f.w[t] = ldtr(ad, ad + 512);
      if (t == NT - 1) f.x = ld128(xa);
    }
    SB();
  }
}
template <int NT, bool LAST>
DI void p1_pair(const P1<NT>& a, Frag<NT>& f0, Frag<NT>& f1, f32x16 (&acc)[NT], bf16x8 sel, f32x4& cs0, f32x4& cs2, u32 w1, u32 w2, u32 xsl, u32 xn) {
  {
    const Addr<NT> wa = waddr(a, w1);
    p1_step<NT, true, true>(f0, acc, sel, cs0, cs2, wa, a.xa[2] + xsl, 0);
    p1_step<NT, false, true>(f1, acc, sel, cs0, cs2, wa, a.xa[3] + xsl, 1);
  }
  barrier();
  {
    const Addr<NT> wa = waddr(a, w2);
    p1_step<NT, true, !LAST>(f0, acc, sel, cs0, cs2, wa, a.xa[0] + xn, 0);
    p1_step<NT, false, !LAST>(f1, acc, sel, cs0, cs2, wa, a.xa[1] + xn, 1);
  }
  if constexpr (!LAST) barrier();
}
struct Stat { u32x4 g; };
DI u32x4 stat_load(rsrc_t rws, int row0, int lane) { return __builtin_amdgcn_raw_buffer_load_b128(rws, (lane & 31) * 16, WS_ST + row0 * 16, 16); }
DI void stat_get(rsrc_t rws, int row0, int lane, u64 tag, u32x4 g, float& nmu, float& r) {
#pragma unroll 1
  for (u32 it = 0; it < SPIN_CAP; ++it) {
    if (__all(g[1] == (u32)tag && g[3] == (u32)(tag >> 32))) break;
    __builtin_amdgcn_s_sleep(1);
    g = stat_load(rws, row0, lane);
  }
  nmu = -u2f(g[0]); r = u2f(g[2]);
}
DI void stat_publish(const Params& p, rsrc_t rws, int row, int lane, u64 tag, u32x4 xv) {
  float s0 = 0.f, s1 = 0.f, q0 = 0.f, q1 = 0.f;
#pragma unroll
  for (int e = 0; e < 4; ++e) {
    const float lo = u2f(xv[e] << 16), hi = u2f(xv[e] & 0xffff0000u);
    s0 += lo; s1 += hi; q0 = fmaf(lo, lo, q0); q1 = fmaf(hi, hi, q1);
  }
  float s = s0 + s1, q = q0 + q1;
#define DPP_ADD(v, ctrl) v += u2f(__builtin_amdgcn_update_dpp(0, (int)f2u(v), ctrl, 0xf, 0xf, false))
  DPP_ADD(s, 0x128); DPP_ADD(q, 0x128); DPP_ADD(s, 0x124); DPP_ADD(q, 0x124);
  DPP_ADD(s, 0x122); DPP_ADD(q, 0x122); DPP_ADD(s, 0x121); DPP_ADD(q, 0x121);
#undef DPP_ADD
  { u32x2 a = __builtin_amdgcn_permlane16_swap(f2u(s), f2u(q), false, false);
    float m = u2f(a[0]) + u2f(a[1]);
    u32x2 b = __builtin_amdgcn_permlane16_swap(f2u(m), f2u(m), false, false);
    s = u2f(b[0]); q = u2f(b[1]); }
  xsum2(s, q);
  const float mu = s * (1.0f / D_);
  const float var = fmaf(-mu, mu, q * (1.0f / D_));
  const float r = __builtin_amdgcn_rsqf(fmax2(var, 0.f) + 1e-5f);
  if (lane == 0) {
    const u32x4 g = {f2u(mu), (u32)tag, f2u(r), (u32)(tag >> 32)};
    st128_sc1(rws, g, 0, WS_ST + row * 16);
  }
}
template <int NT> DI void p1_main(const P1<NT>& a, f32x16 (&acc)[NT], f32x4& cs0, f32x4& cs2, int lane, rsrc_t rws, int row0, u32x4& sg) {
  Frag<NT> f0, f1;
  const u32 one = ((lane & 15) == ((lane >> 4) & 1)) ? 0x3f803f80u : 0u;
  const bf16x8 sel = asfrag(one, one, one, one);
#pragma unroll
  for (int t = 0; t < NT; ++t) asm volatile("" : "+v"(acc[t]));
  barrier();
#pragma unroll
  for (int t = 0; t < NT; ++t) f0.w[t] = ldtr(a.wa[t], a.wa[t] + 512);
  f0.x = ld128(a.xa[0]);
  SB();
#pragma unroll
  for (int t = 0; t < NT; ++t) f1.w[t] = ldtr(a.wa[t] ^ 2048, (a.wa[t] ^ 2048) + 512);
  f1.x = ld128(a.xa[1]);
  SB();
  u32 wsl = 0, xsl = 0;
#pragma unroll 1
  for (int cp = 0; cp < 7; ++cp) {
    const u32 w1 = wsl == 4 * 16384 ? 0 : wsl + 16384;
    const u32 w2 = w1 == 4 * 16384 ? 0 : w1 + 16384;
    const u32 xn = (xsl + 16384) & 65535;
    p1_pair<NT, false>(a, f0, f1, acc, sel, cs0, cs2, w1, w2, xsl, xn);
    wsl = w2; xsl = xn;
  }
  sg = stat_load(rws, row0, lane);
  p1_pair<NT, true>(a, f0, f1, acc, sel, cs0, cs2, (15 % NW1) * 16384, 0, xsl, 0);
}

DI void p1_finish(f32x4 cs0, f32x4 cs2, int g0, int g2, bool add2, int lane) {
  if (lane < 16) {
    __builtin_amdgcn_ds_faddf((LAS float*)&lds32[(L_CS + g0 * 128 + lane * 4) >> 2], cs0[0], 0, 0, false);
    __builtin_amdgcn_ds_faddf((LAS float*)&lds32[(L_CS + g0 * 128 + 64 + lane * 4) >> 2], cs0[1], 0, 0, false);
    if (add2) {
      __builtin_amdgcn_ds_faddf((LAS float*)&lds32[(L_CS + g2 * 128 + lane * 4) >> 2], cs2[0], 0, 0, false);
      __builtin_amdgcn_ds_faddf((LAS float*)&lds32[(L_CS + g2 * 128 + 64 + lane * 4) >> 2], cs2[1], 0, 0, false);
    }
  }
  wait_lgkm0();
  barrier();
}
DI void cs_get(float (&c)[16], int gt, int lane) {
  const int h = lane >> 5;
#pragma unroll
  for (int g = 0; g < 4; ++g) {
    f32x4 v = ldf4(L_CS + gt * 128 + (8 * g + 4 * h) * 4);
    c[4 * g] = v[0]; c[4 * g + 1] = v[1]; c[4 * g + 2] = v[2]; c[4 * g + 3] = v[3];
  }
}

DI void silu_tile(const f32x16& acc, const float (&cs)[16], float nmu, float nrl, u32x4 (&o)[2]) {
  float y[16];
#pragma unroll
  for (int e = 0; e < 16; ++e) {
    const float t = fmaf(nmu, cs[e], acc[e]);
    y[e] = t * rcp(1.0f + ex2(t * nrl));
  }
#pragma unroll
  for (int u = 0; u < 2; ++u)
#pragma unroll
    for (int e = 0; e < 4; ++e) o[u][e] = pk2(y[8 * u + 2 * e], y[8 * u + 2 * e + 1]);
}

DI f32x16 phi_logits(const f32x16& acc, const float (&cs)[16], float nmu, bf16x8 a0, bf16x8 a1) {
  float t[16];
#pragma unroll
  for (int e = 0; e < 16; ++e) t[e] = fmaf(nmu, cs[e], acc[e]);
  bf16x8 b0 = asfrag(pk2(t[0], t[1]), pk2(t[2], t[3]), pk2(t[4], t[5]), pk2(t[6], t[7]));
  bf16x8 b1 = asfrag(pk2(t[8], t[9]), pk2(t[10], t[11]), pk2(t[12], t[13]), pk2(t[14], t[15]));
  f32x16 z = {};
  z = mfma32(a0, b0, z);
  z = mfma32(a1, b1, z);
  return z;
}
DI void phi_exp(const f32x16& pl, int hh, float rl, float (&ep)[8], float (&en)[8], float& zp, float& zn) {
  const int o = 8 * hh;
  float mx = fmax2(fmax2(fmax2(fmax2(fmax2(pl[o], pl[o + 1]), pl[o + 2]), fmax2(fmax2(pl[o + 3], pl[o + 4]), pl[o + 5])), pl[o + 6]), pl[o + 7]);
  float mn = fmin2(fmin2(fmin2(fmin2(fmin2(pl[o], pl[o + 1]), pl[o + 2]), fmin2(fmin2(pl[o + 3], pl[o + 4]), pl[o + 5])), pl[o + 6]), pl[o + 7]);
  xmaxmin(mx, mn);
  const float a = -rl * mx, b = rl * mn;
  zp = 0.f; zn = 0.f;
#pragma unroll
  for (int e = 0; e < 8; ++e) {
    ep[e] = ex2(fmaf(pl[8 * hh + e], rl, a));
    en[e] = ex2(fmaf(pl[8 * hh + e], -rl, b));
    zp += ep[e]; zn += en[e];
  }
  xsum2(zp, zn);
}

DI void load_wphi(const Params& p, int head0, int lane, bf16x8& a0, bf16x8& a1) {
  const int row = lane & 31, h = lane >> 5;
  const u16* wp = p.wphi + (head0 + (row >> 4)) * 256 + (row & 15);
  u32 v[4];
#pragma unroll
  for (int e = 0; e < 4; ++e) {
    const int j0 = 2 * e, j1 = 2 * e + 1;
    const int d0 = 8 * (j0 >> 2) + 4 * h + (j0 & 3), d1 = 8 * (j1 >> 2) + 4 * h + (j1 & 3);
    v[e] = (u32)wp[d0 * 16] | ((u32)wp[d1 * 16] << 16);
  }
  const bool lo = row < 16;
  a0 = asfrag(lo ? v[0] : 0u, lo ? v[1] : 0u, lo ? v[2] : 0u, lo ? v[3] : 0u);
  a1 = asfrag(lo ? 0u : v[0], lo ? 0u : v[1], lo ? 0u : v[2], lo ? 0u : v[3]);
}

DI void store_h(rsrc_t rws, int i, int rg, int tile, int lane, const u32x4 (&o)[2]) {
#pragma unroll
  for (int u = 0; u < 2; ++u)
    st128_sc1(rws, o[u], lane * 16, WS_H + ((4 * i + rg) * 112 + 2 * tile + u) * 1024);
}

#define HWIN 7
constexpr int HW = HWIN;
DI u32x4 srd(const void* ptr, u32 bytes) {
  const u64 a = (u64)ptr;
  u32x4 d = {(u32)a, (u32)(a >> 32) & 0xffffu, bytes, 0x00020000u};
  d[0] = __builtin_amdgcn_readfirstlane(d[0]); d[1] = __builtin_amdgcn_readfirstlane(d[1]);
  return d;
}
DI bf16x8 asm_gload(u32x4 rs, u32 voff, u32 soff) {
  bf16x8 v;
  asm volatile("buffer_load_dwordx4 %0, %1, %2, %3 offen sc1" : "=v"(v) : "v"(voff), "s"(rs), "s"(soff) : "memory");
  return v;
}
DI void asm_dma(u32x4 rs, u32 ldsa, u32 voff, u32 soff) {
  asm volatile("s_mov_b32 m0, %0\n\ts_nop 0\n\tbuffer_load_dwordx4 %1, %2, %3 offen lds" ::"s"(ldsa), "v"(voff), "s"(rs), "s"(soff) : "memory");
}
struct P2 { u32 wa[2], hv, wv, hs[2], w2s, wos, ld; u32x4 rws, rw2, rwo; };
constexpr int p2_ops(int m) { return (m + HW < 28 ? 2 : 0) + (m < 18 ? 2 : 0); }
constexpr int p2_younger(int n) {
  int c = 0;
  if (n < HW) { c = 2 * (HW - 1 - n); for (int m = 0; m < n; ++m) c += p2_ops(m); }
  else { c = (n - HW < 18) ? 2 : 0; for (int m = n - HW + 1; m < n; ++m) c += p2_ops(m); }
  return c;
}
DI u32 opq(u32 x) { asm volatile("" : "+v"(x)); return x; }
template <int N> DI void p2a_step(const P2& c, f32x16 (&ac)[4], bf16x8 (&hw)[HW][2], bf16x8 (&wf)[2][2], int q, u32 b0, u32 b1) {
  asm volatile("s_waitcnt vmcnt(%2)" : "+v"(hw[N % HW][0]), "+v"(hw[N % HW][1]) : "n"(p2_younger(N)) : "memory");
  if constexpr (N == 11) lds_spin(L_PF + 4 * q, 18);
  if constexpr (N + 1 < 28) {
    constexpr u32 sl = (N + 1) % 18, o = (sl & 7) * 8192;
    if constexpr ((sl & 7) == 0 || sl == 0) { b0 = opq(c.wa[0] + (sl >> 3) * 65536); b1 = opq(c.wa[1] + (sl >> 3) * 65536); }
    wf[(N + 1) & 1][0] = ldtr(b0 + o, b0 + o + 1024);
    wf[(N + 1) & 1][1] = ldtr(b1 + o, b1 + o + 1024);
  }
  SB();
  ac[0] = mfma32(wf[N & 1][0], hw[N % HW][0], ac[0]);
  ac[1] = mfma32(wf[N & 1][1], hw[N % HW][0], ac[1]);
  ac[2] = mfma32(wf[N & 1][0], hw[N % HW][1], ac[2]);
  ac[3] = mfma32(wf[N & 1][1], hw[N % HW][1], ac[3]);
  SB();
  if constexpr (N + HW < 28) {
    hw[N % HW][0] = asm_gload(c.rws, c.hv, c.hs[0] + (N + HW) * 1024);
    hw[N % HW][1] = asm_gload(c.rws, c.hv, c.hs[1] + (N + HW) * 1024);
  }
  if constexpr (N < 10) {
    asm_dma(c.rw2, c.ld + N * 8192, c.wv, c.w2s + (N + 18) * 16384);
    asm_dma(c.rw2, c.ld + N * 8192 + 1024, c.wv, c.w2s + (N + 18) * 16384 + 8192);
  } else if constexpr (N < 18) {
    asm_dma(c.rwo, c.ld + N * 8192, c.wv, c.wos + (N - 10) * 16384);
    asm_dma(c.rwo, c.ld + N * 8192 + 1024, c.wv, c.wos + (N - 10) * 16384 + 8192);
  }
  SB();
  if constexpr (N + 1 < 28) p2a_step<N + 1>(c, ac, hw, wf, q, b0, b1);
}
template <int N> DI void p2b_step(f32x16 (&ac)[4], bf16x8 (&ow)[8][2], u32 b0, u32 b1) {
  constexpr u32 o = N * 8192;
  asm volatile("s_waitcnt vmcnt(%2)" : "+v"(ow[N][0]), "+v"(ow[N][1]) : "n"(2 * (7 - N)) : "memory");
  const bf16x8 w0 = ldtr(b0 + o, b0 + o + 1024), w1 = ldtr(b1 + o, b1 + o + 1024);
  SB();
  ac[0] = mfma32(w0, ow[N][0], ac[0]);
  ac[1] = mfma32(w1, ow[N][0], ac[1]);
  ac[2] = mfma32(w0, ow[N][1], ac[2]);
  ac[3] = mfma32(w1, ow[N][1], ac[3]);
  SB();
  if constexpr (N < 7) p2b_step<N + 1>(ac, ow, b0, b1);
}
DI void red_send(const f32x16 (&ac)[4], int q, int lane, int slot0) {
  const u32 b = opq(slot0 * 8192 + q * 2048 + lane * 16);
#pragma unroll
  for (int l = 1; l < 4; ++l)
#pragma unroll
    for (int g = 0; g < 4; ++g) {
      const f32x4 v = {ac[l][4 * g], ac[l][4 * g + 1], ac[l][4 * g + 2], ac[l][4 * g + 3]};
      *(LAS f32x4*)(lds + b + (2 * (l - 1) + (g >> 1)) * 8192 + (g & 1) * 1024) = v;
    }
}
DI void red_recv(f32x16& a, int q, int lane, int slot0) {
#pragma unroll
  for (int l = 1; l < 4; ++l) {
    const u32 b = opq(slot0 * 8192 + (q ^ l) * 2048 + lane * 16);
#pragma unroll
    for (int g = 0; g < 4; ++g) {
      const f32x4 v = ldf4(b + (2 * (l - 1) + (g >> 1)) * 8192 + (g & 1) * 1024);
#pragma unroll
      for (int e = 0; e < 4; ++e) a[4 * g + e] += v[e];
    }
  }
}

#define PF_TLB 65536
#define PF_CODE 250
#define PF_DATA 2
DI u64 compute_prologue(const Params& p, rsrc_t rws, int w, int lane, int i, int j, u64 pc) {
  const int row = 128 * i + 8 * j + w;
  const u32x4 xv = *(const GAS u32x4*)((const char*)p.x + row * 1024 + lane * 16);
  const u64 tag = __hip_atomic_load((GAS u64*)(p.ws + WS_E), RLX) + 1;
  u32 pf0 = 0, pf1 = 0;
  const int b = blockIdx.x;
  if (w == 5) {
    const u32 line = (b >> 3) * 8 + (lane & 7);
    if (lane < 8 && line < PF_CODE) pf0 = *(const volatile GAS u32*)((pc & ~127ull) + line * 128);
  }
  if (w >= 4) {
    const u32 t = (b * 4 + (w - 4)) * 64 + lane;
    const char* a = nullptr;
    if (PF_DATA == 1) {
      const u32 t1 = (b * 4 + (w - 4)) * 18 + lane;
      if (lane < 18) { if (t1 < 14336) a = (const char*)p.w2 + t1 * 128; else a = (const char*)p.wo + (t1 - 14336) * 128; }
    } else {
      if (t < 14336) a = (const char*)p.w2 + t * 128; else if (t < 18432) a = (const char*)p.wo + (t - 14336) * 128;
      else if (t < 32768) a = (const char*)p.w1 + (t - 18432) * 128; else if (t < 45056) a = (const char*)p.wqkv + (t - 32768) * 128;
      else if (t < 61440) a = (const char*)p.x + (t - 45056) * 128;
    }
    if (a) pf1 = *(const volatile GAS u32*)a;
  }
  u32 pf2 = 0;
  if (w == 6) {
    constexpr u32 NWS = (WS_END + PF_TLB - 1) / PF_TLB, NOUT = (S_ * D_ * 2) / PF_TLB;
    const u32 t = (b >> 3) * 64 + lane;
    if (t < NWS) pf2 = *(const volatile GAS u32*)(p.ws + t * PF_TLB + 64);
    else if (t < NWS + NOUT) pf2 = *(const volatile GAS u32*)((const char*)p.out + (t - NWS) * PF_TLB);
  }
  stat_publish(p, rws, row, lane, tag, xv);
  asm volatile("" :: "v"(pf0), "v"(pf1), "v"(pf2));
  return tag;
}

DI void roleA_main(const Params& p, int w, int lane, int i, int j, int i2, int j2, u64 pc) {
  STAMP_DECL;
  const int rg = w, pr = j >> 1, isq = j & 1, odd = rg & 1;
  const int s = lane & 31, h = lane >> 5;
  const rsrc_t rws = mkrs(p.ws, WS_END);
  const u64 tag = compute_prologue(p, rws, w, lane, i, j, pc);
  if (w == 0 && lane == 0) *(LAS u64*)(lds + L_TAG) = tag;
  const int t0 = (!isq && (pr & 1)) ? 5 : 4;
  const int tg[3] = {t0 + odd, t0 + 1 - odd, t0 + 2};
  P1<3> ad; ad.init(rg, lane, tg);
  f32x16 acc[3] = {};
  f32x4 cs0 = {}, cs2 = {};
  u32x4 sg;
  STAMP(0);
  p1_main(ad, acc, cs0, cs2, lane, rws, 128 * i + 32 * rg, sg);
  STAMP(1);
  p1_finish(cs0, cs2, tg[0], tg[2], !odd, lane);
  STAMP(2);
  if (PRIO == 0 || (PRIO == 1 && isq)) __builtin_amdgcn_s_setprio(1);
  float nmu, r;
  stat_get(rws, 128 * i + 32 * rg, lane, tag, sg, nmu, r);
  const float rl = r * LOG2E;
  const int Tb = 2 * h_unit0(pr, isq) - 4;
#pragma unroll
  for (int t = 0; t < 3; ++t) {
    float c16[16];
    u32x4 o[2];
    cs_get(c16, tg[t], lane);
    silu_tile(acc[t], c16, nmu, -rl, o);
    store_h(rws, i, rg, Tb + tg[t], lane, o);
  }
  STAMP(3);
  wait_vm0();
  if (arrive(C_H, lane) == (isq ? 7u : 3u)) set_flag(p.ws, WS_FH + (i * 16 + j) * 128, tag, lane);
  __builtin_amdgcn_s_setprio(0);
  const int q = rg, rb = q >> 1, cb = q & 1;
  const int orow = 64 * i2 + 32 * rb + s;
  const u32 ooff = orow * 1024 + (64 * j2 + 32 * cb) * 2 + h * 16;
  float nmu2, r2;
  stat_get(rws, 64 * i2 + 32 * rb, lane, tag, stat_load(rws, 64 * i2 + 32 * rb, lane), nmu2, r2);
  P2 c2;
  {
    const int blk = (lane >> 4) & 1, qq = (lane & 15) >> 2, pp = lane & 3;
    c2.wa[0] = q * 2048 + (4 * h + qq) * 128 + ((cb ^ (qq >> 1)) * 64) + 32 * blk + 8 * pp;
    c2.wa[1] = c2.wa[0] ^ 64;
    c2.hv = lane * 16;
    c2.wv = (lane >> 3) * 1024 + ((((lane & 7) >> 2) ^ ((lane >> 4) & 1)) * 64) + (lane & 3) * 16;
    c2.rws = srd(p.ws, WS_END); c2.rw2 = srd(p.w2, F_ * D_ * 2); c2.rwo = srd(p.wo, D_ * D_ * 2);
    c2.hs[0] = WS_H + ((2 * i2 + rb) * 112 + 28 * q) * 1024; c2.hs[1] = WS_H + ((2 * i2 + (rb ^ 1)) * 112 + 28 * q) * 1024;
    c2.w2s = 16 * 28 * q * 1024 + j2 * 128;
    c2.wos = 128 * q * 1024 + j2 * 128;
    c2.ld = ldsaddr(q * 2048);
  }
  f32x16 ac[4] = {};
  bf16x8 hw[HW][2], wf[2][2];
  STAMP(6);
  lds_spin(L_PF + 4 * q, 12);
  lds_spin(L_CNT + 4 * C_HR, 1);
  STAMP(7);
#pragma unroll
  for (int n = 0; n < HW; ++n) { hw[n][0] = asm_gload(c2.rws, c2.hv, c2.hs[0] + n * 1024); hw[n][1] = asm_gload(c2.rws, c2.hv, c2.hs[1] + n * 1024); }
  wf[0][0] = ldtr(c2.wa[0], c2.wa[0] + 1024); wf[0][1] = ldtr(c2.wa[1], c2.wa[1] + 1024);
  p2a_step<0>(c2, ac, hw, wf, q, c2.wa[0], c2.wa[1]);
  STAMP(8);
  red_send(ac, q, lane, 0);
  arrive(C_RG, lane);
  u32x4 xr[2];
#pragma unroll
  for (int u = 0; u < 2; ++u) xr[u] = *(const GAS u32x4*)((const char*)p.x + ooff + 32 * u);
  lds_spin(L_CNT + 4 * C_RG, 4);
  red_recv(ac[0], q, lane, 0);
#pragma unroll
  for (int g = 0; g < 4; ++g) {
    f32x4 v;
#pragma unroll
    for (int e = 0; e < 4; ++e) { const float gp = ac[0][4 * g + e] * r2; v[e] = gp * rcp(1.0f + ex2(-LOG2E * gp)); }
    *(LAS f32x4*)(lds + (6 + (g >> 1)) * 8192 + q * 2048 + (g & 1) * 1024 + lane * 16) = v;
  }
#pragma unroll
  for (int u = 0; u < 2; ++u) *(LAS u32x4*)(lds + 8 * 8192 + q * 2048 + u * 1024 + lane * 16) = xr[u];
  const u32 os0 = WS_O + ((2 * i2 + rb) * 32 + 8 * q) * 1024, os1 = WS_O + ((2 * i2 + (rb ^ 1)) * 32 + 8 * q) * 1024;
  lds_spin(L_CNT + 4 * C_OR, 1);
  STAMP(9);
  bf16x8 ow[8][2];
#pragma unroll
  for (int n = 0; n < 8; ++n) { ow[n][0] = asm_gload(c2.rws, c2.hv, os0 + n * 1024); ow[n][1] = asm_gload(c2.rws, c2.hv, os1 + n * 1024); }
#pragma unroll
  for (int l = 0; l < 4; ++l) ac[l] = f32x16{};
  p2b_step<0>(ac, ow, opq(c2.wa[0] + 10 * 8192), opq(c2.wa[1] + 10 * 8192));
  red_send(ac, q, lane, 10);
  arrive(C_RS, lane);
  lds_spin(L_CNT + 4 * C_RS, 4);
  red_recv(ac[0], q, lane, 10);
  const f32x16 as = ac[0];
  float G[16];
#pragma unroll
  for (int g = 0; g < 4; ++g) {
    const f32x4 v = ldf4((6 + (g >> 1)) * 8192 + q * 2048 + (g & 1) * 1024 + lane * 16);
    G[4 * g] = v[0]; G[4 * g + 1] = v[1]; G[4 * g + 2] = v[2]; G[4 * g + 3] = v[3];
  }
#pragma unroll
  for (int u = 0; u < 2; ++u) xr[u] = *(LAS u32x4*)(lds + 8 * 8192 + q * 2048 + u * 1024 + lane * 16);
  STAMP(10);
#pragma unroll
  for (int u = 0; u < 2; ++u) {
    u32x2 ra = __builtin_amdgcn_permlane32_swap(xr[u][0], xr[u][2], false, false);
    u32x2 rb_ = __builtin_amdgcn_permlane32_swap(xr[u][1], xr[u][3], false, false);
    u32 pk[2][2];
#pragma unroll
    for (int g = 0; g < 2; ++g) {
      const u32 x01 = ra[g], x23 = rb_[g];
      const int e = 8 * u + 4 * g;
      const float y0 = fmaf(as[e], G[e], u2f(x01 << 16)), y1 = fmaf(as[e + 1], G[e + 1], u2f(x01 & 0xffff0000u));
      const float y2 = fmaf(as[e + 2], G[e + 2], u2f(x23 << 16)), y3 = fmaf(as[e + 3], G[e + 3], u2f(x23 & 0xffff0000u));
      pk[g][0] = pk2(y0, y1); pk[g][1] = pk2(y2, y3);
    }
    u32x2 s0 = __builtin_amdgcn_permlane32_swap(pk[0][0], pk[1][0], false, false);
    u32x2 s1 = __builtin_amdgcn_permlane32_swap(pk[0][1], pk[1][1], false, false);
    u32x4 v = {s0[0], s1[0], s0[1], s1[1]};
    *(GAS u32x4*)((char*)p.out + ooff + 32 * u) = v;
  }
  STAMP(11);
  STAMP_DUMP;
}

DI void roleB_kv(const Params& p, int w, int lane, int i, int j, u64 pc) {
  STAMP_DECL;
  const int rg = w - 4, pr = j >> 1, odd = rg & 1;
  const int s = lane & 31, h = lane >> 5;
  bf16x8 a0[2], a1[2];
  load_wphi(p, 4 * pr + 2 * odd, lane, a0[0], a1[0]);
  load_wphi(p, 4 * pr + 2 - 2 * odd, lane, a0[1], a1[1]);
  const int tg[4] = {odd, 1 - odd, 2 + odd, 3 - odd};
  const rsrc_t rws = mkrs(p.ws, WS_END);
  const u64 tag = compute_prologue(p, rws, w, lane, i, j, pc);
  P1<4> ad; ad.init(rg, lane, tg);
  f32x16 acc[4] = {};
  f32x4 cs0 = {}, cs2 = {};
  u32x4 sg;
  STAMP(0);
  p1_main(ad, acc, cs0, cs2, lane, rws, 128 * i + 32 * rg, sg);
  STAMP(1);
  p1_finish(cs0, cs2, tg[0], tg[2], true, lane);
  STAMP(2);
  if (PRIO >= 1) __builtin_amdgcn_s_setprio(1);
  float nmu, r;
  stat_get(rws, 128 * i + 32 * rg, lane, tag, sg, nmu, r);
  const float rl = r * LOG2E;
  const u32 ib = (32 * rg + s) * 64 + ((h ^ ((s >> 1) & 7)) * 8);
  const u32 ia[4] = {ib, ib ^ 16, ib ^ 32, ib ^ 48};
#pragma unroll
  for (int t = 0; t < 2; ++t) {
    float c16[16];
    cs_get(c16, tg[t], lane);
    const f32x16 pl = phi_logits(acc[t], c16, nmu, a0[t], a1[t]);
#pragma unroll
    for (int hh = 0; hh < 2; ++hh) {
      float ep_[8], en_[8], zp, zn;
      phi_exp(pl, hh, rl, ep_, en_, zp, zn);
      const float ip = rcp(zp), in = rcp(zn);
      const u32 base = L_PHI + (2 * tg[t] + hh) * 8192;
#pragma unroll
      for (int g = 0; g < 2; ++g) {
        u32x2 vp = {pk2(ep_[4 * g] * ip, ep_[4 * g + 1] * ip), pk2(ep_[4 * g + 2] * ip, ep_[4 * g + 3] * ip)};
        u32x2 vn = {pk2(en_[4 * g] * in, en_[4 * g + 1] * in), pk2(en_[4 * g + 2] * in, en_[4 * g + 3] * in)};
        *(LAS u32x2*)(lds + base + ia[g]) = vp;
        *(LAS u32x2*)(lds + base + ia[2 + g]) = vn;
      }
    }
  }
#pragma unroll
  for (int t = 0; t < 2; ++t) {
    float c16[16];
    cs_get(c16, tg[2 + t], lane);
    const u32 base = L_V + (tg[2 + t] - 2) * 8192;
#pragma unroll
    for (int g = 0; g < 4; ++g) {
      float v[4];
#pragma unroll
      for (int e = 0; e < 4; ++e) v[e] = fmaf(nmu, c16[4 * g + e], acc[2 + t][4 * g + e]) * r;
      u32x2 vv = {pk2(v[0], v[1]), pk2(v[2], v[3])};
      *(LAS u32x2*)(lds + base + ia[g]) = vv;
    }
  }
  arrive(C_PHI, lane);
  STAMP(3);
  lds_spin(L_CNT + 4 * C_PHI, 4);
  {
    const int blk = (lane >> 4) & 1, q = (lane & 15) >> 2, pp = lane & 3;
    const int r0 = 8 * h + q, r1 = r0 + 4;
    const u32 pa0 = L_PHI + rg * 8192 + r0 * 64 + (((4 * blk + pp) ^ ((r0 >> 1) & 7)) * 8);
    const u32 pa1 = L_PHI + rg * 8192 + r1 * 64 + (((4 * blk + pp) ^ ((r1 >> 1) & 7)) * 8);
    const u32 vb = L_V + (rg >> 1) * 8192;
    const u32 pv0 = vb + r0 * 64 + (((4 * (rg & 1) + pp) ^ ((r0 >> 1) & 7)) * 8);
    const u32 pv1 = vb + r1 * 64 + (((4 * (rg & 1) + pp) ^ ((r1 >> 1) & 7)) * 8);
    const bf16x8 ones = asfrag(0x3f803f80u, 0x3f803f80u, 0x3f803f80u, 0x3f803f80u);
    f32x16 d = {};
#pragma unroll
    for (int u = 0; u < 8; ++u) {
      const bf16x8 vv = ldtr(pv0 + u * 1024, pv1 + u * 1024);
      d = mfma32(ldtr(pa0 + u * 1024, pa1 + u * 1024), blk ? ones : vv, d);
    }
    asm volatile("" :: "v"(d));
    arrive(C_IMG, lane);
    if (s <= 16) {
#pragma unroll
      for (int u = 0; u < 2; ++u) {
        u32x4 v = {pk2(d[8 * u], d[8 * u + 1]), pk2(d[8 * u + 2], d[8 * u + 3]), pk2(d[8 * u + 4], d[8 * u + 5]), pk2(d[8 * u + 6], d[8 * u + 7])};
        st128_sc1(rws, v, (2 * s + h) * 16, WS_KV + (((pr * 16 + i) * 4 + rg) * 2 + u) * 544);
      }
    }
  }
  STAMP(4);
  wait_vm0();
  if (arrive(C_KV, lane) == 3) set_flag(p.ws, WS_FK + (pr * 16 + i) * 128, tag, lane);
  STAMP(5);
  STAMP_DUMP;
}

DI void roleB_q(const Params& p, int w, int lane, int i, int j, u64 pc) {
  STAMP_DECL;
  const int rg = w - 4, pr = j >> 1, odd = rg & 1;
  const int s = lane & 31, h = lane >> 5;
  bf16x8 a0[2], a1[2];
  load_wphi(p, 4 * pr + 2 * odd, lane, a0[0], a1[0]);
  load_wphi(p, 4 * pr + 2 - 2 * odd, lane, a0[1], a1[1]);
  const int tg[3] = {odd, 1 - odd, 7};
  const rsrc_t rws = mkrs(p.ws, WS_END);
  const u64 tag = compute_prologue(p, rws, w, lane, i, j, pc);
  P1<3> ad; ad.init(rg, lane, tg);
  f32x16 acc[3] = {};
  f32x4 cs0 = {}, cs2 = {};
  u32x4 sg;
  STAMP(0);
  p1_main(ad, acc, cs0, cs2, lane, rws, 128 * i + 32 * rg, sg);
  STAMP(1);
  p1_finish(cs0, cs2, tg[0], tg[2], !odd, lane);
  STAMP(2);
  float nmu, r;
  stat_get(rws, 128 * i + 32 * rg, lane, tag, sg, nmu, r);
  const float rl = r * LOG2E;
  {
    float c16[16];
    u32x4 o[2];
    cs_get(c16, 7, lane);
    silu_tile(acc[2], c16, nmu, -rl, o);
    store_h(rws, i, rg, 2 * h_unit0(pr, 1) + 3, lane, o);
  }
  STAMP(3);
  bf16x8 bq[4][2];
  float zeps[4];
#pragma unroll
  for (int t = 0; t < 2; ++t) {
    float c16[16];
    cs_get(c16, tg[t], lane);
    const f32x16 pl = phi_logits(acc[t], c16, nmu, a0[t], a1[t]);
#pragma unroll
    for (int hh = 0; hh < 2; ++hh) {
      float ep_[8], en_[8], zp, zn;
      phi_exp(pl, hh, rl, ep_, en_, zp, zn);
      const float rho = zp * rcp(zn);
      bq[2 * t + hh][0] = asfrag(pk2(ep_[0], ep_[1]), pk2(ep_[2], ep_[3]), pk2(ep_[4], ep_[5]), pk2(ep_[6], ep_[7]));
      bq[2 * t + hh][1] = asfrag(pk2(en_[0] * rho, en_[1] * rho), pk2(en_[2] * rho, en_[3] * rho), pk2(en_[4] * rho, en_[5] * rho), pk2(en_[6] * rho, en_[7] * rho));
      zeps[2 * t + hh] = zp * 1e-6f;
    }
    if (t == 0) {
      wait_vm0();
      if (arrive(C_H, lane) == 7) set_flag(p.ws, WS_FH + (i * 16 + j) * 128, tag, lane);
    }
  }
  STAMP(4);
  poll16(p.ws, WS_FK + pr * 16 * 128, 128, tag, lane, 15);
  STAMP(5);
  {
    u32x4 pv[2][8];
    u32x2 pz[2];
#pragma unroll
    for (int u = 0; u < 2; ++u) {
      const u32 pbase = WS_KV + ((pr * 16 * 4 + rg) * 2 + u) * 544;
#pragma unroll
      for (int m = 0; m < 8; ++m)
        pv[u][m] = __builtin_amdgcn_raw_buffer_load_b128(rws, ((lane >> 5) * 8 * 544) + (lane & 31) * 16, pbase + 2 * m * 8 * 544, 16);
      pz[u] = __builtin_amdgcn_raw_buffer_load_b64(rws, (lane >> 2) * 8 * 544 + 512 + (lane & 3) * 8, pbase, 16);
    }
    u32 ps[4];
#pragma unroll
    for (int e = 0; e < 4; ++e) ps[e] = (s == 2 * e ? 0x00003f80u : 0u) | (s == 2 * e + 1 ? 0x3f800000u : 0u);
    const bf16x8 psel = asfrag(ps[0], ps[1], ps[2], ps[3]);
#pragma unroll
    for (int u = 0; u < 2; ++u) {
      f32x16 sm = {};
#pragma unroll
      for (int m = 0; m < 8; ++m) sm = mfma32(psel, __builtin_bit_cast(bf16x8, pv[u][m]), sm);
      float zz[4] = {u2f(pz[u][0] << 16), u2f(pz[u][0] & 0xffff0000u), u2f(pz[u][1] << 16), u2f(pz[u][1] & 0xffff0000u)};
#pragma unroll
      for (int e = 0; e < 4; ++e) {
        float v = zz[e];
        v += u2f(__builtin_amdgcn_update_dpp(0, (int)f2u(v), 0x128, 0xf, 0xf, false));
        v += u2f(__builtin_amdgcn_update_dpp(0, (int)f2u(v), 0x124, 0xf, 0xf, false));
        u32x2 t = __builtin_amdgcn_permlane16_swap(f2u(v), f2u(v), false, false);
        v = u2f(t[0]) + u2f(t[1]);
        zz[e] = xsum(v);
      }
      const u32 kb = L_KVA + (rg * 2 + u) * 544;
      { u32x2 v = {pk2(sm[0], sm[1]), pk2(sm[2], sm[3])}; *(LAS u32x2*)(lds + kb + s * 16 + h * 8) = v; }
      if (lane < 4) { u32x2 v = {pk2(zz[0], zz[1]), pk2(zz[2], zz[3])}; *(LAS u32x2*)(lds + kb + 512 + lane * 8) = v; }
    }
  }
  arrive(C_CH, lane);
  lds_spin(L_CNT + 4 * C_CH, 4);
  STAMP(6);
#pragma unroll
  for (int hh = 0; hh < 4; ++hh) {
    const u32 ao = (s < 16 ? s : 16) * 32 + h * 16;
    f32x16 d = {};
    const int ah = hh ^ (2 * odd);
    d = mfma32(ld128(L_KVA + (ah * 2) * 544 + ao), bq[hh][0], d);
    d = mfma32(ld128(L_KVA + (ah * 2 + 1) * 544 + ao), bq[hh][1], d);
    const float inv = rcp(d[8] + zeps[hh]);
    u32x4 v = {pk2(d[0] * inv, d[1] * inv), pk2(d[2] * inv, d[3] * inv), pk2(d[4] * inv, d[5] * inv), pk2(d[6] * inv, d[7] * inv)};
    st128_sc1(rws, v, lane * 16, WS_O + ((4 * i + rg) * 32 + 4 * pr + ah) * 1024);
  }
  STAMP(7);
  wait_vm0();
  if (arrive(C_O, lane) == 3) set_flag(p.ws, WS_FO + (i * 8 + pr) * 128, tag, lane);
  STAMP(8);
  STAMP_DUMP;
}

__global__ void __launch_bounds__(768, 3) fused_hedgehog_ffn(Params p) {
  u64 pc;
  asm volatile("s_getpc_b64 %0" : "=s"(pc));
  const int tid = threadIdx.x, lane = tid & 63;
  const int w = __builtin_amdgcn_readfirstlane(tid >> 6);
  const int b = blockIdx.x, g = b & 7, m = b >> 3;
#define MAP 2
  const int i = MAP == 0 ? 4 * (g >> 1) + (m & 3) : MAP == 1 ? 2 * g + (m & 1) : MAP == 2 ? 8 * (g >> 2) + (m & 7) : (b >> 4);
  const int j = MAP == 0 ? 8 * (g & 1) + (m >> 2) : MAP == 1 ? (m >> 1) : MAP == 2 ? 4 * (g & 3) + (m >> 3) : (b & 15);
  if (w == 4) {
    *(LAS u32*)(lds + L_CNT + 4 * (lane & 31)) = 0;
#pragma unroll
    for (int q = 0; q < 4; ++q) *(LAS u32*)(lds + L_CS + 4 * (lane + 64 * q)) = 0;
    wait_lgkm0();
  }
#define MAP2 1
  const int i2 = MAP2 == 0 ? 2 * i + (j & 1) : 8 * (g >> 1) + (m & 7), j2 = MAP2 == 0 ? j >> 1 : 4 * (g & 1) + (m >> 3);
  if (w >= 8) loader_main(p, w, lane, i, j, i2, j2);
  else if (w < 4) roleA_main(p, w, lane, i, j, i2, j2, pc);
  else if (j & 1) roleB_q(p, w, lane, i, j, pc);
  else roleB_kv(p, w, lane, i, j, pc);
}

extern "C" void kernel_launch(void* const* d_in, const int* in_sizes, int n_in, void* d_out, int out_size, void* d_ws, size_t ws_size,
                              hipStream_t stream) {
  Params p;
  p.x = (const u16*)d_in[0]; p.wqkv = (const u16*)d_in[1]; p.wphi = (const u16*)d_in[2]; p.wo = (const u16*)d_in[3];
  p.w1 = (const u16*)d_in[5]; p.w2 = (const u16*)d_in[6]; p.out = (u16*)d_out; p.ws = (char*)d_ws;
  fused_hedgehog_ffn<<<256, 768, 0, stream>>>(p);
}
```

```cpp
#define PROBE 20
#include <hip/hip_runtime.h>

typedef unsigned short u16;
typedef unsigned u32;
typedef unsigned long long u64;
typedef short bf16x8 __attribute__((ext_vector_type(8)));
typedef short s16x4 __attribute__((ext_vector_type(4)));
typedef __bf16 bf2 __attribute__((ext_vector_type(2)));
typedef float f32x16 __attribute__((ext_vector_type(16)));
typedef float f32x4 __attribute__((ext_vector_type(4)));
typedef u32 u32x4 __attribute__((ext_vector_type(4)));
typedef u32 u32x2 __attribute__((ext_vector_type(2)));
#define LAS __attribute__((address_space(3)))
#define GAS __attribute__((address_space(1)))
#define DI __device__ __forceinline__
#define RLX __ATOMIC_RELAXED, __HIP_MEMORY_SCOPE_AGENT
#define RLXW __ATOMIC_RELAXED, __HIP_MEMORY_SCOPE_WORKGROUP

#define PRIO 1
#define TIMING 0

constexpr int S_ = 2048, D_ = 512, F_ = 1792;
constexpr float LOG2E = 1.4426950408889634f;

constexpr u32 WS_E = 0;
constexpr u32 WS_FH = 4096;
constexpr u32 WS_FK = WS_FH + 32768;
constexpr u32 WS_FO = WS_FK + 32768;
constexpr u32 WS_ST = 102400;
constexpr u32 WS_KV = 262144;
constexpr u32 WS_H = 1u << 20;
constexpr u32 WS_O = WS_H + 64u * 112u * 1024u;
constexpr u32 WS_END = WS_O + 64u * 32u * 1024u;
constexpr u32 WS_TM = 16u << 20;

constexpr u32 W1R = 0, NW1 = 5;
constexpr u32 X1R = 81920, NX1 = 4;
constexpr u32 L_PHI = 98304, L_V = 131072;
constexpr u32 L_KVA = 147456;
constexpr u32 MISC = 157696;
constexpr u32 L_CS = MISC + 1024;
constexpr u32 L_CNT = MISC + 2048, L_TAG = MISC;
constexpr u32 L_PF = L_CNT + 64;
enum { C_H = 0, C_PHI = 1, C_KV = 2, C_CH = 3, C_O = 4, C_HR = 5, C_OR = 6, C_IMG = 7, C_RG = 8, C_RS = 9 };
constexpr u32 SPIN_CAP = 1u << 22;

__shared__ __attribute__((aligned(1024))) u32 lds32[40960];
#define lds ((char*)lds32)

DI f32x16 mfma32(bf16x8 a, bf16x8 b, f32x16 c) { return __builtin_amdgcn_mfma_f32_32x32x16_bf16(a, b, c, 0, 0, 0); }
DI u32 pk2(float a, float b) { bf2 v = {(__bf16)a, (__bf16)b}; return __builtin_bit_cast(u32, v); }
DI float dot2(u32 a, u32 b, float c) { return __builtin_amdgcn_fdot2_f32_bf16(__builtin_bit_cast(bf2, a), __builtin_bit_cast(bf2, b), c, false); }
DI float ex2(float x) { return __builtin_amdgcn_exp2f(x); }
DI float rcp(float x) { return __builtin_amdgcn_rcpf(x); }
DI float fmax2(float a, float b) { return __builtin_elementwise_maximum(a, b); }
DI float fmin2(float a, float b) { return __builtin_elementwise_minimum(a, b); }
DI u32 f2u(float x) { return __builtin_bit_cast(u32, x); }
DI float u2f(u32 x) { return __builtin_bit_cast(float, x); }
DI bf16x8 ld128(u32 off) { return *(LAS bf16x8*)(lds + off); }
DI f32x4 ldf4(u32 off) { return *(LAS f32x4*)(lds + off); }
DI float ldf(u32 off) { return *(LAS float*)(lds + off); }
DI void stf(u32 off, float v) { *(LAS float*)(lds + off) = v; }
DI bf16x8 ldtr(u32 off0, u32 off1) {
  s16x4 a = __builtin_amdgcn_ds_read_tr16_b64_v4i16((LAS s16x4*)(lds + off0));
  s16x4 b = __builtin_amdgcn_ds_read_tr16_b64_v4i16((LAS s16x4*)(lds + off1));
  return __builtin_shufflevector(a, b, 0, 1, 2, 3, 4, 5, 6, 7);
}
DI bf16x8 asfrag(u32 a, u32 b, u32 c, u32 d) { u32x4 v = {a, b, c, d}; return __builtin_bit_cast(bf16x8, v); }
DI void barrier() { asm volatile("s_barrier" ::: "memory"); }
DI void wait_lgkm0() { asm volatile("s_waitcnt lgkmcnt(0)" ::: "memory"); }
DI void wait_vm0() { asm volatile("s_waitcnt vmcnt(0)" ::: "memory"); }
template <int N> DI void wait_vm() { asm volatile("s_waitcnt vmcnt(%0)" ::"n"(N) : "memory"); }
DI float xsum(float x) { u32x2 r = __builtin_amdgcn_permlane32_swap(f2u(x), f2u(x), false, false); return u2f(r[0]) + u2f(r[1]); }
DI void xmaxmin(float& mx, float& mn) {
  u32x2 r = __builtin_amdgcn_permlane32_swap(f2u(mx), f2u(-mn), false, false);
  float m = fmax2(u2f(r[0]), u2f(r[1]));
  u32x2 q = __builtin_amdgcn_permlane32_swap(f2u(m), f2u(m), false, false);
  mx = u2f(q[0]); mn = -u2f(q[1]);
}
DI void xsum2(float& a, float& b) {
  u32x2 r = __builtin_amdgcn_permlane32_swap(f2u(a), f2u(b), false, false);
  float m = u2f(r[0]) + u2f(r[1]);
  u32x2 q = __builtin_amdgcn_permlane32_swap(f2u(m), f2u(m), false, false);
  a = u2f(q[0]); b = u2f(q[1]);
}
DI void lds_st(u32 off, u32 v) { __hip_atomic_store((LAS u32*)&lds32[off >> 2], v, RLXW); }
DI u32 ldsaddr(u32 off) { return (u32)(size_t)(LAS char*)lds + off; }
DI u32x4 asm_ld128(u32 off) {
  u32x4 v;
  asm volatile("ds_read_b128 %0, %1\n\ts_waitcnt lgkmcnt(0)" : "=&v"(v) : "v"(ldsaddr(off)) : "memory");
  return v;
}
DI void asm_st32(u32 off, u32 val) { asm volatile("ds_write_b32 %0, %1" ::"v"(ldsaddr(off)), "v"(val) : "memory"); }
DI u32 asm_ld32(u32 off) {
  u32 v;
  asm volatile("ds_read_b32 %0, %1\n\ts_waitcnt lgkmcnt(0)" : "=&v"(v) : "v"(ldsaddr(off)) : "memory");
  return v;
}
DI void lds_spin(u32 off, u32 target) {
#pragma unroll 1
  for (u32 it = 0; it < SPIN_CAP; ++it) {
    if (__builtin_amdgcn_readfirstlane(asm_ld32(off)) >= target) break;
    __builtin_amdgcn_s_sleep(1);
  }
}
DI u32 lds_add_rtn(u32 off) { return __hip_atomic_fetch_add((LAS u32*)&lds32[off >> 2], 1u, RLXW); }
DI u32 lds_ld(u32 off) { return __hip_atomic_load((LAS u32*)&lds32[off >> 2], RLXW); }
DI u32 arrive(int cnt, int lane) {
  asm volatile("" ::: "memory");
  u32 old = 0;
  if (lane == 0) old = lds_add_rtn(L_CNT + 4 * cnt);
  old = __builtin_amdgcn_readfirstlane(old);
  asm volatile("" ::: "memory");
  return old;
}
DI void poll16(const char* ws, u32 base, u32 stride, u64 tag, int lane, int mask) {
  GAS u64* p = (GAS u64*)(ws + base + (u32)(lane & mask) * stride);
#pragma unroll 1
  for (u32 it = 0; it < SPIN_CAP; ++it) {
    u64 v = __hip_atomic_load(p, RLX);
    if (__all(v == tag)) break;
    __builtin_amdgcn_s_sleep(1);
  }
  asm volatile("" ::: "memory");
}
DI void set_flag(char* ws, u32 off, u64 tag, int lane) {
  if (lane == 0) __hip_atomic_store((GAS u64*)(ws + off), tag, RLX);
}

#define STAMP(k) do { if ((k) == PROBE) { __builtin_amdgcn_sched_barrier(0); probe_t = __builtin_amdgcn_s_memrealtime(); __builtin_amdgcn_s_waitcnt(0xC07F); __builtin_amdgcn_sched_barrier(0); } } while (0)
#define STAMP_DECL u64 probe_t = 0
#define STAMP_DUMP do { if (w < 4) { while (__builtin_amdgcn_s_memrealtime() < probe_t + 6000) __builtin_amdgcn_s_sleep(2); } } while (0)

struct Params { const u16* x; const u16* wqkv; const u16* wphi; const u16* wo; const u16* w1; const u16* w2; u16* out; char* ws; };

typedef __amdgpu_buffer_rsrc_t rsrc_t;
DI rsrc_t mkrs(const void* p, u32 bytes) { return __builtin_amdgcn_make_buffer_rsrc((void*)p, 0, bytes, 0x00020000); }
DI void st128_sc1(rsrc_t rs, u32x4 v, u32 voff, u32 soff) { __builtin_amdgcn_raw_buffer_store_b128(v, rs, voff + soff, 0, 16); }
template <int AUX> DI void dma16(rsrc_t rs, u32 ldsoff, u32 voff, u32 soff) {
  __builtin_amdgcn_raw_ptr_buffer_load_lds(rs, (LAS void*)(lds + ldsoff), 16, voff, soff, 0, AUX);
}

DI int h_unit0(int p, int isq) { return isq ? ((p & 1) ? (7 * p + 3) >> 1 : (7 * p) >> 1) : ((p & 1) ? (7 * p - 1) >> 1 : (7 * p + 4) >> 1); }

DI void loader_main(const Params& p, int w, int lane, int i, int j, int i2, int j2) {
  STAMP_DECL;
  const int wl = w - 8, pr = j >> 1, isq = j & 1;
  const rsrc_t rx = mkrs(p.x, S_ * D_ * 2), rq = mkrs(p.wqkv, D_ * 1536 * 2), r1 = mkrs(p.w1, D_ * F_ * 2);
  const u32 gsw = ((((lane & 7) >> 2) ^ ((lane >> 4) & 1)) * 64) + (lane & 3) * 16;
  const u32 xv = (lane >> 3) * 1024 + (((lane & 7) ^ ((4 * (wl & 1) + (lane >> 4)) & 7)) * 16);
  const u32 qv = (lane >> 3) * 3072 + gsw, hv1 = (lane >> 3) * 3584 + gsw;
  const u32 xs = (128 * i + 8 * wl) * 1024;
  const u32 s0 = (isq ? pr * 128 : 1024 + pr * 128) + 8 * wl * 3072, s1 = 2048 + pr * 128 + 8 * wl * 3072;
  const u32 h0 = h_unit0(pr, isq) * 128 + 8 * wl * 3584;
  auto issue_w = [&](int c, u32 slot) {
    const u32 d = slot + wl * 1024;
    dma16<0>(rq, d, qv, s0 + c * (32 * 3072));
    if (!isq) dma16<0>(rq, d + 4096, qv, s1 + c * (32 * 3072));
    dma16<0>(r1, d + 8192, hv1, h0 + c * (32 * 3584));
    dma16<0>(r1, d + 12288, hv1, h0 + 128 + c * (32 * 3584));
  };
  auto issue_x = [&](int P, int half) {
    const u32 d = X1R + (P & 3) * 16384 + (wl + 8 * half) * 1024;
    dma16<0>(rx, d, xv, xs + half * 65536 + P * 128);
    dma16<0>(rx, d + 4096, xv, xs + half * 65536 + 32768 + P * 128);
  };
  STAMP(0);
  issue_x(0, 0); issue_x(0, 1);
  issue_w(0, 0); issue_w(1, 16384);
  issue_x(1, 0); issue_x(1, 1);
  issue_w(2, 2 * 16384);
  STAMP(1);
  if (isq) wait_vm<7>(); else wait_vm<8>();
  barrier();
  issue_w(3, 3 * 16384);
  issue_x(2, 0); issue_x(2, 1);
  issue_w(4, 4 * 16384);
  if (isq) wait_vm<10>(); else wait_vm<12>();
  barrier();
  issue_w(5, 0);
  issue_x(3, 0); issue_x(3, 1);
  u32 wslot = 16384;
#pragma unroll 1
  for (int c = 2; c < 16; ++c) {
    if (isq) {
      if (c <= 11) wait_vm<10>(); else if (c == 12) wait_vm<6>(); else if (c == 13) wait_vm<3>(); else wait_vm<0>();
    } else {
      if (c <= 11) wait_vm<12>(); else if (c == 12) wait_vm<8>(); else if (c == 13) wait_vm<4>(); else wait_vm<0>();
    }
    barrier();
    if (c <= 11) issue_w(c + 4, wslot);
    if (c <= 9) issue_x((c + 6) >> 1, c & 1);
    wslot = wslot == 4 * 16384 ? 0 : wslot + 16384;
  }
  STAMP(2);
  barrier();
  STAMP(3);
  const rsrc_t rw2 = mkrs(p.w2, F_ * D_ * 2);
  const u32 wv = (lane >> 3) * 1024 + gsw, wsrc = 16 * 28 * wl * 1024 + j2 * 128;
  auto issue_w2 = [&](int n) {
    dma16<0>(rw2, n * 8192 + wl * 2048, wv, wsrc + n * 16384);
    dma16<0>(rw2, n * 8192 + wl * 2048 + 1024, wv, wsrc + n * 16384 + 8192);
  };
#pragma unroll 1
  for (int n = 0; n < 12; ++n) issue_w2(n);
  wait_vm0();
  asm_st32(L_PF + 4 * wl, 12);
  if (!isq) lds_spin(L_CNT + 4 * C_IMG, 4);
#pragma unroll 1
  for (int n = 12; n < 18; ++n) issue_w2(n);
  wait_vm0();
  asm_st32(L_PF + 4 * wl, 18);
  STAMP(4);
  if (wl == 0) {
    const u32x4 tg = asm_ld128(L_TAG);
    const u64 tag = (u64)tg[0] | ((u64)tg[1] << 32);
    poll16(p.ws, WS_FH + (i2 >> 1) * 16 * 128, 128, tag, lane, 15);
    asm_st32(L_CNT + 4 * C_HR, 1);
    STAMP(5);
    poll16(p.ws, WS_FO + (i2 >> 1) * 8 * 128, 128, tag, lane, 7);
    asm_st32(L_CNT + 4 * C_OR, 1);
    STAMP(6);
    if (blockIdx.x == 0) {
      poll16(p.ws, WS_FO, 128, tag, lane, 63);
      poll16(p.ws, WS_FO + 64 * 128, 128, tag, lane, 63);
      if (lane == 0) __hip_atomic_store((GAS u64*)(p.ws + WS_E), tag, RLX);
    }
  }
  STAMP_DUMP;
}

template <int NT> struct Frag { bf16x8 x; bf16x8 w[NT]; };

template <int NT> struct P1 {
  u32 xa[4];
  u32 wa[NT];
  DI void init(int rg, int lane, const int (&tg)[NT]) {
    const int s = lane & 31, h = lane >> 5, blk = (lane >> 4) & 1, q = (lane & 15) >> 2, pp = lane & 3, hi = rg >> 1;
#pragma unroll
    for (int k = 0; k < 4; ++k) xa[k] = X1R + (((32 * rg + s) * 128 + ((h ^ ((s >> 1) & 7)) * 16)) ^ ((k ^ hi) << 5));
#pragma unroll
    for (int t = 0; t < NT; ++t)
      wa[t] = W1R + (tg[t] >> 1) * 4096 + hi * 2048 + (8 * h + q) * 128 + (((tg[t] & 1) ^ (q >> 1)) * 64) + 32 * blk + 8 * pp;
  }
};
#define SB() __builtin_amdgcn_sched_barrier(0)
template <int NT> struct Addr { u32 w[NT]; };
template <int NT> DI Addr<NT> waddr(const P1<NT>& a, u32 slot) {
  Addr<NT> r;
#pragma unroll
  for (int t = 0; t < NT; ++t) r.w[t] = a.wa[t] + slot;
  return r;
}

template <int NT, bool CS, bool RELOAD>
DI void p1_step(Frag<NT>& f, f32x16 (&acc)[NT], bf16x8 sel, f32x4& cs0, f32x4& cs2, const Addr<NT>& wa, u32 xa, int ks) {
#pragma unroll
  for (int t = 0; t < NT; ++t) {
    acc[t] = mfma32(f.w[t], f.x, acc[t]);
    if constexpr (CS) {
      if (t == 0) cs0 = __builtin_amdgcn_mfma_f32_16x16x32_bf16(sel, f.w[0], cs0, 0, 0, 0);
      if (t == 2) cs2 = __builtin_amdgcn_mfma_f32_16x16x32_bf16(sel, f.w[2], cs2, 0, 0, 0);
    }
    SB();
    if constexpr (RELOAD) {
      const u32 ad = wa.w[t] ^ (ks << 11);
      f.w[t] = ldtr(ad, ad + 512);
      if (t == NT - 1) f.x = ld128(xa);
    }
    SB();
  }
}
template <int NT, bool LAST>
DI void p1_pair(const P1<NT>& a, Frag<NT>& f0, Frag<NT>& f1, f32x16 (&acc)[NT], bf16x8 sel, f32x4& cs0, f32x4& cs2, u32 w1, u32 w2, u32 xsl, u32 xn) {
  {
    const Addr<NT> wa = waddr(a, w1);
    p1_step<NT, true, true>(f0, acc, sel, cs0, cs2, wa, a.xa[2] + xsl, 0);
    p1_step<NT, false, true>(f1, acc, sel, cs0, cs2, wa, a.xa[3] + xsl, 1);
  }
  barrier();
  {
    const Addr<NT> wa = waddr(a, w2);
    p1_step<NT, true, !LAST>(f0, acc, sel, cs0, cs2, wa, a.xa[0] + xn, 0);
    p1_step<NT, false, !LAST>(f1, acc, sel, cs0, cs2, wa, a.xa[1] + xn, 1);
  }
  if constexpr (!LAST) barrier();
}
struct Stat { u32x4 g; };
DI u32x4 stat_load(rsrc_t rws, int row0, int lane) { return __builtin_amdgcn_raw_buffer_load_b128(rws, (lane & 31) * 16, WS_ST + row0 * 16, 16); }
DI void stat_get(rsrc_t rws, int row0, int lane, u64 tag, u32x4 g, float& nmu, float& r) {
#pragma unroll 1
  for (u32 it = 0; it < SPIN_CAP; ++it) {
    if (__all(g[1] == (u32)tag && g[3] == (u32)(tag >> 32))) break;
    __builtin_amdgcn_s_sleep(1);
    g = stat_load(rws, row0, lane);
  }
  nmu = -u2f(g[0]); r = u2f(g[2]);
}
DI void stat_publish(const Params& p, rsrc_t rws, int row, int lane, u64 tag, u32x4 xv) {
  float s0 = 0.f, s1 = 0.f, q0 = 0.f, q1 = 0.f;
#pragma unroll
  for (int e = 0; e < 4; ++e) {
    const float lo = u2f(xv[e] << 16), hi = u2f(xv[e] & 0xffff0000u);
    s0 += lo; s1 += hi; q0 = fmaf(lo, lo, q0); q1 = fmaf(hi, hi, q1);
  }
  float s = s0 + s1, q = q0 + q1;
#define DPP_ADD(v, ctrl) v += u2f(__builtin_amdgcn_update_dpp(0, (int)f2u(v), ctrl, 0xf, 0xf, false))
  DPP_ADD(s, 0x128); DPP_ADD(q, 0x128); DPP_ADD(s, 0x124); DPP_ADD(q, 0x124);
  DPP_ADD(s, 0x122); DPP_ADD(q, 0x122); DPP_ADD(s, 0x121); DPP_ADD(q, 0x121);
#undef DPP_ADD
  { u32x2 a = __builtin_amdgcn_permlane16_swap(f2u(s), f2u(q), false, false);
    float m = u2f(a[0]) + u2f(a[1]);
    u32x2 b = __builtin_amdgcn_permlane16_swap(f2u(m), f2u(m), false, false);
    s = u2f(b[0]); q = u2f(b[1]); }
  xsum2(s, q);
  const float mu = s * (1.0f / D_);
  const float var = fmaf(-mu, mu, q * (1.0f / D_));
  const float r = __builtin_amdgcn_rsqf(fmax2(var, 0.f) + 1e-5f);
  if (lane == 0) {
    const u32x4 g = {f2u(mu), (u32)tag, f2u(r), (u32)(tag >> 32)};
    st128_sc1(rws, g, 0, WS_ST + row * 16);
  }
}
template <int NT> DI void p1_main(const P1<NT>& a, f32x16 (&acc)[NT], f32x4& cs0, f32x4& cs2, int lane, rsrc_t rws, int row0, u32x4& sg) {
  Frag<NT> f0, f1;
  const u32 one = ((lane & 15) == ((lane >> 4) & 1)) ? 0x3f803f80u : 0u;
  const bf16x8 sel = asfrag(one, one, one, one);
#pragma unroll
  for (int t = 0; t < NT; ++t) asm volatile("" : "+v"(acc[t]));
  barrier();
#pragma unroll
  for (int t = 0; t < NT; ++t) f0.w[t] = ldtr(a.wa[t], a.wa[t] + 512);
  f0.x = ld128(a.xa[0]);
  SB();
#pragma unroll
  for (int t = 0; t < NT; ++t) f1.w[t] = ldtr(a.wa[t] ^ 2048, (a.wa[t] ^ 2048) + 512);
  f1.x = ld128(a.xa[1]);
  SB();
  u32 wsl = 0, xsl = 0;
#pragma unroll 1
  for (int cp = 0; cp < 7; ++cp) {
    const u32 w1 = wsl == 4 * 16384 ? 0 : wsl + 16384;
    const u32 w2 = w1 == 4 * 16384 ? 0 : w1 + 16384;
    const u32 xn = (xsl + 16384) & 65535;
    p1_pair<NT, false>(a, f0, f1, acc, sel, cs0, cs2, w1, w2, xsl, xn);
    wsl = w2; xsl = xn;
  }
  sg = stat_load(rws, row0, lane);
  p1_pair<NT, true>(a, f0, f1, acc, sel, cs0, cs2, (15 % NW1) * 16384, 0, xsl, 0);
}

DI void p1_finish(f32x4 cs0, f32x4 cs2, int g0, int g2, bool add2, int lane) {
  if (lane < 16) {
    __builtin_amdgcn_ds_faddf((LAS float*)&lds32[(L_CS + g0 * 128 + lane * 4) >> 2], cs0[0], 0, 0, false);
    __builtin_amdgcn_ds_faddf((LAS float*)&lds32[(L_CS + g0 * 128 + 64 + lane * 4) >> 2], cs0[1], 0, 0, false);
    if (add2) {
      __builtin_amdgcn_ds_faddf((LAS float*)&lds32[(L_CS + g2 * 128 + lane * 4) >> 2], cs2[0], 0, 0, false);
      __builtin_amdgcn_ds_faddf((LAS float*)&lds32[(L_CS + g2 * 128 + 64 + lane * 4) >> 2], cs2[1], 0, 0, false);
    }
  }
  wait_lgkm0();
  barrier();
}
DI void cs_get(float (&c)[16], int gt, int lane) {
  const int h = lane >> 5;
#pragma unroll
  for (int g = 0; g < 4; ++g) {
    f32x4 v = ldf4(L_CS + gt * 128 + (8 * g + 4 * h) * 4);
    c[4 * g] = v[0]; c[4 * g + 1] = v[1]; c[4 * g + 2] = v[2]; c[4 * g + 3] = v[3];
  }
}

DI void silu_tile(const f32x16& acc, const float (&cs)[16], float nmu, float nrl, u32x4 (&o)[2]) {
  float y[16];
#pragma unroll
  for (int e = 0; e < 16; ++e) {
    const float t = fmaf(nmu, cs[e], acc[e]);
    y[e] = t * rcp(1.0f + ex2(t * nrl));
  }
#pragma unroll
  for (int u = 0; u < 2; ++u)
#pragma unroll
    for (int e = 0; e < 4; ++e) o[u][e] = pk2(y[8 * u + 2 * e], y[8 * u + 2 * e + 1]);
}

DI f32x16 phi_logits(const f32x16& acc, const float (&cs)[16], float nmu, bf16x8 a0, bf16x8 a1) {
  float t[16];
#pragma unroll
  for (int e = 0; e < 16; ++e) t[e] = fmaf(nmu, cs[e], acc[e]);
  bf16x8 b0 = asfrag(pk2(t[0], t[1]), pk2(t[2], t[3]), pk2(t[4], t[5]), pk2(t[6], t[7]));
  bf16x8 b1 = asfrag(pk2(t[8], t[9]), pk2(t[10], t[11]), pk2(t[12], t[13]), pk2(t[14], t[15]));
  f32x16 z = {};
  z = mfma32(a0, b0, z);
  z = mfma32(a1, b1, z);
  return z;
}
DI void phi_exp(const f32x16& pl, int hh, float rl, float (&ep)[8], float (&en)[8], float& zp, float& zn) {
  const int o = 8 * hh;
  float mx = fmax2(fmax2(fmax2(fmax2(fmax2(pl[o], pl[o + 1]), pl[o + 2]), fmax2(fmax2(pl[o + 3], pl[o + 4]), pl[o + 5])), pl[o + 6]), pl[o + 7]);
  float mn = fmin2(fmin2(fmin2(fmin2(fmin2(pl[o], pl[o + 1]), pl[o + 2]), fmin2(fmin2(pl[o + 3], pl[o + 4]), pl[o + 5])), pl[o + 6]), pl[o + 7]);
  xmaxmin(mx, mn);
  const float a = -rl * mx, b = rl * mn;
  zp = 0.f; zn = 0.f;
#pragma unroll
  for (int e = 0; e < 8; ++e) {
    ep[e] = ex2(fmaf(pl[8 * hh + e], rl, a));
    en[e] = ex2(fmaf(pl[8 * hh + e], -rl, b));
    zp += ep[e]; zn += en[e];
  }
  xsum2(zp, zn);
}

DI void load_wphi(const Params& p, int head0, int lane, bf16x8& a0, bf16x8& a1) {
  const int row = lane & 31, h = lane >> 5;
  const u16* wp = p.wphi + (head0 + (row >> 4)) * 256 + (row & 15);
  u32 v[4];
#pragma unroll
  for (int e = 0; e < 4; ++e) {
    const int j0 = 2 * e, j1 = 2 * e + 1;
    const int d0 = 8 * (j0 >> 2) + 4 * h + (j0 & 3), d1 = 8 * (j1 >> 2) + 4 * h + (j1 & 3);
    v[e] = (u32)wp[d0 * 16] | ((u32)wp[d1 * 16] << 16);
  }
  const bool lo = row < 16;
  a0 = asfrag(lo ? v[0] : 0u, lo ? v[1] : 0u, lo ? v[2] : 0u, lo ? v[3] : 0u);
  a1 = asfrag(lo ? 0u : v[0], lo ? 0u : v[1], lo ? 0u : v[2], lo ? 0u : v[3]);
}

DI void store_h(rsrc_t rws, int i, int rg, int tile, int lane, const u32x4 (&o)[2]) {
#pragma unroll
  for (int u = 0; u < 2; ++u)
    st128_sc1(rws, o[u], lane * 16, WS_H + ((4 * i + rg) * 112 + 2 * tile + u) * 1024);
}

#define HWIN 7
constexpr int HW = HWIN;
DI u32x4 srd(const void* ptr, u32 bytes) {
  const u64 a = (u64)ptr;
  u32x4 d = {(u32)a, (u32)(a >> 32) & 0xffffu, bytes, 0x00020000u};
  d[0] = __builtin_amdgcn_readfirstlane(d[0]); d[1] = __builtin_amdgcn_readfirstlane(d[1]);
  return d;
}
DI bf16x8 asm_gload(u32x4 rs, u32 voff, u32 soff) {
  bf16x8 v;
  asm volatile("buffer_load_dwordx4 %0, %1, %2, %3 offen sc1" : "=v"(v) : "v"(voff), "s"(rs), "s"(soff) : "memory");
  return v;
}
DI void asm_dma(u32x4 rs, u32 ldsa, u32 voff, u32 soff) {
  asm volatile("s_mov_b32 m0, %0\n\ts_nop 0\n\tbuffer_load_dwordx4 %1, %2, %3 offen lds" ::"s"(ldsa), "v"(voff), "s"(rs), "s"(soff) : "memory");
}
struct P2 { u32 wa[2], hv, wv, hs[2], w2s, wos, ld; u32x4 rws, rw2, rwo; };
constexpr int p2_ops(int m) { return (m + HW < 28 ? 2 : 0) + (m < 18 ? 2 : 0); }
constexpr int p2_younger(int n) {
  int c = 0;
  if (n < HW) { c = 2 * (HW - 1 - n); for (int m = 0; m < n; ++m) c += p2_ops(m); }
  else { c = (n - HW < 18) ? 2 : 0; for (int m = n - HW + 1; m < n; ++m) c += p2_ops(m); }
  return c;
}
DI u32 opq(u32 x) { asm volatile("" : "+v"(x)); return x; }
template <int N> DI void p2a_step(const P2& c, f32x16 (&ac)[4], bf16x8 (&hw)[HW][2], bf16x8 (&wf)[2][2], int q, u32 b0, u32 b1) {
  asm volatile("s_waitcnt vmcnt(%2)" : "+v"(hw[N % HW][0]), "+v"(hw[N % HW][1]) : "n"(p2_younger(N)) : "memory");
  if constexpr (N == 11) lds_spin(L_PF + 4 * q, 18);
  if constexpr (N + 1 < 28) {
    constexpr u32 sl = (N + 1) % 18, o = (sl & 7) * 8192;
    if constexpr ((sl & 7) == 0 || sl == 0) { b0 = opq(c.wa[0] + (sl >> 3) * 65536); b1 = opq(c.wa[1] + (sl >> 3) * 65536); }
    wf[(N + 1) & 1][0] = ldtr(b0 + o, b0 + o + 1024);
    wf[(N + 1) & 1][1] = ldtr(b1 + o, b1 + o + 1024);
  }
  SB();
  ac[0] = mfma32(wf[N & 1][0], hw[N % HW][0], ac[0]);
  ac[1] = mfma32(wf[N & 1][1], hw[N % HW][0], ac[1]);
  ac[2] = mfma32(wf[N & 1][0], hw[N % HW][1], ac[2]);
  ac[3] = mfma32(wf[N & 1][1], hw[N % HW][1], ac[3]);
  SB();
  if constexpr (N + HW < 28) {
    hw[N % HW][0] = asm_gload(c.rws, c.hv, c.hs[0] + (N + HW) * 1024);
    hw[N % HW][1] = asm_gload(c.rws, c.hv, c.hs[1] + (N + HW) * 1024);
  }
  if constexpr (N < 10) {
    asm_dma(c.rw2, c.ld + N * 8192, c.wv, c.w2s + (N + 18) * 16384);
    asm_dma(c.rw2, c.ld + N * 8192 + 1024, c.wv, c.w2s + (N + 18) * 16384 + 8192);
  } else if constexpr (N < 18) {
    asm_dma(c.rwo, c.ld + N * 8192, c.wv, c.wos + (N - 10) * 16384);
    asm_dma(c.rwo, c.ld + N * 8192 + 1024, c.wv, c.wos + (N - 10) * 16384 + 8192);
  }
  SB();
  if constexpr (N + 1 < 28) p2a_step<N + 1>(c, ac, hw, wf, q, b0, b1);
}
template <int N> DI void p2b_step(f32x16 (&ac)[4], bf16x8 (&ow)[8][2], u32 b0, u32 b1) {
  constexpr u32 o = N * 8192;
  asm volatile("s_waitcnt vmcnt(%2)" : "+v"(ow[N][0]), "+v"(ow[N][1]) : "n"(2 * (7 - N)) : "memory");
  const bf16x8 w0 = ldtr(b0 + o, b0 + o + 1024), w1 = ldtr(b1 + o, b1 + o + 1024);
  SB();
  ac[0] = mfma32(w0, ow[N][0], ac[0]);
  ac[1] = mfma32(w1, ow[N][0], ac[1]);
  ac[2] = mfma32(w0, ow[N][1], ac[2]);
  ac[3] = mfma32(w1, ow[N][1], ac[3]);
  SB();
  if constexpr (N < 7) p2b_step<N + 1>(ac, ow, b0, b1);
}
DI void red_send(const f32x16 (&ac)[4], int q, int lane, int slot0) {
  const u32 b = opq(slot0 * 8192 + q * 2048 + lane * 16);
#pragma unroll
  for (int l = 1; l < 4; ++l)
#pragma unroll
    for (int g = 0; g < 4; ++g) {
      const f32x4 v = {ac[l][4 * g], ac[l][4 * g + 1], ac[l][4 * g + 2], ac[l][4 * g + 3]};
      *(LAS f32x4*)(lds + b + (2 * (l - 1) + (g >> 1)) * 8192 + (g & 1) * 1024) = v;
    }
}
DI void red_recv(f32x16& a, int q, int lane, int slot0) {
#pragma unroll
  for (int l = 1; l < 4; ++l) {
    const u32 b = opq(slot0 * 8192 + (q ^ l) * 2048 + lane * 16);
#pragma unroll
    for (int g = 0; g < 4; ++g) {
      const f32x4 v = ldf4(b + (2 * (l - 1) + (g >> 1)) * 8192 + (g & 1) * 1024);
#pragma unroll
      for (int e = 0; e < 4; ++e) a[4 * g + e] += v[e];
    }
  }
}

DI u64 compute_prologue(const Params& p, rsrc_t rws, int w, int lane, int i, int j) {
  const int row = 128 * i + 8 * j + w;
  const u32x4 xv = *(const GAS u32x4*)((const char*)p.x + row * 1024 + lane * 16);
  const u64 tag = __hip_atomic_load((GAS u64*)(p.ws + WS_E), RLX) + 1;
  stat_publish(p, rws, row, lane, tag, xv);
  return tag;
}

DI void roleA_main(const Params& p, int w, int lane, int i, int j, int i2, int j2) {
  STAMP_DECL;
  STAMP(20);
  const int rg = w, pr = j >> 1, isq = j & 1, odd = rg & 1;
  const int s = lane & 31, h = lane >> 5;
  const rsrc_t rws = mkrs(p.ws, WS_END);
  const u64 tag = compute_prologue(p, rws, w, lane, i, j);
  if (w == 0 && lane == 0) *(LAS u64*)(lds + L_TAG) = tag;
  const int t0 = (!isq && (pr & 1)) ? 5 : 4;
  const int tg[3] = {t0 + odd, t0 + 1 - odd, t0 + 2};
  P1<3> ad; ad.init(rg, lane, tg);
  f32x16 acc[3] = {};
  f32x4 cs0 = {}, cs2 = {};
  u32x4 sg;
  STAMP(0);
  p1_main(ad, acc, cs0, cs2, lane, rws, 128 * i + 32 * rg, sg);
  STAMP(1);
  p1_finish(cs0, cs2, tg[0], tg[2], !odd, lane);
  STAMP(2);
  if (PRIO == 0 || (PRIO == 1 && isq)) __builtin_amdgcn_s_setprio(1);
  float nmu, r;
  stat_get(rws, 128 * i + 32 * rg, lane, tag, sg, nmu, r);
  const float rl = r * LOG2E;
  const int Tb = 2 * h_unit0(pr, isq) - 4;
#pragma unroll
  for (int t = 0; t < 3; ++t) {
    float c16[16];
    u32x4 o[2];
    cs_get(c16, tg[t], lane);
    silu_tile(acc[t], c16, nmu, -rl, o);
    store_h(rws, i, rg, Tb + tg[t], lane, o);
  }
  STAMP(3);
  wait_vm0();
  if (arrive(C_H, lane) == (isq ? 7u : 3u)) set_flag(p.ws, WS_FH + (i * 16 + j) * 128, tag, lane);
  __builtin_amdgcn_s_setprio(0);
  const int q = rg, rb = q >> 1, cb = q & 1;
  const int orow = 64 * i2 + 32 * rb + s;
  const u32 ooff = orow * 1024 + (64 * j2 + 32 * cb) * 2 + h * 16;
  float nmu2, r2;
  stat_get(rws, 64 * i2 + 32 * rb, lane, tag, stat_load(rws, 64 * i2 + 32 * rb, lane), nmu2, r2);
  P2 c2;
  {
    const int blk = (lane >> 4) & 1, qq = (lane & 15) >> 2, pp = lane & 3;
    c2.wa[0] = q * 2048 + (4 * h + qq) * 128 + ((cb ^ (qq >> 1)) * 64) + 32 * blk + 8 * pp;
    c2.wa[1] = c2.wa[0] ^ 64;
    c2.hv = lane * 16;
    c2.wv = (lane >> 3) * 1024 + ((((lane & 7) >> 2) ^ ((lane >> 4) & 1)) * 64) + (lane & 3) * 16;
    c2.rws = srd(p.ws, WS_END); c2.rw2 = srd(p.w2, F_ * D_ * 2); c2.rwo = srd(p.wo, D_ * D_ * 2);
    c2.hs[0] = WS_H + ((2 * i2 + rb) * 112 + 28 * q) * 1024; c2.hs[1] = WS_H + ((2 * i2 + (rb ^ 1)) * 112 + 28 * q) * 1024;
    c2.w2s = 16 * 28 * q * 1024 + j2 * 128;
    c2.wos = 128 * q * 1024 + j2 * 128;
    c2.ld = ldsaddr(q * 2048);
  }
  f32x16 ac[4] = {};
  bf16x8 hw[HW][2], wf[2][2];
  STAMP(6);
  lds_spin(L_PF + 4 * q, 12);
  lds_spin(L_CNT + 4 * C_HR, 1);
  STAMP(7);
#pragma unroll
  for (int n = 0; n < HW; ++n) { hw[n][0] = asm_gload(c2.rws, c2.hv, c2.hs[0] + n * 1024); hw[n][1] = asm_gload(c2.rws, c2.hv, c2.hs[1] + n * 1024); }
  wf[0][0] = ldtr(c2.wa[0], c2.wa[0] + 1024); wf[0][1] = ldtr(c2.wa[1], c2.wa[1] + 1024);
  p2a_step<0>(c2, ac, hw, wf, q, c2.wa[0], c2.wa[1]);
  STAMP(8);
  red_send(ac, q, lane, 0);
  arrive(C_RG, lane);
  u32x4 xr[2];
#pragma unroll
  for (int u = 0; u < 2; ++u) xr[u] = *(const GAS u32x4*)((const char*)p.x + ooff + 32 * u);
  lds_spin(L_CNT + 4 * C_RG, 4);
  red_recv(ac[0], q, lane, 0);
#pragma unroll
  for (int g = 0; g < 4; ++g) {
    f32x4 v;
#pragma unroll
    for (int e = 0; e < 4; ++e) { const float gp = ac[0][4 * g + e] * r2; v[e] = gp * rcp(1.0f + ex2(-LOG2E * gp)); }
    *(LAS f32x4*)(lds + (6 + (g >> 1)) * 8192 + q * 2048 + (g & 1) * 1024 + lane * 16) = v;
  }
#pragma unroll
  for (int u = 0; u < 2; ++u) *(LAS u32x4*)(lds + 8 * 8192 + q * 2048 + u * 1024 + lane * 16) = xr[u];
  const u32 os0 = WS_O + ((2 * i2 + rb) * 32 + 8 * q) * 1024, os1 = WS_O + ((2 * i2 + (rb ^ 1)) * 32 + 8 * q) * 1024;
  lds_spin(L_CNT + 4 * C_OR, 1);
  STAMP(9);
  bf16x8 ow[8][2];
#pragma unroll
  for (int n = 0; n < 8; ++n) { ow[n][0] = asm_gload(c2.rws, c2.hv, os0 + n * 1024); ow[n][1] = asm_gload(c2.rws, c2.hv, os1 + n * 1024); }
#pragma unroll
  for (int l = 0; l < 4; ++l) ac[l] = f32x16{};
  p2b_step<0>(ac, ow, opq(c2.wa[0] + 10 * 8192), opq(c2.wa[1] + 10 * 8192));
  red_send(ac, q, lane, 10);
  arrive(C_RS, lane);
  lds_spin(L_CNT + 4 * C_RS, 4);
  red_recv(ac[0], q, lane, 10);
  const f32x16 as = ac[0];
  float G[16];
#pragma unroll
  for (int g = 0; g < 4; ++g) {
    const f32x4 v = ldf4((6 + (g >> 1)) * 8192 + q * 2048 + (g & 1) * 1024 + lane * 16);
    G[4 * g] = v[0]; G[4 * g + 1] = v[1]; G[4 * g + 2] = v[2]; G[4 * g + 3] = v[3];
  }
#pragma unroll
  for (int u = 0; u < 2; ++u) xr[u] = *(LAS u32x4*)(lds + 8 * 8192 + q * 2048 + u * 1024 + lane * 16);
  STAMP(10);
#pragma unroll
  for (int u = 0; u < 2; ++u) {
    u32x2 ra = __builtin_amdgcn_permlane32_swap(xr[u][0], xr[u][2], false, false);
    u32x2 rb_ = __builtin_amdgcn_permlane32_swap(xr[u][1], xr[u][3], false, false);
    u32 pk[2][2];
#pragma unroll
    for (int g = 0; g < 2; ++g) {
      const u32 x01 = ra[g], x23 = rb_[g];
      const int e = 8 * u + 4 * g;
      const float y0 = fmaf(as[e], G[e], u2f(x01 << 16)), y1 = fmaf(as[e + 1], G[e + 1], u2f(x01 & 0xffff0000u));
      const float y2 = fmaf(as[e + 2], G[e + 2], u2f(x23 << 16)), y3 = fmaf(as[e + 3], G[e + 3], u2f(x23 & 0xffff0000u));
      pk[g][0] = pk2(y0, y1); pk[g][1] = pk2(y2, y3);
    }
    u32x2 s0 = __builtin_amdgcn_permlane32_swap(pk[0][0], pk[1][0], false, false);
    u32x2 s1 = __builtin_amdgcn_permlane32_swap(pk[0][1], pk[1][1], false, false);
    u32x4 v = {s0[0], s1[0], s0[1], s1[1]};
    *(GAS u32x4*)((char*)p.out + ooff + 32 * u) = v;
  }
  STAMP(11);
  STAMP_DUMP;
}

DI void roleB_kv(const Params& p, int w, int lane, int i, int j) {
  STAMP_DECL;
  const int rg = w - 4, pr = j >> 1, odd = rg & 1;
  const int s = lane & 31, h = lane >> 5;
  bf16x8 a0[2], a1[2];
  load_wphi(p, 4 * pr + 2 * odd, lane, a0[0], a1[0]);
  load_wphi(p, 4 * pr + 2 - 2 * odd, lane, a0[1], a1[1]);
  const int tg[4] = {odd, 1 - odd, 2 + odd, 3 - odd};
  const rsrc_t rws = mkrs(p.ws, WS_END);
  const u64 tag = compute_prologue(p, rws, w, lane, i, j);
  P1<4> ad; ad.init(rg, lane, tg);
  f32x16 acc[4] = {};
  f32x4 cs0 = {}, cs2 = {};
  u32x4 sg;
  STAMP(0);
  p1_main(ad, acc, cs0, cs2, lane, rws, 128 * i + 32 * rg, sg);
  STAMP(1);
  p1_finish(cs0, cs2, tg[0], tg[2], true, lane);
  STAMP(2);
  if (PRIO >= 1) __builtin_amdgcn_s_setprio(1);
  float nmu, r;
  stat_get(rws, 128 * i + 32 * rg, lane, tag, sg, nmu, r);
  const float rl = r * LOG2E;
  const u32 ib = (32 * rg + s) * 64 + ((h ^ ((s >> 1) & 7)) * 8);
  const u32 ia[4] = {ib, ib ^ 16, ib ^ 32, ib ^ 48};
#pragma unroll
  for (int t = 0; t < 2; ++t) {
    float c16[16];
    cs_get(c16, tg[t], lane);
    const f32x16 pl = phi_logits(acc[t], c16, nmu, a0[t], a1[t]);
#pragma unroll
    for (int hh = 0; hh < 2; ++hh) {
      float ep_[8], en_[8], zp, zn;
      phi_exp(pl, hh, rl, ep_, en_, zp, zn);
      const float ip = rcp(zp), in = rcp(zn);
      const u32 base = L_PHI + (2 * tg[t] + hh) * 8192;
#pragma unroll
      for (int g = 0; g < 2; ++g) {
        u32x2 vp = {pk2(ep_[4 * g] * ip, ep_[4 * g + 1] * ip), pk2(ep_[4 * g + 2] * ip, ep_[4 * g + 3] * ip)};
        u32x2 vn = {pk2(en_[4 * g] * in, en_[4 * g + 1] * in), pk2(en_[4 * g + 2] * in, en_[4 * g + 3] * in)};
        *(LAS u32x2*)(lds + base + ia[g]) = vp;
        *(LAS u32x2*)(lds + base + ia[2 + g]) = vn;
      }
    }
  }
#pragma unroll
  for (int t = 0; t < 2; ++t) {
    float c16[16];
    cs_get(c16, tg[2 + t], lane);
    const u32 base = L_V + (tg[2 + t] - 2) * 8192;
#pragma unroll
    for (int g = 0; g < 4; ++g) {
      float v[4];
#pragma unroll
      for (int e = 0; e < 4; ++e) v[e] = fmaf(nmu, c16[4 * g + e], acc[2 + t][4 * g + e]) * r;
      u32x2 vv = {pk2(v[0], v[1]), pk2(v[2], v[3])};
      *(LAS u32x2*)(lds + base + ia[g]) = vv;
    }
  }
  arrive(C_PHI, lane);
  STAMP(3);
  lds_spin(L_CNT + 4 * C_PHI, 4);
  {
    const int blk = (lane >> 4) & 1, q = (lane & 15) >> 2, pp = lane & 3;
    const int r0 = 8 * h + q, r1 = r0 + 4;
    const u32 pa0 = L_PHI + rg * 8192 + r0 * 64 + (((4 * blk + pp) ^ ((r0 >> 1) & 7)) * 8);
    const u32 pa1 = L_PHI + rg * 8192 + r1 * 64 + (((4 * blk + pp) ^ ((r1 >> 1) & 7)) * 8);
    const u32 vb = L_V + (rg >> 1) * 8192;
    const u32 pv0 = vb + r0 * 64 + (((4 * (rg & 1) + pp) ^ ((r0 >> 1) & 7)) * 8);
    const u32 pv1 = vb + r1 * 64 + (((4 * (rg & 1) + pp) ^ ((r1 >> 1) & 7)) * 8);
    const bf16x8 ones = asfrag(0x3f803f80u, 0x3f803f80u, 0x3f803f80u, 0x3f803f80u);
    f32x16 d = {};
#pragma unroll
    for (int u = 0; u < 8; ++u) {
      const bf16x8 vv = ldtr(pv0 + u * 1024, pv1 + u * 1024);
      d = mfma32(ldtr(pa0 + u * 1024, pa1 + u * 1024), blk ? ones : vv, d);
    }
    asm volatile("" :: "v"(d));
    arrive(C_IMG, lane);
    if (s <= 16) {
#pragma unroll
      for (int u = 0; u < 2; ++u) {
        u32x4 v = {pk2(d[8 * u], d[8 * u + 1]), pk2(d[8 * u + 2], d[8 * u + 3]), pk2(d[8 * u + 4], d[8 * u + 5]), pk2(d[8 * u + 6], d[8 * u + 7])};
        st128_sc1(rws, v, (2 * s + h) * 16, WS_KV + (((pr * 16 + i) * 4 + rg) * 2 + u) * 544);
      }
    }
  }
  STAMP(4);
  wait_vm0();
  if (arrive(C_KV, lane) == 3) set_flag(p.ws, WS_FK + (pr * 16 + i) * 128, tag, lane);
  STAMP(5);
  STAMP_DUMP;
}

DI void roleB_q(const Params& p, int w, int lane, int i, int j) {
  STAMP_DECL;
  const int rg = w - 4, pr = j >> 1, odd = rg & 1;
  const int s = lane & 31, h = lane >> 5;
  bf16x8 a0[2], a1[2];
  load_wphi(p, 4 * pr + 2 * odd, lane, a0[0], a1[0]);
  load_wphi(p, 4 * pr + 2 - 2 * odd, lane, a0[1], a1[1]);
  const int tg[3] = {odd, 1 - odd, 7};
  const rsrc_t rws = mkrs(p.ws, WS_END);
  const u64 tag = compute_prologue(p, rws, w, lane, i, j);
  P1<3> ad; ad.init(rg, lane, tg);
  f32x16 acc[3] = {};
  f32x4 cs0 = {}, cs2 = {};
  u32x4 sg;
  STAMP(0);
  p1_main(ad, acc, cs0, cs2, lane, rws, 128 * i + 32 * rg, sg);
  STAMP(1);
  p1_finish(cs0, cs2, tg[0], tg[2], !odd, lane);
  STAMP(2);
  float nmu, r;
  stat_get(rws, 128 * i + 32 * rg, lane, tag, sg, nmu, r);
  const float rl = r * LOG2E;
  {
    float c16[16];
    u32x4 o[2];
    cs_get(c16, 7, lane);
    silu_tile(acc[2], c16, nmu, -rl, o);
    store_h(rws, i, rg, 2 * h_unit0(pr, 1) + 3, lane, o);
  }
  STAMP(3);
  bf16x8 bq[4][2];
  float zeps[4];
#pragma unroll
  for (int t = 0; t < 2; ++t) {
    float c16[16];
    cs_get(c16, tg[t], lane);
    const f32x16 pl = phi_logits(acc[t], c16, nmu, a0[t], a1[t]);
#pragma unroll
    for (int hh = 0; hh < 2; ++hh) {
      float ep_[8], en_[8], zp, zn;
      phi_exp(pl, hh, rl, ep_, en_, zp, zn);
      const float rho = zp * rcp(zn);
      bq[2 * t + hh][0] = asfrag(pk2(ep_[0], ep_[1]), pk2(ep_[2], ep_[3]), pk2(ep_[4], ep_[5]), pk2(ep_[6], ep_[7]));
      bq[2 * t + hh][1] = asfrag(pk2(en_[0] * rho, en_[1] * rho), pk2(en_[2] * rho, en_[3] * rho), pk2(en_[4] * rho, en_[5] * rho), pk2(en_[6] * rho, en_[7] * rho));
      zeps[2 * t + hh] = zp * 1e-6f;
    }
    if (t == 0) {
      wait_vm0();
      if (arrive(C_H, lane) == 7) set_flag(p.ws, WS_FH + (i * 16 + j) * 128, tag, lane);
    }
  }
  STAMP(4);
  poll16(p.ws, WS_FK + pr * 16 * 128, 128, tag, lane, 15);
  STAMP(5);
  {
    u32x4 pv[2][8];
    u32x2 pz[2];
#pragma unroll
    for (int u = 0; u < 2; ++u) {
      const u32 pbase = WS_KV + ((pr * 16 * 4 + rg) * 2 + u) * 544;
#pragma unroll
      for (int m = 0; m < 8; ++m)
        pv[u][m] = __builtin_amdgcn_raw_buffer_load_b128(rws, ((lane >> 5) * 8 * 544) + (lane & 31) * 16, pbase + 2 * m * 8 * 544, 16);
      pz[u] = __builtin_amdgcn_raw_buffer_load_b64(rws, (lane >> 2) * 8 * 544 + 512 + (lane & 3) * 8, pbase, 16);
    }
    u32 ps[4];
#pragma unroll
    for (int e = 0; e < 4; ++e) ps[e] = (s == 2 * e ? 0x00003f80u : 0u) | (s == 2 * e + 1 ? 0x3f800000u : 0u);
    const bf16x8 psel = asfrag(ps[0], ps[1], ps[2], ps[3]);
#pragma unroll
    for (int u = 0; u < 2; ++u) {
      f32x16 sm = {};
#pragma unroll
      for (int m = 0; m < 8; ++m) sm = mfma32(psel, __builtin_bit_cast(bf16x8, pv[u][m]), sm);
      float zz[4] = {u2f(pz[u][0] << 16), u2f(pz[u][0] & 0xffff0000u), u2f(pz[u][1] << 16), u2f(pz[u][1] & 0xffff0000u)};
#pragma unroll
      for (int e = 0; e < 4; ++e) {
        float v = zz[e];
        v += u2f(__builtin_amdgcn_update_dpp(0, (int)f2u(v), 0x128, 0xf, 0xf, false));
        v += u2f(__builtin_amdgcn_update_dpp(0, (int)f2u(v), 0x124, 0xf, 0xf, false));
        u32x2 t = __builtin_amdgcn_permlane16_swap(f2u(v), f2u(v), false, false);
        v = u2f(t[0]) + u2f(t[1]);
        zz[e] = xsum(v);
      }
      const u32 kb = L_KVA + (rg * 2 + u) * 544;
      { u32x2 v = {pk2(sm[0], sm[1]), pk2(sm[2], sm[3])}; *(LAS u32x2*)(lds + kb + s * 16 + h * 8) = v; }
      if (lane < 4) { u32x2 v = {pk2(zz[0], zz[1]), pk2(zz[2], zz[3])}; *(LAS u32x2*)(lds + kb + 512 + lane * 8) = v; }
    }
  }
  arrive(C_CH, lane);
  lds_spin(L_CNT + 4 * C_CH, 4);
  STAMP(6);
#pragma unroll
  for (int hh = 0; hh < 4; ++hh) {
    const u32 ao = (s < 16 ? s : 16) * 32 + h * 16;
    f32x16 d = {};
    const int ah = hh ^ (2 * odd);
    d = mfma32(ld128(L_KVA + (ah * 2) * 544 + ao), bq[hh][0], d);
    d = mfma32(ld128(L_KVA + (ah * 2 + 1) * 544 + ao), bq[hh][1], d);
    const float inv = rcp(d[8] + zeps[hh]);
    u32x4 v = {pk2(d[0] * inv, d[1] * inv), pk2(d[2] * inv, d[3] * inv), pk2(d[4] * inv, d[5] * inv), pk2(d[6] * inv, d[7] * inv)};
    st128_sc1(rws, v, lane * 16, WS_O + ((4 * i + rg) * 32 + 4 * pr + ah) * 1024);
  }
  STAMP(7);
  wait_vm0();
  if (arrive(C_O, lane) == 3) set_flag(p.ws, WS_FO + (i * 8 + pr) * 128, tag, lane);
  STAMP(8);
  STAMP_DUMP;
}

__global__ void __launch_bounds__(768, 3) fused_hedgehog_ffn(Params p) {
  const int tid = threadIdx.x, lane = tid & 63;
  const int w = __builtin_amdgcn_readfirstlane(tid >> 6);
  const int b = blockIdx.x, g = b & 7, m = b >> 3;
#define MAP 2
  const int i = MAP == 0 ? 4 * (g >> 1) + (m & 3) : MAP == 1 ? 2 * g + (m & 1) : MAP == 2 ? 8 * (g >> 2) + (m & 7) : (b >> 4);
  const int j = MAP == 0 ? 8 * (g & 1) + (m >> 2) : MAP == 1 ? (m >> 1) : MAP == 2 ? 4 * (g & 3) + (m >> 3) : (b & 15);
  if (w == 4) {
    *(LAS u32*)(lds + L_CNT + 4 * (lane & 31)) = 0;
#pragma unroll
    for (int q = 0; q < 4; ++q) *(LAS u32*)(lds + L_CS + 4 * (lane + 64 * q)) = 0;
    wait_lgkm0();
  }
#define MAP2 1
  const int i2 = MAP2 == 0 ? 2 * i + (j & 1) : 8 * (g >> 1) + (m & 7), j2 = MAP2 == 0 ? j >> 1 : 4 * (g & 1) + (m >> 3);
  if (w >= 8) loader_main(p, w, lane, i, j, i2, j2);
  else if (w < 4) roleA_main(p, w, lane, i, j, i2, j2);
  else if (j & 1) roleB_q(p, w, lane, i, j);
  else roleB_kv(p, w, lane, i, j);
}

extern "C" void kernel_launch(void* const* d_in, const int* in_sizes, int n_in, void* d_out, int out_size, void* d_ws, size_t ws_size,
                              hipStream_t stream) {
  Params p;
  p.x = (const u16*)d_in[0]; p.wqkv = (const u16*)d_in[1]; p.wphi = (const u16*)d_in[2]; p.wo = (const u16*)d_in[3];
  p.w1 = (const u16*)d_in[5]; p.w2 = (const u16*)d_in[6]; p.out = (u16*)d_out; p.ws = (char*)d_ws;
  fused_hedgehog_ffn<<<256, 768, 0, stream>>>(p);
}
```

```cpp
#define PROBE 21
#include <hip/hip_runtime.h>

typedef unsigned short u16;
typedef unsigned u32;
typedef unsigned long long u64;
typedef short bf16x8 __attribute__((ext_vector_type(8)));
typedef short s16x4 __attribute__((ext_vector_type(4)));
typedef __bf16 bf2 __attribute__((ext_vector_type(2)));
typedef float f32x16 __attribute__((ext_vector_type(16)));
typedef float f32x4 __attribute__((ext_vector_type(4)));
typedef u32 u32x4 __attribute__((ext_vector_type(4)));
typedef u32 u32x2 __attribute__((ext_vector_type(2)));
#define LAS __attribute__((address_space(3)))
#define GAS __attribute__((address_space(1)))
#define DI __device__ __forceinline__
#define RLX __ATOMIC_RELAXED, __HIP_MEMORY_SCOPE_AGENT
#define RLXW __ATOMIC_RELAXED, __HIP_MEMORY_SCOPE_WORKGROUP

#define PRIO 1
#define TIMING 0

constexpr int S_ = 2048, D_ = 512, F_ = 1792;
constexpr float LOG2E = 1.4426950408889634f;

constexpr u32 WS_E = 0;
constexpr u32 WS_FH = 4096;
constexpr u32 WS_FK = WS_FH + 32768;
constexpr u32 WS_FO = WS_FK + 32768;
constexpr u32 WS_ST = 102400;
constexpr u32 WS_KV = 262144;
constexpr u32 WS_H = 1u << 20;
constexpr u32 WS_O = WS_H + 64u * 112u * 1024u;
constexpr u32 WS_END = WS_O + 64u * 32u * 1024u;
constexpr u32 WS_TM = 16u << 20;

constexpr u32 W1R = 0, NW1 = 5;
constexpr u32 X1R = 81920, NX1 = 4;
constexpr u32 L_PHI = 98304, L_V = 131072;
constexpr u32 L_KVA = 147456;
constexpr u32 MISC = 157696;
constexpr u32 L_CS = MISC + 1024;
constexpr u32 L_CNT = MISC + 2048, L_TAG = MISC;
constexpr u32 L_PF = L_CNT + 64;
enum { C_H = 0, C_PHI = 1, C_KV = 2, C_CH = 3, C_O = 4, C_HR = 5, C_OR = 6, C_IMG = 7, C_RG = 8, C_RS = 9 };
constexpr u32 SPIN_CAP = 1u << 22;

__shared__ __attribute__((aligned(1024))) u32 lds32[40960];
#define lds ((char*)lds32)

DI f32x16 mfma32(bf16x8 a, bf16x8 b, f32x16 c) { return __builtin_amdgcn_mfma_f32_32x32x16_bf16(a, b, c, 0, 0, 0); }
DI u32 pk2(float a, float b) { bf2 v = {(__bf16)a, (__bf16)b}; return __builtin_bit_cast(u32, v); }
DI float dot2(u32 a, u32 b, float c) { return __builtin_amdgcn_fdot2_f32_bf16(__builtin_bit_cast(bf2, a), __builtin_bit_cast(bf2, b), c, false); }
DI float ex2(float x) { return __builtin_amdgcn_exp2f(x); }
DI float rcp(float x) { return __builtin_amdgcn_rcpf(x); }
DI float fmax2(float a, float b) { return __builtin_elementwise_maximum(a, b); }
DI float fmin2(float a, float b) { return __builtin_elementwise_minimum(a, b); }
DI u32 f2u(float x) { return __builtin_bit_cast(u32, x); }
DI float u2f(u32 x) { return __builtin_bit_cast(float, x); }
DI bf16x8 ld128(u32 off) { return *(LAS bf16x8*)(lds + off); }
DI f32x4 ldf4(u32 off) { return *(LAS f32x4*)(lds + off); }
DI float ldf(u32 off) { return *(LAS float*)(lds + off); }
DI void stf(u32 off, float v) { *(LAS float*)(lds + off) = v; }
DI bf16x8 ldtr(u32 off0, u32 off1) {
  s16x4 a = __builtin_amdgcn_ds_read_tr16_b64_v4i16((LAS s16x4*)(lds + off0));
  s16x4 b = __builtin_amdgcn_ds_read_tr16_b64_v4i16((LAS s16x4*)(lds + off1));
  return __builtin_shufflevector(a, b, 0, 1, 2, 3, 4, 5, 6, 7);
}
DI bf16x8 asfrag(u32 a, u32 b, u32 c, u32 d) { u32x4 v = {a, b, c, d}; return __builtin_bit_cast(bf16x8, v); }
DI void barrier() { asm volatile("s_barrier" ::: "memory"); }
DI void wait_lgkm0() { asm volatile("s_waitcnt lgkmcnt(0)" ::: "memory"); }
DI void wait_vm0() { asm volatile("s_waitcnt vmcnt(0)" ::: "memory"); }
template <int N> DI void wait_vm() { asm volatile("s_waitcnt vmcnt(%0)" ::"n"(N) : "memory"); }
DI float xsum(float x) { u32x2 r = __builtin_amdgcn_permlane32_swap(f2u(x), f2u(x), false, false); return u2f(r[0]) + u2f(r[1]); }
DI void xmaxmin(float& mx, float& mn) {
  u32x2 r = __builtin_amdgcn_permlane32_swap(f2u(mx), f2u(-mn), false, false);
  float m = fmax2(u2f(r[0]), u2f(r[1]));
  u32x2 q = __builtin_amdgcn_permlane32_swap(f2u(m), f2u(m), false, false);
  mx = u2f(q[0]); mn = -u2f(q[1]);
}
DI void xsum2(float& a, float& b) {
  u32x2 r = __builtin_amdgcn_permlane32_swap(f2u(a), f2u(b), false, false);
  float m = u2f(r[0]) + u2f(r[1]);
  u32x2 q = __builtin_amdgcn_permlane32_swap(f2u(m), f2u(m), false, false);
  a = u2f(q[0]); b = u2f(q[1]);
}
DI void lds_st(u32 off, u32 v) { __hip_atomic_store((LAS u32*)&lds32[off >> 2], v, RLXW); }
DI u32 ldsaddr(u32 off) { return (u32)(size_t)(LAS char*)lds + off; }
DI u32x4 asm_ld128(u32 off) {
  u32x4 v;
  asm volatile("ds_read_b128 %0, %1\n\ts_waitcnt lgkmcnt(0)" : "=&v"(v) : "v"(ldsaddr(off)) : "memory");
  return v;
}
DI void asm_st32(u32 off, u32 val) { asm volatile("ds_write_b32 %0, %1" ::"v"(ldsaddr(off)), "v"(val) : "memory"); }
DI u32 asm_ld32(u32 off) {
  u32 v;
  asm volatile("ds_read_b32 %0, %1\n\ts_waitcnt lgkmcnt(0)" : "=&v"(v) : "v"(ldsaddr(off)) : "memory");
  return v;
}
DI void lds_spin(u32 off, u32 target) {
#pragma unroll 1
  for (u32 it = 0; it < SPIN_CAP; ++it) {
    if (__builtin_amdgcn_readfirstlane(asm_ld32(off)) >= target) break;
    __builtin_amdgcn_s_sleep(1);
  }
}
DI u32 lds_add_rtn(u32 off) { return __hip_atomic_fetch_add((LAS u32*)&lds32[off >> 2], 1u, RLXW); }
DI u32 lds_ld(u32 off) { return __hip_atomic_load((LAS u32*)&lds32[off >> 2], RLXW); }
DI u32 arrive(int cnt, int lane) {
  asm volatile("" ::: "memory");
  u32 old = 0;
  if (lane == 0) old = lds_add_rtn(L_CNT + 4 * cnt);
  old = __builtin_amdgcn_readfirstlane(old);
  asm volatile("" ::: "memory");
  return old;
}
DI void poll16(const char* ws, u32 base, u32 stride, u64 tag, int lane, int mask) {
  GAS u64* p = (GAS u64*)(ws + base + (u32)(lane & mask) * stride);
#pragma unroll 1
  for (u32 it = 0; it < SPIN_CAP; ++it) {
    u64 v = __hip_atomic_load(p, RLX);
    if (__all(v == tag)) break;
    __builtin_amdgcn_s_sleep(1);
  }
  asm volatile("" ::: "memory");
}
DI void set_flag(char* ws, u32 off, u64 tag, int lane) {
  if (lane == 0) __hip_atomic_store((GAS u64*)(ws + off), tag, RLX);
}

#define STAMP(k) do { if ((k) == PROBE) { __builtin_amdgcn_sched_barrier(0); probe_t = __builtin_amdgcn_s_memrealtime(); __builtin_amdgcn_s_waitcnt(0xC07F); __builtin_amdgcn_sched_barrier(0); } } while (0)
#define STAMP_DECL u64 probe_t = 0
#define STAMP_DUMP do { if (w < 4) { while (__builtin_amdgcn_s_memrealtime() < probe_t + 6000) __builtin_amdgcn_s_sleep(2); } } while (0)

struct Params { const u16* x; const u16* wqkv; const u16* wphi; const u16* wo; const u16* w1; const u16* w2; u16* out; char* ws; };

typedef __amdgpu_buffer_rsrc_t rsrc_t;
DI rsrc_t mkrs(const void* p, u32 bytes) { return __builtin_amdgcn_make_buffer_rsrc((void*)p, 0, bytes, 0x00020000); }
DI void st128_sc1(rsrc_t rs, u32x4 v, u32 voff, u32 soff) { __builtin_amdgcn_raw_buffer_store_b128(v, rs, voff + soff, 0, 16); }
template <int AUX> DI void dma16(rsrc_t rs, u32 ldsoff, u32 voff, u32 soff) {
  __builtin_amdgcn_raw_ptr_buffer_load_lds(rs, (LAS void*)(lds + ldsoff), 16, voff, soff, 0, AUX);
}

DI int h_unit0(int p, int isq) { return isq ? ((p & 1) ? (7 * p + 3) >> 1 : (7 * p) >> 1) : ((p & 1) ? (7 * p - 1) >> 1 : (7 * p + 4) >> 1); }

DI void loader_main(const Params& p, int w, int lane, int i, int j, int i2, int j2) {
  STAMP_DECL;
  const int wl = w - 8, pr = j >> 1, isq = j & 1;
  const rsrc_t rx = mkrs(p.x, S_ * D_ * 2), rq = mkrs(p.wqkv, D_ * 1536 * 2), r1 = mkrs(p.w1, D_ * F_ * 2);
  const u32 gsw = ((((lane & 7) >> 2) ^ ((lane >> 4) & 1)) * 64) + (lane & 3) * 16;
  const u32 xv = (lane >> 3) * 1024 + (((lane & 7) ^ ((4 * (wl & 1) + (lane >> 4)) & 7)) * 16);
  const u32 qv = (lane >> 3) * 3072 + gsw, hv1 = (lane >> 3) * 3584 + gsw;
  const u32 xs = (128 * i + 8 * wl) * 1024;
  const u32 s0 = (isq ? pr * 128 : 1024 + pr * 128) + 8 * wl * 3072, s1 = 2048 + pr * 128 + 8 * wl * 3072;
  const u32 h0 = h_unit0(pr, isq) * 128 + 8 * wl * 3584;
  auto issue_w = [&](int c, u32 slot) {
    const u32 d = slot + wl * 1024;
    dma16<0>(rq, d, qv, s0 + c * (32 * 3072));
    if (!isq) dma16<0>(rq, d + 4096, qv, s1 + c * (32 * 3072));
    dma16<0>(r1, d + 8192, hv1, h0 + c * (32 * 3584));
    dma16<0>(r1, d + 12288, hv1, h0 + 128 + c * (32 * 3584));
  };
  auto issue_x = [&](int P, int half) {
    const u32 d = X1R + (P & 3) * 16384 + (wl + 8 * half) * 1024;
    dma16<0>(rx, d, xv, xs + half * 65536 + P * 128);
    dma16<0>(rx, d + 4096, xv, xs + half * 65536 + 32768 + P * 128);
  };
  STAMP(0);
  issue_x(0, 0); issue_x(0, 1);
  issue_w(0, 0); issue_w(1, 16384);
  issue_x(1, 0); issue_x(1, 1);
  issue_w(2, 2 * 16384);
  STAMP(1);
  if (isq) wait_vm<7>(); else wait_vm<8>();
  barrier();
  issue_w(3, 3 * 16384);
  issue_x(2, 0); issue_x(2, 1);
  issue_w(4, 4 * 16384);
  if (isq) wait_vm<10>(); else wait_vm<12>();
  barrier();
  issue_w(5, 0);
  issue_x(3, 0); issue_x(3, 1);
  u32 wslot = 16384;
#pragma unroll 1
  for (int c = 2; c < 16; ++c) {
    if (isq) {
      if (c <= 11) wait_vm<10>(); else if (c == 12) wait_vm<6>(); else if (c == 13) wait_vm<3>(); else wait_vm<0>();
    } else {
      if (c <= 11) wait_vm<12>(); else if (c == 12) wait_vm<8>(); else if (c == 13) wait_vm<4>(); else wait_vm<0>();
    }
    barrier();
    if (c <= 11) issue_w(c + 4, wslot);
    if (c <= 9) issue_x((c + 6) >> 1, c & 1);
    wslot = wslot == 4 * 16384 ? 0 : wslot + 16384;
  }
  STAMP(2);
  barrier();
  STAMP(3);
  const rsrc_t rw2 = mkrs(p.w2, F_ * D_ * 2);
  const u32 wv = (lane >> 3) * 1024 + gsw, wsrc = 16 * 28 * wl * 1024 + j2 * 128;
  auto issue_w2 = [&](int n) {
    dma16<0>(rw2, n * 8192 + wl * 2048, wv, wsrc + n * 16384);
    dma16<0>(rw2, n * 8192 + wl * 2048 + 1024, wv, wsrc + n * 16384 + 8192);
  };
#pragma unroll 1
  for (int n = 0; n < 12; ++n) issue_w2(n);
  wait_vm0();
  asm_st32(L_PF + 4 * wl, 12);
  if (!isq) lds_spin(L_CNT + 4 * C_IMG, 4);
#pragma unroll 1
  for (int n = 12; n < 18; ++n) issue_w2(n);
  wait_vm0();
  asm_st32(L_PF + 4 * wl, 18);
  STAMP(4);
  if (wl == 0) {
    const u32x4 tg = asm_ld128(L_TAG);
    const u64 tag = (u64)tg[0] | ((u64)tg[1] << 32);
    poll16(p.ws, WS_FH + (i2 >> 1) * 16 * 128, 128, tag, lane, 15);
    asm_st32(L_CNT + 4 * C_HR, 1);
    STAMP(5);
    poll16(p.ws, WS_FO + (i2 >> 1) * 8 * 128, 128, tag, lane, 7);
    asm_st32(L_CNT + 4 * C_OR, 1);
    STAMP(6);
    if (blockIdx.x == 0) {
      poll16(p.ws, WS_FO, 128, tag, lane, 63);
      poll16(p.ws, WS_FO + 64 * 128, 128, tag, lane, 63);
      if (lane == 0) __hip_atomic_store((GAS u64*)(p.ws + WS_E), tag, RLX);
    }
  }
  STAMP_DUMP;
}

template <int NT> struct Frag { bf16x8 x; bf16x8 w[NT]; };

template <int NT> struct P1 {
  u32 xa[4];
  u32 wa[NT];
  DI void init(int rg, int lane, const int (&tg)[NT]) {
    const int s = lane & 31, h = lane >> 5, blk = (lane >> 4) & 1, q = (lane & 15) >> 2, pp = lane & 3, hi = rg >> 1;
#pragma unroll
    for (int k = 0; k < 4; ++k) xa[k] = X1R + (((32 * rg + s) * 128 + ((h ^ ((s >> 1) & 7)) * 16)) ^ ((k ^ hi) << 5));
#pragma unroll
    for (int t = 0; t < NT; ++t)
      wa[t] = W1R + (tg[t] >> 1) * 4096 + hi * 2048 + (8 * h + q) * 128 + (((tg[t] & 1) ^ (q >> 1)) * 64) + 32 * blk + 8 * pp;
  }
};
#define SB() __builtin_amdgcn_sched_barrier(0)
template <int NT> struct Addr { u32 w[NT]; };
template <int NT> DI Addr<NT> waddr(const P1<NT>& a, u32 slot) {
  Addr<NT> r;
#pragma unroll
  for (int t = 0; t < NT; ++t) r.w[t] = a.wa[t] + slot;
  return r;
}

template <int NT, bool CS, bool RELOAD>
DI void p1_step(Frag<NT>& f, f32x16 (&acc)[NT], bf16x8 sel, f32x4& cs0, f32x4& cs2, const Addr<NT>& wa, u32 xa, int ks) {
#pragma unroll
  for (int t = 0; t < NT; ++t) {
    acc[t] = mfma32(f.w[t], f.x, acc[t]);
    if constexpr (CS) {
      if (t == 0) cs0 = __builtin_amdgcn_mfma_f32_16x16x32_bf16(sel, f.w[0], cs0, 0, 0, 0);
      if (t == 2) cs2 = __builtin_amdgcn_mfma_f32_16x16x32_bf16(sel, f.w[2], cs2, 0, 0, 0);
    }
    SB();
    if constexpr (RELOAD) {
      const u32 ad = wa.w[t] ^ (ks << 11);
      f.w[t] = ldtr(ad, ad + 512);
      if (t == NT - 1) f.x = ld128(xa);
    }
    SB();
  }
}
template <int NT, bool LAST>
DI void p1_pair(const P1<NT>& a, Frag<NT>& f0, Frag<NT>& f1, f32x16 (&acc)[NT], bf16x8 sel, f32x4& cs0, f32x4& cs2, u32 w1, u32 w2, u32 xsl, u32 xn) {
  {
    const Addr<NT> wa = waddr(a, w1);
    p1_step<NT, true, true>(f0, acc, sel, cs0, cs2, wa, a.xa[2] + xsl, 0);
    p1_step<NT, false, true>(f1, acc, sel, cs0, cs2, wa, a.xa[3] + xsl, 1);
  }
  barrier();
  {
    const Addr<NT> wa = waddr(a, w2);
    p1_step<NT, true, !LAST>(f0, acc, sel, cs0, cs2, wa, a.xa[0] + xn, 0);
    p1_step<NT, false, !LAST>(f1, acc, sel, cs0, cs2, wa, a.xa[1] + xn, 1);
  }
  if constexpr (!LAST) barrier();
}
struct Stat { u32x4 g; };
DI u32x4 stat_load(rsrc_t rws, int row0, int lane) { return __builtin_amdgcn_raw_buffer_load_b128(rws, (lane & 31) * 16, WS_ST + row0 * 16, 16); }
DI void stat_get(rsrc_t rws, int row0, int lane, u64 tag, u32x4 g, float& nmu, float& r) {
#pragma unroll 1
  for (u32 it = 0; it < SPIN_CAP; ++it) {
    if (__all(g[1] == (u32)tag && g[3] == (u32)(tag >> 32))) break;
    __builtin_amdgcn_s_sleep(1);
    g = stat_load(rws, row0, lane);
  }
  nmu = -u2f(g[0]); r = u2f(g[2]);
}
DI void stat_publish(const Params& p, rsrc_t rws, int row, int lane, u64 tag, u32x4 xv) {
  float s0 = 0.f, s1 = 0.f, q0 = 0.f, q1 = 0.f;
#pragma unroll
  for (int e = 0; e < 4; ++e) {
    const float lo = u2f(xv[e] << 16), hi = u2f(xv[e] & 0xffff0000u);
    s0 += lo; s1 += hi; q0 = fmaf(lo, lo, q0); q1 = fmaf(hi, hi, q1);
  }
  float s = s0 + s1, q = q0 + q1;
#define DPP_ADD(v, ctrl) v += u2f(__builtin_amdgcn_update_dpp(0, (int)f2u(v), ctrl, 0xf, 0xf, false))
  DPP_ADD(s, 0x128); DPP_ADD(q, 0x128); DPP_ADD(s, 0x124); DPP_ADD(q, 0x124);
  DPP_ADD(s, 0x122); DPP_ADD(q, 0x122); DPP_ADD(s, 0x121); DPP_ADD(q, 0x121);
#undef DPP_ADD
  { u32x2 a = __builtin_amdgcn_permlane16_swap(f2u(s), f2u(q), false, false);
    float m = u2f(a[0]) + u2f(a[1]);
    u32x2 b = __builtin_amdgcn_permlane16_swap(f2u(m), f2u(m), false, false);
    s = u2f(b[0]); q = u2f(b[1]); }
  xsum2(s, q);
  const float mu = s * (1.0f / D_);
  const float var = fmaf(-mu, mu, q * (1.0f / D_));
  const float r = __builtin_amdgcn_rsqf(fmax2(var, 0.f) + 1e-5f);
  if (lane == 0) {
    const u32x4 g = {f2u(mu), (u32)tag, f2u(r), (u32)(tag >> 32)};
    st128_sc1(rws, g, 0, WS_ST + row * 16);
  }
}
template <int NT> DI void p1_main(const P1<NT>& a, f32x16 (&acc)[NT], f32x4& cs0, f32x4& cs2, int lane, rsrc_t rws, int row0, u32x4& sg) {
  Frag<NT> f0, f1;
  const u32 one = ((lane & 15) == ((lane >> 4) & 1)) ? 0x3f803f80u : 0u;
  const bf16x8 sel = asfrag(one, one, one, one);
#pragma unroll
  for (int t = 0; t < NT; ++t) asm volatile("" : "+v"(acc[t]));
  barrier();
#pragma unroll
  for (int t = 0; t < NT; ++t) f0.w[t] = ldtr(a.wa[t], a.wa[t] + 512);
  f0.x = ld128(a.xa[0]);
  SB();
#pragma unroll
  for (int t = 0; t < NT; ++t) f1.w[t] = ldtr(a.wa[t] ^ 2048, (a.wa[t] ^ 2048) + 512);
  f1.x = ld128(a.xa[1]);
  SB();
  u32 wsl = 0, xsl = 0;
#pragma unroll 1
  for (int cp = 0; cp < 7; ++cp) {
    const u32 w1 = wsl == 4 * 16384 ? 0 : wsl + 16384;
    const u32 w2 = w1 == 4 * 16384 ? 0 : w1 + 16384;
    const u32 xn = (xsl + 16384) & 65535;
    p1_pair<NT, false>(a, f0, f1, acc, sel, cs0, cs2, w1, w2, xsl, xn);
    wsl = w2; xsl = xn;
  }
  sg = stat_load(rws, row0, lane);
  p1_pair<NT, true>(a, f0, f1, acc, sel, cs0, cs2, (15 % NW1) * 16384, 0, xsl, 0);
}

DI void p1_finish(f32x4 cs0, f32x4 cs2, int g0, int g2, bool add2, int lane) {
  if (lane < 16) {
    __builtin_amdgcn_ds_faddf((LAS float*)&lds32[(L_CS + g0 * 128 + lane * 4) >> 2], cs0[0], 0, 0, false);
    __builtin_amdgcn_ds_faddf((LAS float*)&lds32[(L_CS + g0 * 128 + 64 + lane * 4) >> 2], cs0[1], 0, 0, false);
    if (add2) {
      __builtin_amdgcn_ds_faddf((LAS float*)&lds32[(L_CS + g2 * 128 + lane * 4) >> 2], cs2[0], 0, 0, false);
      __builtin_amdgcn_ds_faddf((LAS float*)&lds32[(L_CS + g2 * 128 + 64 + lane * 4) >> 2], cs2[1], 0, 0, false);
    }
  }
  wait_lgkm0();
  barrier();
}
DI void cs_get(float (&c)[16], int gt, int lane) {
  const int h = lane >> 5;
#pragma unroll
  for (int g = 0; g < 4; ++g) {
    f32x4 v = ldf4(L_CS + gt * 128 + (8 * g + 4 * h) * 4);
    c[4 * g] = v[0]; c[4 * g + 1] = v[1]; c[4 * g + 2] = v[2]; c[4 * g + 3] = v[3];
  }
}

DI void silu_tile(const f32x16& acc, const float (&cs)[16], float nmu, float nrl, u32x4 (&o)[2]) {
  float y[16];
#pragma unroll
  for (int e = 0; e < 16; ++e) {
    const float t = fmaf(nmu, cs[e], acc[e]);
    y[e] = t * rcp(1.0f + ex2(t * nrl));
  }
#pragma unroll
  for (int u = 0; u < 2; ++u)
#pragma unroll
    for (int e = 0; e < 4; ++e) o[u][e] = pk2(y[8 * u + 2 * e], y[8 * u + 2 * e + 1]);
}

DI f32x16 phi_logits(const f32x16& acc, const float (&cs)[16], float nmu, bf16x8 a0, bf16x8 a1) {
  float t[16];
#pragma unroll
  for (int e = 0; e < 16; ++e) t[e] = fmaf(nmu, cs[e], acc[e]);
  bf16x8 b0 = asfrag(pk2(t[0], t[1]), pk2(t[2], t[3]), pk2(t[4], t[5]), pk2(t[6], t[7]));
  bf16x8 b1 = asfrag(pk2(t[8], t[9]), pk2(t[10], t[11]), pk2(t[12], t[13]), pk2(t[14], t[15]));
  f32x16 z = {};
  z = mfma32(a0, b0, z);
  z = mfma32(a1, b1, z);
  return z;
}
DI void phi_exp(const f32x16& pl, int hh, float rl, float (&ep)[8], float (&en)[8], float& zp, float& zn) {
  const int o = 8 * hh;
  float mx = fmax2(fmax2(fmax2(fmax2(fmax2(pl[o], pl[o + 1]), pl[o + 2]), fmax2(fmax2(pl[o + 3], pl[o + 4]), pl[o + 5])), pl[o + 6]), pl[o + 7]);
  float mn = fmin2(fmin2(fmin2(fmin2(fmin2(pl[o], pl[o + 1]), pl[o + 2]), fmin2(fmin2(pl[o + 3], pl[o + 4]), pl[o + 5])), pl[o + 6]), pl[o + 7]);
  xmaxmin(mx, mn);
  const float a = -rl * mx, b = rl * mn;
  zp = 0.f; zn = 0.f;
#pragma unroll
  for (int e = 0; e < 8; ++e) {
    ep[e] = ex2(fmaf(pl[8 * hh + e], rl, a));
    en[e] = ex2(fmaf(pl[8 * hh + e], -rl, b));
    zp += ep[e]; zn += en[e];
  }
  xsum2(zp, zn);
}

DI void load_wphi(const Params& p, int head0, int lane, bf16x8& a0, bf16x8& a1) {
  const int row = lane & 31, h = lane >> 5;
  const u16* wp = p.wphi + (head0 + (row >> 4)) * 256 + (row & 15);
  u32 v[4];
#pragma unroll
  for (int e = 0; e < 4; ++e) {
    const int j0 = 2 * e, j1 = 2 * e + 1;
    const int d0 = 8 * (j0 >> 2) + 4 * h + (j0 & 3), d1 = 8 * (j1 >> 2) + 4 * h + (j1 & 3);
    v[e] = (u32)wp[d0 * 16] | ((u32)wp[d1 * 16] << 16);
  }
  const bool lo = row < 16;
  a0 = asfrag(lo ? v[0] : 0u, lo ? v[1] : 0u, lo ? v[2] : 0u, lo ? v[3] : 0u);
  a1 = asfrag(lo ? 0u : v[0], lo ? 0u : v[1], lo ? 0u : v[2], lo ? 0u : v[3]);
}

DI void store_h(rsrc_t rws, int i, int rg, int tile, int lane, const u32x4 (&o)[2]) {
#pragma unroll
  for (int u = 0; u < 2; ++u)
    st128_sc1(rws, o[u], lane * 16, WS_H + ((4 * i + rg) * 112 + 2 * tile + u) * 1024);
}

#define HWIN 7
constexpr int HW = HWIN;
DI u32x4 srd(const void* ptr, u32 bytes) {
  const u64 a = (u64)ptr;
  u32x4 d = {(u32)a, (u32)(a >> 32) & 0xffffu, bytes, 0x00020000u};
  d[0] = __builtin_amdgcn_readfirstlane(d[0]); d[1] = __builtin_amdgcn_readfirstlane(d[1]);
  return d;
}
DI bf16x8 asm_gload(u32x4 rs, u32 voff, u32 soff) {
  bf16x8 v;
  asm volatile("buffer_load_dwordx4 %0, %1, %2, %3 offen sc1" : "=v"(v) : "v"(voff), "s"(rs), "s"(soff) : "memory");
  return v;
}
DI void asm_dma(u32x4 rs, u32 ldsa, u32 voff, u32 soff) {
  asm volatile("s_mov_b32 m0, %0\n\ts_nop 0\n\tbuffer_load_dwordx4 %1, %2, %3 offen lds" ::"s"(ldsa), "v"(voff), "s"(rs), "s"(soff) : "memory");
}
struct P2 { u32 wa[2], hv, wv, hs[2], w2s, wos, ld; u32x4 rws, rw2, rwo; };
constexpr int p2_ops(int m) { return (m + HW < 28 ? 2 : 0) + (m < 18 ? 2 : 0); }
constexpr int p2_younger(int n) {
  int c = 0;
  if (n < HW) { c = 2 * (HW - 1 - n); for (int m = 0; m < n; ++m) c += p2_ops(m); }
  else { c = (n - HW < 18) ? 2 : 0; for (int m = n - HW + 1; m < n; ++m) c += p2_ops(m); }
  return c;
}
DI u32 opq(u32 x) { asm volatile("" : "+v"(x)); return x; }
template <int N> DI void p2a_step(const P2& c, f32x16 (&ac)[4], bf16x8 (&hw)[HW][2], bf16x8 (&wf)[2][2], int q, u32 b0, u32 b1) {
  asm volatile("s_waitcnt vmcnt(%2)" : "+v"(hw[N % HW][0]), "+v"(hw[N % HW][1]) : "n"(p2_younger(N)) : "memory");
  if constexpr (N == 11) lds_spin(L_PF + 4 * q, 18);
  if constexpr (N + 1 < 28) {
    constexpr u32 sl = (N + 1) % 18, o = (sl & 7) * 8192;
    if constexpr ((sl & 7) == 0 || sl == 0) { b0 = opq(c.wa[0] + (sl >> 3) * 65536); b1 = opq(c.wa[1] + (sl >> 3) * 65536); }
    wf[(N + 1) & 1][0] = ldtr(b0 + o, b0 + o + 1024);
    wf[(N + 1) & 1][1] = ldtr(b1 + o, b1 + o + 1024);
  }
  SB();
  ac[0] = mfma32(wf[N & 1][0], hw[N % HW][0], ac[0]);
  ac[1] = mfma32(wf[N & 1][1], hw[N % HW][0], ac[1]);
  ac[2] = mfma32(wf[N & 1][0], hw[N % HW][1], ac[2]);
  ac[3] = mfma32(wf[N & 1][1], hw[N % HW][1], ac[3]);
  SB();
  if constexpr (N + HW < 28) {
    hw[N % HW][0] = asm_gload(c.rws, c.hv, c.hs[0] + (N + HW) * 1024);
    hw[N % HW][1] = asm_gload(c.rws, c.hv, c.hs[1] + (N + HW) * 1024);
  }
  if constexpr (N < 10) {
    asm_dma(c.rw2, c.ld + N * 8192, c.wv, c.w2s + (N + 18) * 16384);
    asm_dma(c.rw2, c.ld + N * 8192 + 1024, c.wv, c.w2s + (N + 18) * 16384 + 8192);
  } else if constexpr (N < 18) {
    asm_dma(c.rwo, c.ld + N * 8192, c.wv, c.wos + (N - 10) * 16384);
    asm_dma(c.rwo, c.ld + N * 8192 + 1024, c.wv, c.wos + (N - 10) * 16384 + 8192);
  }
  SB();
  if constexpr (N + 1 < 28) p2a_step<N + 1>(c, ac, hw, wf, q, b0, b1);
}
template <int N> DI void p2b_step(f32x16 (&ac)[4], bf16x8 (&ow)[8][2], u32 b0, u32 b1) {
  constexpr u32 o = N * 8192;
  asm volatile("s_waitcnt vmcnt(%2)" : "+v"(ow[N][0]), "+v"(ow[N][1]) : "n"(2 * (7 - N)) : "memory");
  const bf16x8 w0 = ldtr(b0 + o, b0 + o + 1024), w1 = ldtr(b1 + o, b1 + o + 1024);
  SB();
  ac[0] = mfma32(w0, ow[N][0], ac[0]);
  ac[1] = mfma32(w1, ow[N][0], ac[1]);
  ac[2] = mfma32(w0, ow[N][1], ac[2]);
  ac[3] = mfma32(w1, ow[N][1], ac[3]);
  SB();
  if constexpr (N < 7) p2b_step<N + 1>(ac, ow, b0, b1);
}
DI void red_send(const f32x16 (&ac)[4], int q, int lane, int slot0) {
  const u32 b = opq(slot0 * 8192 + q * 2048 + lane * 16);
#pragma unroll
  for (int l = 1; l < 4; ++l)
#pragma unroll
    for (int g = 0; g < 4; ++g) {
      const f32x4 v = {ac[l][4 * g], ac[l][4 * g + 1], ac[l][4 * g + 2], ac[l][4 * g + 3]};
      *(LAS f32x4*)(lds + b + (2 * (l - 1) + (g >> 1)) * 8192 + (g & 1) * 1024) = v;
    }
}
DI void red_recv(f32x16& a, int q, int lane, int slot0) {
#pragma unroll
  for (int l = 1; l < 4; ++l) {
    const u32 b = opq(slot0 * 8192 + (q ^ l) * 2048 + lane * 16);
#pragma unroll
    for (int g = 0; g < 4; ++g) {
      const f32x4 v = ldf4(b + (2 * (l - 1) + (g >> 1)) * 8192 + (g & 1) * 1024);
#pragma unroll
      for (int e = 0; e < 4; ++e) a[4 * g + e] += v[e];
    }
  }
}

DI u64 compute_prologue(const Params& p, rsrc_t rws, int w, int lane, int i, int j) {
  const int row = 128 * i + 8 * j + w;
  const u32x4 xv = *(const GAS u32x4*)((const char*)p.x + row * 1024 + lane * 16);
  const u64 tag = __hip_atomic_load((GAS u64*)(p.ws + WS_E), RLX) + 1;
  stat_publish(p, rws, row, lane, tag, xv);
  return tag;
}

DI void roleA_main(const Params& p, int w, int lane, int i, int j, int i2, int j2, u64 t_first) {
  STAMP_DECL;
  STAMP(20);
  if (PROBE == 21) probe_t = t_first;
  const int rg = w, pr = j >> 1, isq = j & 1, odd = rg & 1;
  const int s = lane & 31, h = lane >> 5;
  const rsrc_t rws = mkrs(p.ws, WS_END);
  const u64 tag = compute_prologue(p, rws, w, lane, i, j);
  if (w == 0 && lane == 0) *(LAS u64*)(lds + L_TAG) = tag;
  const int t0 = (!isq && (pr & 1)) ? 5 : 4;
  const int tg[3] = {t0 + odd, t0 + 1 - odd, t0 + 2};
  P1<3> ad; ad.init(rg, lane, tg);
  f32x16 acc[3] = {};
  f32x4 cs0 = {}, cs2 = {};
  u32x4 sg;
  STAMP(0);
  p1_main(ad, acc, cs0, cs2, lane, rws, 128 * i + 32 * rg, sg);
  STAMP(1);
  p1_finish(cs0, cs2, tg[0], tg[2], !odd, lane);
  STAMP(2);
  if (PRIO == 0 || (PRIO == 1 && isq)) __builtin_amdgcn_s_setprio(1);
  float nmu, r;
  stat_get(rws, 128 * i + 32 * rg, lane, tag, sg, nmu, r);
  const float rl = r * LOG2E;
  const int Tb = 2 * h_unit0(pr, isq) - 4;
#pragma unroll
  for (int t = 0; t < 3; ++t) {
    float c16[16];
    u32x4 o[2];
    cs_get(c16, tg[t], lane);
    silu_tile(acc[t], c16, nmu, -rl, o);
    store_h(rws, i, rg, Tb + tg[t], lane, o);
  }
  STAMP(3);
  wait_vm0();
  if (arrive(C_H, lane) == (isq ? 7u : 3u)) set_flag(p.ws, WS_FH + (i * 16 + j) * 128, tag, lane);
  __builtin_amdgcn_s_setprio(0);
  const int q = rg, rb = q >> 1, cb = q & 1;
  const int orow = 64 * i2 + 32 * rb + s;
  const u32 ooff = orow * 1024 + (64 * j2 + 32 * cb) * 2 + h * 16;
  float nmu2, r2;
  stat_get(rws, 64 * i2 + 32 * rb, lane, tag, stat_load(rws, 64 * i2 + 32 * rb, lane), nmu2, r2);
  P2 c2;
  {
    const int blk = (lane >> 4) & 1, qq = (lane & 15) >> 2, pp = lane & 3;
    c2.wa[0] = q * 2048 + (4 * h + qq) * 128 + ((cb ^ (qq >> 1)) * 64) + 32 * blk + 8 * pp;
    c2.wa[1] = c2.wa[0] ^ 64;
    c2.hv = lane * 16;
    c2.wv = (lane >> 3) * 1024 + ((((lane & 7) >> 2) ^ ((lane >> 4) & 1)) * 64) + (lane & 3) * 16;
    c2.rws = srd(p.ws, WS_END); c2.rw2 = srd(p.w2, F_ * D_ * 2); c2.rwo = srd(p.wo, D_ * D_ * 2);
    c2.hs[0] = WS_H + ((2 * i2 + rb) * 112 + 28 * q) * 1024; c2.hs[1] = WS_H + ((2 * i2 + (rb ^ 1)) * 112 + 28 * q) * 1024;
    c2.w2s = 16 * 28 * q * 1024 + j2 * 128;
    c2.wos = 128 * q * 1024 + j2 * 128;
    c2.ld = ldsaddr(q * 2048);
  }
  f32x16 ac[4] = {};
  bf16x8 hw[HW][2], wf[2][2];
  STAMP(6);
  lds_spin(L_PF + 4 * q, 12);
  lds_spin(L_CNT + 4 * C_HR, 1);
  STAMP(7);
#pragma unroll
  for (int n = 0; n < HW; ++n) { hw[n][0] = asm_gload(c2.rws, c2.hv, c2.hs[0] + n * 1024); hw[n][1] = asm_gload(c2.rws, c2.hv, c2.hs[1] + n * 1024); }
  wf[0][0] = ldtr(c2.wa[0], c2.wa[0] + 1024); wf[0][1] = ldtr(c2.wa[1], c2.wa[1] + 1024);
  p2a_step<0>(c2, ac, hw, wf, q, c2.wa[0], c2.wa[1]);
  STAMP(8);
  red_send(ac, q, lane, 0);
  arrive(C_RG, lane);
  u32x4 xr[2];
#pragma unroll
  for (int u = 0; u < 2; ++u) xr[u] = *(const GAS u32x4*)((const char*)p.x + ooff + 32 * u);
  lds_spin(L_CNT + 4 * C_RG, 4);
  red_recv(ac[0], q, lane, 0);
#pragma unroll
  for (int g = 0; g < 4; ++g) {
    f32x4 v;
#pragma unroll
    for (int e = 0; e < 4; ++e) { const float gp = ac[0][4 * g + e] * r2; v[e] = gp * rcp(1.0f + ex2(-LOG2E * gp)); }
    *(LAS f32x4*)(lds + (6 + (g >> 1)) * 8192 + q * 2048 + (g & 1) * 1024 + lane * 16) = v;
  }
#pragma unroll
  for (int u = 0; u < 2; ++u) *(LAS u32x4*)(lds + 8 * 8192 + q * 2048 + u * 1024 + lane * 16) = xr[u];
  const u32 os0 = WS_O + ((2 * i2 + rb) * 32 + 8 * q) * 1024, os1 = WS_O + ((2 * i2 + (rb ^ 1)) * 32 + 8 * q) * 1024;
  lds_spin(L_CNT + 4 * C_OR, 1);
  STAMP(9);
  bf16x8 ow[8][2];
#pragma unroll
  for (int n = 0; n < 8; ++n) { ow[n][0] = asm_gload(c2.rws, c2.hv, os0 + n * 1024); ow[n][1] = asm_gload(c2.rws, c2.hv, os1 + n * 1024); }
#pragma unroll
  for (int l = 0; l < 4; ++l) ac[l] = f32x16{};
  p2b_step<0>(ac, ow, opq(c2.wa[0] + 10 * 8192), opq(c2.wa[1] + 10 * 8192));
  red_send(ac, q, lane, 10);
  arrive(C_RS, lane);
  lds_spin(L_CNT + 4 * C_RS, 4);
  red_recv(ac[0], q, lane, 10);
  const f32x16 as = ac[0];
  float G[16];
#pragma unroll
  for (int g = 0; g < 4; ++g) {
    const f32x4 v = ldf4((6 + (g >> 1)) * 8192 + q * 2048 + (g & 1) * 1024 + lane * 16);
    G[4 * g] = v[0]; G[4 * g + 1] = v[1]; G[4 * g + 2] = v[2]; G[4 * g + 3] = v[3];
  }
#pragma unroll
  for (int u = 0; u < 2; ++u) xr[u] = *(LAS u32x4*)(lds + 8 * 8192 + q * 2048 + u * 1024 + lane * 16);
  STAMP(10);
#pragma unroll
  for (int u = 0; u < 2; ++u) {
    u32x2 ra = __builtin_amdgcn_permlane32_swap(xr[u][0], xr[u][2], false, false);
    u32x2 rb_ = __builtin_amdgcn_permlane32_swap(xr[u][1], xr[u][3], false, false);
    u32 pk[2][2];
#pragma unroll
    for (int g = 0; g < 2; ++g) {
      const u32 x01 = ra[g], x23 = rb_[g];
      const int e = 8 * u + 4 * g;
      const float y0 = fmaf(as[e], G[e], u2f(x01 << 16)), y1 = fmaf(as[e + 1], G[e + 1], u2f(x01 & 0xffff0000u));
      const float y2 = fmaf(as[e + 2], G[e + 2], u2f(x23 << 16)), y3 = fmaf(as[e + 3], G[e + 3], u2f(x23 & 0xffff0000u));
      pk[g][0] = pk2(y0, y1); pk[g][1] = pk2(y2, y3);
    }
    u32x2 s0 = __builtin_amdgcn_permlane32_swap(pk[0][0], pk[1][0], false, false);
    u32x2 s1 = __builtin_amdgcn_permlane32_swap(pk[0][1], pk[1][1], false, false);
    u32x4 v = {s0[0], s1[0], s0[1], s1[1]};
    *(GAS u32x4*)((char*)p.out + ooff + 32 * u) = v;
  }
  STAMP(11);
  STAMP_DUMP;
}

DI void roleB_kv(const Params& p, int w, int lane, int i, int j) {
  STAMP_DECL;
  const int rg = w - 4, pr = j >> 1, odd = rg & 1;
  const int s = lane & 31, h = lane >> 5;
  bf16x8 a0[2], a1[2];
  load_wphi(p, 4 * pr + 2 * odd, lane, a0[0], a1[0]);
  load_wphi(p, 4 * pr + 2 - 2 * odd, lane, a0[1], a1[1]);
  const int tg[4] = {odd, 1 - odd, 2 + odd, 3 - odd};
  const rsrc_t rws = mkrs(p.ws, WS_END);
  const u64 tag = compute_prologue(p, rws, w, lane, i, j);
  P1<4> ad; ad.init(rg, lane, tg);
  f32x16 acc[4] = {};
  f32x4 cs0 = {}, cs2 = {};
  u32x4 sg;
  STAMP(0);
  p1_main(ad, acc, cs0, cs2, lane, rws, 128 * i + 32 * rg, sg);
  STAMP(1);
  p1_finish(cs0, cs2, tg[0], tg[2], true, lane);
  STAMP(2);
  if (PRIO >= 1) __builtin_amdgcn_s_setprio(1);
  float nmu, r;
  stat_get(rws, 128 * i + 32 * rg, lane, tag, sg, nmu, r);
  const float rl = r * LOG2E;
  const u32 ib = (32 * rg + s) * 64 + ((h ^ ((s >> 1) & 7)) * 8);
  const u32 ia[4] = {ib, ib ^ 16, ib ^ 32, ib ^ 48};
#pragma unroll
  for (int t = 0; t < 2; ++t) {
    float c16[16];
    cs_get(c16, tg[t], lane);
    const f32x16 pl = phi_logits(acc[t], c16, nmu, a0[t], a1[t]);
#pragma unroll
    for (int hh = 0; hh < 2; ++hh) {
      float ep_[8], en_[8], zp, zn;
      phi_exp(pl, hh, rl, ep_, en_, zp, zn);
      const float ip = rcp(zp), in = rcp(zn);
      const u32 base = L_PHI + (2 * tg[t] + hh) * 8192;
#pragma unroll
      for (int g = 0; g < 2; ++g) {
        u32x2 vp = {pk2(ep_[4 * g] * ip, ep_[4 * g + 1] * ip), pk2(ep_[4 * g + 2] * ip, ep_[4 * g + 3] * ip)};
        u32x2 vn = {pk2(en_[4 * g] * in, en_[4 * g + 1] * in), pk2(en_[4 * g + 2] * in, en_[4 * g + 3] * in)};
        *(LAS u32x2*)(lds + base + ia[g]) = vp;
        *(LAS u32x2*)(lds + base + ia[2 + g]) = vn;
      }
    }
  }
#pragma unroll
  for (int t = 0; t < 2; ++t) {
    float c16[16];
    cs_get(c16, tg[2 + t], lane);
    const u32 base = L_V + (tg[2 + t] - 2) * 8192;
#pragma unroll
    for (int g = 0; g < 4; ++g) {
      float v[4];
#pragma unroll
      for (int e = 0; e < 4; ++e) v[e] = fmaf(nmu, c16[4 * g + e], acc[2 + t][4 * g + e]) * r;
      u32x2 vv = {pk2(v[0], v[1]), pk2(v[2], v[3])};
      *(LAS u32x2*)(lds + base + ia[g]) = vv;
    }
  }
  arrive(C_PHI, lane);
  STAMP(3);
  lds_spin(L_CNT + 4 * C_PHI, 4);
  {
    const int blk = (lane >> 4) & 1, q = (lane & 15) >> 2, pp = lane & 3;
    const int r0 = 8 * h + q, r1 = r0 + 4;
    const u32 pa0 = L_PHI + rg * 8192 + r0 * 64 + (((4 * blk + pp) ^ ((r0 >> 1) & 7)) * 8);
    const u32 pa1 = L_PHI + rg * 8192 + r1 * 64 + (((4 * blk + pp) ^ ((r1 >> 1) & 7)) * 8);
    const u32 vb = L_V + (rg >> 1) * 8192;
    const u32 pv0 = vb + r0 * 64 + (((4 * (rg & 1) + pp) ^ ((r0 >> 1) & 7)) * 8);
    const u32 pv1 = vb + r1 * 64 + (((4 * (rg & 1) + pp) ^ ((r1 >> 1) & 7)) * 8);
    const bf16x8 ones = asfrag(0x3f803f80u, 0x3f803f80u, 0x3f803f80u, 0x3f803f80u);
    f32x16 d = {};
#pragma unroll
    for (int u = 0; u < 8; ++u) {
      const bf16x8 vv = ldtr(pv0 + u * 1024, pv1 + u * 1024);
      d = mfma32(ldtr(pa0 + u * 1024, pa1 + u * 1024), blk ? ones : vv, d);
    }
    asm volatile("" :: "v"(d));
    arrive(C_IMG, lane);
    if (s <= 16) {
#pragma unroll
      for (int u = 0; u < 2; ++u) {
        u32x4 v = {pk2(d[8 * u], d[8 * u + 1]), pk2(d[8 * u + 2], d[8 * u + 3]), pk2(d[8 * u + 4], d[8 * u + 5]), pk2(d[8 * u + 6], d[8 * u + 7])};
        st128_sc1(rws, v, (2 * s + h) * 16, WS_KV + (((pr * 16 + i) * 4 + rg) * 2 + u) * 544);
      }
    }
  }
  STAMP(4);
  wait_vm0();
  if (arrive(C_KV, lane) == 3) set_flag(p.ws, WS_FK + (pr * 16 + i) * 128, tag, lane);
  STAMP(5);
  STAMP_DUMP;
}

DI void roleB_q(const Params& p, int w, int lane, int i, int j) {
  STAMP_DECL;
  const int rg = w - 4, pr = j >> 1, odd = rg & 1;
  const int s = lane & 31, h = lane >> 5;
  bf16x8 a0[2], a1[2];
  load_wphi(p, 4 * pr + 2 * odd, lane, a0[0], a1[0]);
  load_wphi(p, 4 * pr + 2 - 2 * odd, lane, a0[1], a1[1]);
  const int tg[3] = {odd, 1 - odd, 7};
  const rsrc_t rws = mkrs(p.ws, WS_END);
  const u64 tag = compute_prologue(p, rws, w, lane, i, j);
  P1<3> ad; ad.init(rg, lane, tg);
  f32x16 acc[3] = {};
  f32x4 cs0 = {}, cs2 = {};
  u32x4 sg;
  STAMP(0);
  p1_main(ad, acc, cs0, cs2, lane, rws, 128 * i + 32 * rg, sg);
  STAMP(1);
  p1_finish(cs0, cs2, tg[0], tg[2], !odd, lane);
  STAMP(2);
  float nmu, r;
  stat_get(rws, 128 * i + 32 * rg, lane, tag, sg, nmu, r);
  const float rl = r * LOG2E;
  {
    float c16[16];
    u32x4 o[2];
    cs_get(c16, 7, lane);
    silu_tile(acc[2], c16, nmu, -rl, o);
    store_h(rws, i, rg, 2 * h_unit0(pr, 1) + 3, lane, o);
  }
  STAMP(3);
  bf16x8 bq[4][2];
  float zeps[4];
#pragma unroll
  for (int t = 0; t < 2; ++t) {
    float c16[16];
    cs_get(c16, tg[t], lane);
    const f32x16 pl = phi_logits(acc[t], c16, nmu, a0[t], a1[t]);
#pragma unroll
    for (int hh = 0; hh < 2; ++hh) {
      float ep_[8], en_[8], zp, zn;
      phi_exp(pl, hh, rl, ep_, en_, zp, zn);
      const float rho = zp * rcp(zn);
      bq[2 * t + hh][0] = asfrag(pk2(ep_[0], ep_[1]), pk2(ep_[2], ep_[3]), pk2(ep_[4], ep_[5]), pk2(ep_[6], ep_[7]));
      bq[2 * t + hh][1] = asfrag(pk2(en_[0] * rho, en_[1] * rho), pk2(en_[2] * rho, en_[3] * rho), pk2(en_[4] * rho, en_[5] * rho), pk2(en_[6] * rho, en_[7] * rho));
      zeps[2 * t + hh] = zp * 1e-6f;
    }
    if (t == 0) {
      wait_vm0();
      if (arrive(C_H, lane) == 7) set_flag(p.ws, WS_FH + (i * 16 + j) * 128, tag, lane);
    }
  }
  STAMP(4);
  poll16(p.ws, WS_FK + pr * 16 * 128, 128, tag, lane, 15);
  STAMP(5);
  {
    u32x4 pv[2][8];
    u32x2 pz[2];
#pragma unroll
    for (int u = 0; u < 2; ++u) {
      const u32 pbase = WS_KV + ((pr * 16 * 4 + rg) * 2 + u) * 544;
#pragma unroll
      for (int m = 0; m < 8; ++m)
        pv[u][m] = __builtin_amdgcn_raw_buffer_load_b128(rws, ((lane >> 5) * 8 * 544) + (lane & 31) * 16, pbase + 2 * m * 8 * 544, 16);
      pz[u] = __builtin_amdgcn_raw_buffer_load_b64(rws, (lane >> 2) * 8 * 544 + 512 + (lane & 3) * 8, pbase, 16);
    }
    u32 ps[4];
#pragma unroll
    for (int e = 0; e < 4; ++e) ps[e] = (s == 2 * e ? 0x00003f80u : 0u) | (s == 2 * e + 1 ? 0x3f800000u : 0u);
    const bf16x8 psel = asfrag(ps[0], ps[1], ps[2], ps[3]);
#pragma unroll
    for (int u = 0; u < 2; ++u) {
      f32x16 sm = {};
#pragma unroll
      for (int m = 0; m < 8; ++m) sm = mfma32(psel, __builtin_bit_cast(bf16x8, pv[u][m]), sm);
      float zz[4] = {u2f(pz[u][0] << 16), u2f(pz[u][0] & 0xffff0000u), u2f(pz[u][1] << 16), u2f(pz[u][1] & 0xffff0000u)};
#pragma unroll
      for (int e = 0; e < 4; ++e) {
        float v = zz[e];
        v += u2f(__builtin_amdgcn_update_dpp(0, (int)f2u(v), 0x128, 0xf, 0xf, false));
        v += u2f(__builtin_amdgcn_update_dpp(0, (int)f2u(v), 0x124, 0xf, 0xf, false));
        u32x2 t = __builtin_amdgcn_permlane16_swap(f2u(v), f2u(v), false, false);
        v = u2f(t[0]) + u2f(t[1]);
        zz[e] = xsum(v);
      }
      const u32 kb = L_KVA + (rg * 2 + u) * 544;
      { u32x2 v = {pk2(sm[0], sm[1]), pk2(sm[2], sm[3])}; *(LAS u32x2*)(lds + kb + s * 16 + h * 8) = v; }
      if (lane < 4) { u32x2 v = {pk2(zz[0], zz[1]), pk2(zz[2], zz[3])}; *(LAS u32x2*)(lds + kb + 512 + lane * 8) = v; }
    }
  }
  arrive(C_CH, lane);
  lds_spin(L_CNT + 4 * C_CH, 4);
  STAMP(6);
#pragma unroll
  for (int hh = 0; hh < 4; ++hh) {
    const u32 ao = (s < 16 ? s : 16) * 32 + h * 16;
    f32x16 d = {};
    const int ah = hh ^ (2 * odd);
    d = mfma32(ld128(L_KVA + (ah * 2) * 544 + ao), bq[hh][0], d);
    d = mfma32(ld128(L_KVA + (ah * 2 + 1) * 544 + ao), bq[hh][1], d);
    const float inv = rcp(d[8] + zeps[hh]);
    u32x4 v = {pk2(d[0] * inv, d[1] * inv), pk2(d[2] * inv, d[3] * inv), pk2(d[4] * inv, d[5] * inv), pk2(d[6] * inv, d[7] * inv)};
    st128_sc1(rws, v, lane * 16, WS_O + ((4 * i + rg) * 32 + 4 * pr + ah) * 1024);
  }
  STAMP(7);
  wait_vm0();
  if (arrive(C_O, lane) == 3) set_flag(p.ws, WS_FO + (i * 8 + pr) * 128, tag, lane);
  STAMP(8);
  STAMP_DUMP;
}

__global__ void __launch_bounds__(768, 3) fused_hedgehog_ffn(Params p) {
  u64 t_first = __builtin_amdgcn_s_memrealtime();
  __builtin_amdgcn_sched_barrier(0);
  const int tid = threadIdx.x, lane = tid & 63;
  const int w = __builtin_amdgcn_readfirstlane(tid >> 6);
  const int b = blockIdx.x, g = b & 7, m = b >> 3;
#define MAP 2
  const int i = MAP == 0 ? 4 * (g >> 1) + (m & 3) : MAP == 1 ? 2 * g + (m & 1) : MAP == 2 ? 8 * (g >> 2) + (m & 7) : (b >> 4);
  const int j = MAP == 0 ? 8 * (g & 1) + (m >> 2) : MAP == 1 ? (m >> 1) : MAP == 2 ? 4 * (g & 3) + (m >> 3) : (b & 15);
  if (w == 4) {
    *(LAS u32*)(lds + L_CNT + 4 * (lane & 31)) = 0;
#pragma unroll
    for (int q = 0; q < 4; ++q) *(LAS u32*)(lds + L_CS + 4 * (lane + 64 * q)) = 0;
    wait_lgkm0();
  }
#define MAP2 1
  const int i2 = MAP2 == 0 ? 2 * i + (j & 1) : 8 * (g >> 1) + (m & 7), j2 = MAP2 == 0 ? j >> 1 : 4 * (g & 1) + (m >> 3);
  if (w >= 8) loader_main(p, w, lane, i, j, i2, j2);
  else if (w < 4) roleA_main(p, w, lane, i, j, i2, j2, t_first);
  else if (j & 1) roleB_q(p, w, lane, i, j);
  else roleB_kv(p, w, lane, i, j);
}

extern "C" void kernel_launch(void* const* d_in, const int* in_sizes, int n_in, void* d_out, int out_size, void* d_ws, size_t ws_size,
                              hipStream_t stream) {
  Params p;
  p.x = (const u16*)d_in[0]; p.wqkv = (const u16*)d_in[1]; p.wphi = (const u16*)d_in[2]; p.wo = (const u16*)d_in[3];
  p.w1 = (const u16*)d_in[5]; p.w2 = (const u16*)d_in[6]; p.out = (u16*)d_out; p.ws = (char*)d_ws;
  fused_hedgehog_ffn<<<256, 768, 0, stream>>>(p);
}
```

```cpp
#define PG 1
#define PT 64
#define PL 0
#include <hip/hip_runtime.h>

typedef unsigned short u16;
typedef unsigned u32;
typedef unsigned long long u64;
typedef short bf16x8 __attribute__((ext_vector_type(8)));
typedef short s16x4 __attribute__((ext_vector_type(4)));
typedef __bf16 bf2 __attribute__((ext_vector_type(2)));
typedef float f32x16 __attribute__((ext_vector_type(16)));
typedef float f32x4 __attribute__((ext_vector_type(4)));
typedef u32 u32x4 __attribute__((ext_vector_type(4)));
typedef u32 u32x2 __attribute__((ext_vector_type(2)));
#define LAS __attribute__((address_space(3)))
#define GAS __attribute__((address_space(1)))
#define DI __device__ __forceinline__
#define RLX __ATOMIC_RELAXED, __HIP_MEMORY_SCOPE_AGENT
#define RLXW __ATOMIC_RELAXED, __HIP_MEMORY_SCOPE_WORKGROUP

#define PRIO 1
#define TIMING 0

constexpr int S_ = 2048, D_ = 512, F_ = 1792;
constexpr float LOG2E = 1.4426950408889634f;

constexpr u32 WS_E = 0;
constexpr u32 WS_FH = 4096;
constexpr u32 WS_FK = WS_FH + 32768;
constexpr u32 WS_FO = WS_FK + 32768;
constexpr u32 WS_ST = 102400;
constexpr u32 WS_KV = 262144;
constexpr u32 WS_H = 1u << 20;
constexpr u32 WS_O = WS_H + 64u * 112u * 1024u;
constexpr u32 WS_END = WS_O + 64u * 32u * 1024u;
constexpr u32 WS_TM = 16u << 20;

constexpr u32 W1R = 0, NW1 = 5;
constexpr u32 X1R = 81920, NX1 = 4;
constexpr u32 L_PHI = 98304, L_V = 131072;
constexpr u32 L_KVA = 147456;
constexpr u32 MISC = 157696;
constexpr u32 L_CS = MISC + 1024;
constexpr u32 L_CNT = MISC + 2048, L_TAG = MISC;
constexpr u32 L_PF = L_CNT + 64;
enum { C_H = 0, C_PHI = 1, C_KV = 2, C_CH = 3, C_O = 4, C_HR = 5, C_OR = 6, C_IMG = 7, C_RG = 8, C_RS = 9 };
constexpr u32 SPIN_CAP = 1u << 22;

__shared__ __attribute__((aligned(1024))) u32 lds32[40960];
#define lds ((char*)lds32)

DI f32x16 mfma32(bf16x8 a, bf16x8 b, f32x16 c) { return __builtin_amdgcn_mfma_f32_32x32x16_bf16(a, b, c, 0, 0, 0); }
DI u32 pk2(float a, float b) { bf2 v = {(__bf16)a, (__bf16)b}; return __builtin_bit_cast(u32, v); }
DI float dot2(u32 a, u32 b, float c) { return __builtin_amdgcn_fdot2_f32_bf16(__builtin_bit_cast(bf2, a), __builtin_bit_cast(bf2, b), c, false); }
DI float ex2(float x) { return __builtin_amdgcn_exp2f(x); }
DI float rcp(float x) { return __builtin_amdgcn_rcpf(x); }
DI float fmax2(float a, float b) { return __builtin_elementwise_maximum(a, b); }
DI float fmin2(float a, float b) { return __builtin_elementwise_minimum(a, b); }
DI u32 f2u(float x) { return __builtin_bit_cast(u32, x); }
DI float u2f(u32 x) { return __builtin_bit_cast(float, x); }
DI bf16x8 ld128(u32 off) { return *(LAS bf16x8*)(lds + off); }
DI f32x4 ldf4(u32 off) { return *(LAS f32x4*)(lds + off); }
DI float ldf(u32 off) { return *(LAS float*)(lds + off); }
DI void stf(u32 off, float v) { *(LAS float*)(lds + off) = v; }
DI bf16x8 ldtr(u32 off0, u32 off1) {
  s16x4 a = __builtin_amdgcn_ds_read_tr16_b64_v4i16((LAS s16x4*)(lds + off0));
  s16x4 b = __builtin_amdgcn_ds_read_tr16_b64_v4i16((LAS s16x4*)(lds + off1));
  return __builtin_shufflevector(a, b, 0, 1, 2, 3, 4, 5, 6, 7);
}
DI bf16x8 asfrag(u32 a, u32 b, u32 c, u32 d) { u32x4 v = {a, b, c, d}; return __builtin_bit_cast(bf16x8, v); }
DI void barrier() { asm volatile("s_barrier" ::: "memory"); }
DI void wait_lgkm0() { asm volatile("s_waitcnt lgkmcnt(0)" ::: "memory"); }
DI void wait_vm0() { asm volatile("s_waitcnt vmcnt(0)" ::: "memory"); }
template <int N> DI void wait_vm() { asm volatile("s_waitcnt vmcnt(%0)" ::"n"(N) : "memory"); }
DI float xsum(float x) { u32x2 r = __builtin_amdgcn_permlane32_swap(f2u(x), f2u(x), false, false); return u2f(r[0]) + u2f(r[1]); }
DI void xmaxmin(float& mx, float& mn) {
  u32x2 r = __builtin_amdgcn_permlane32_swap(f2u(mx), f2u(-mn), false, false);
  float m = fmax2(u2f(r[0]), u2f(r[1]));
  u32x2 q = __builtin_amdgcn_permlane32_swap(f2u(m), f2u(m), false, false);
  mx = u2f(q[0]); mn = -u2f(q[1]);
}
DI void xsum2(float& a, float& b) {
  u32x2 r = __builtin_amdgcn_permlane32_swap(f2u(a), f2u(b), false, false);
  float m = u2f(r[0]) + u2f(r[1]);
  u32x2 q = __builtin_amdgcn_permlane32_swap(f2u(m), f2u(m), false, false);
  a = u2f(q[0]); b = u2f(q[1]);
}
DI void lds_st(u32 off, u32 v) { __hip_atomic_store((LAS u32*)&lds32[off >> 2], v, RLXW); }
DI u32 ldsaddr(u32 off) { return (u32)(size_t)(LAS char*)lds + off; }
DI u32x4 asm_ld128(u32 off) {
  u32x4 v;
  asm volatile("ds_read_b128 %0, %1\n\ts_waitcnt lgkmcnt(0)" : "=&v"(v) : "v"(ldsaddr(off)) : "memory");
  return v;
}
DI void asm_st32(u32 off, u32 val) { asm volatile("ds_write_b32 %0, %1" ::"v"(ldsaddr(off)), "v"(val) : "memory"); }
DI u32 asm_ld32(u32 off) {
  u32 v;
  asm volatile("ds_read_b32 %0, %1\n\ts_waitcnt lgkmcnt(0)" : "=&v"(v) : "v"(ldsaddr(off)) : "memory");
  return v;
}
DI void lds_spin(u32 off, u32 target) {
#pragma unroll 1
  for (u32 it = 0; it < SPIN_CAP; ++it) {
    if (__builtin_amdgcn_readfirstlane(asm_ld32(off)) >= target) break;
    __builtin_amdgcn_s_sleep(1);
  }
}
DI u32 lds_add_rtn(u32 off) { return __hip_atomic_fetch_add((LAS u32*)&lds32[off >> 2], 1u, RLXW); }
DI u32 lds_ld(u32 off) { return __hip_atomic_load((LAS u32*)&lds32[off >> 2], RLXW); }
DI u32 arrive(int cnt, int lane) {
  asm volatile("" ::: "memory");
  u32 old = 0;
  if (lane == 0) old = lds_add_rtn(L_CNT + 4 * cnt);
  old = __builtin_amdgcn_readfirstlane(old);
  asm volatile("" ::: "memory");
  return old;
}
DI void poll16(const char* ws, u32 base, u32 stride, u64 tag, int lane, int mask) {
  GAS u64* p = (GAS u64*)(ws + base + (u32)(lane & mask) * stride);
#pragma unroll 1
  for (u32 it = 0; it < SPIN_CAP; ++it) {
    u64 v = __hip_atomic_load(p, RLX);
    if (__all(v == tag)) break;
    __builtin_amdgcn_s_sleep(1);
  }
  asm volatile("" ::: "memory");
}
DI void set_flag(char* ws, u32 off, u64 tag, int lane) {
  if (lane == 0) __hip_atomic_store((GAS u64*)(ws + off), tag, RLX);
}

#define STAMP(k) do { } while (0)
#define STAMP_DECL do { } while (0)
#define STAMP_DUMP do { } while (0)

struct Params { const u16* x; const u16* wqkv; const u16* wphi; const u16* wo; const u16* w1; const u16* w2; u16* out; char* ws; };

typedef __amdgpu_buffer_rsrc_t rsrc_t;
DI rsrc_t mkrs(const void* p, u32 bytes) { return __builtin_amdgcn_make_buffer_rsrc((void*)p, 0, bytes, 0x00020000); }
DI void st128_sc1(rsrc_t rs, u32x4 v, u32 voff, u32 soff) { __builtin_amdgcn_raw_buffer_store_b128(v, rs, voff + soff, 0, 16); }
template <int AUX> DI void dma16(rsrc_t rs, u32 ldsoff, u32 voff, u32 soff) {
  __builtin_amdgcn_raw_ptr_buffer_load_lds(rs, (LAS void*)(lds + ldsoff), 16, voff, soff, 0, AUX);
}

DI int h_unit0(int p, int isq) { return isq ? ((p & 1) ? (7 * p + 3) >> 1 : (7 * p) >> 1) : ((p & 1) ? (7 * p - 1) >> 1 : (7 * p + 4) >> 1); }

DI void loader_main(const Params& p, int w, int lane, int i, int j, int i2, int j2) {
  STAMP_DECL;
  const int wl = w - 8, pr = j >> 1, isq = j & 1;
  const rsrc_t rx = mkrs(p.x, S_ * D_ * 2), rq = mkrs(p.wqkv, D_ * 1536 * 2), r1 = mkrs(p.w1, D_ * F_ * 2);
  const u32 gsw = ((((lane & 7) >> 2) ^ ((lane >> 4) & 1)) * 64) + (lane & 3) * 16;
  const u32 xv = (lane >> 3) * 1024 + (((lane & 7) ^ ((4 * (wl & 1) + (lane >> 4)) & 7)) * 16);
  const u32 qv = (lane >> 3) * 3072 + gsw, hv1 = (lane >> 3) * 3584 + gsw;
  const u32 xs = (128 * i + 8 * wl) * 1024;
  const u32 s0 = (isq ? pr * 128 : 1024 + pr * 128) + 8 * wl * 3072, s1 = 2048 + pr * 128 + 8 * wl * 3072;
  const u32 h0 = h_unit0(pr, isq) * 128 + 8 * wl * 3584;
  auto issue_w = [&](int c, u32 slot) {
    const u32 d = slot + wl * 1024;
    dma16<0>(rq, d, qv, s0 + c * (32 * 3072));
    if (!isq) dma16<0>(rq, d + 4096, qv, s1 + c * (32 * 3072));
    dma16<0>(r1, d + 8192, hv1, h0 + c * (32 * 3584));
    dma16<0>(r1, d + 12288, hv1, h0 + 128 + c * (32 * 3584));
  };
  auto issue_x = [&](int P, int half) {
    const u32 d = X1R + (P & 3) * 16384 + (wl + 8 * half) * 1024;
    dma16<0>(rx, d, xv, xs + half * 65536 + P * 128);
    dma16<0>(rx, d + 4096, xv, xs + half * 65536 + 32768 + P * 128);
  };
  STAMP(0);
  issue_x(0, 0); issue_x(0, 1);
  issue_w(0, 0); issue_w(1, 16384);
  issue_x(1, 0); issue_x(1, 1);
  issue_w(2, 2 * 16384);
  STAMP(1);
  if (isq) wait_vm<7>(); else wait_vm<8>();
  barrier();
  issue_w(3, 3 * 16384);
  issue_x(2, 0); issue_x(2, 1);
  issue_w(4, 4 * 16384);
  if (isq) wait_vm<10>(); else wait_vm<12>();
  barrier();
  issue_w(5, 0);
  issue_x(3, 0); issue_x(3, 1);
  u32 wslot = 16384;
#pragma unroll 1
  for (int c = 2; c < 16; ++c) {
    if (isq) {
      if (c <= 11) wait_vm<10>(); else if (c == 12) wait_vm<6>(); else if (c == 13) wait_vm<3>(); else wait_vm<0>();
    } else {
      if (c <= 11) wait_vm<12>(); else if (c == 12) wait_vm<8>(); else if (c == 13) wait_vm<4>(); else wait_vm<0>();
    }
    barrier();
    if (c <= 11) issue_w(c + 4, wslot);
    if (c <= 9) issue_x((c + 6) >> 1, c & 1);
    wslot = wslot == 4 * 16384 ? 0 : wslot + 16384;
  }
  STAMP(2);
  barrier();
  STAMP(3);
  const rsrc_t rw2 = mkrs(p.w2, F_ * D_ * 2);
  const u32 wv = (lane >> 3) * 1024 + gsw, wsrc = 16 * 28 * wl * 1024 + j2 * 128;
  auto issue_w2 = [&](int n) {
    dma16<0>(rw2, n * 8192 + wl * 2048, wv, wsrc + n * 16384);
    dma16<0>(rw2, n * 8192 + wl * 2048 + 1024, wv, wsrc + n * 16384 + 8192);
  };
#pragma unroll 1
  for (int n = 0; n < 12; ++n) issue_w2(n);
  wait_vm0();
  asm_st32(L_PF + 4 * wl, 12);
  if (!isq) lds_spin(L_CNT + 4 * C_IMG, 4);
#pragma unroll 1
  for (int n = 12; n < 18; ++n) issue_w2(n);
  wait_vm0();
  asm_st32(L_PF + 4 * wl, 18);
  STAMP(4);
  if (wl == 0) {
    const u32x4 tg = asm_ld128(L_TAG);
    const u64 tag = (u64)tg[0] | ((u64)tg[1] << 32);
    poll16(p.ws, WS_FH + (i2 >> 1) * 16 * 128, 128, tag, lane, 15);
    asm_st32(L_CNT + 4 * C_HR, 1);
    STAMP(5);
    poll16(p.ws, WS_FO + (i2 >> 1) * 8 * 128, 128, tag, lane, 7);
    asm_st32(L_CNT + 4 * C_OR, 1);
    STAMP(6);
    if (blockIdx.x == 0) {
      poll16(p.ws, WS_FO, 128, tag, lane, 63);
      poll16(p.ws, WS_FO + 64 * 128, 128, tag, lane, 63);
      if (lane == 0) __hip_atomic_store((GAS u64*)(p.ws + WS_E), tag, RLX);
    }
  }
  STAMP_DUMP;
}

template <int NT> struct Frag { bf16x8 x; bf16x8 w[NT]; };

template <int NT> struct P1 {
  u32 xa[4];
  u32 wa[NT];
  DI void init(int rg, int lane, const int (&tg)[NT]) {
    const int s = lane & 31, h = lane >> 5, blk = (lane >> 4) & 1, q = (lane & 15) >> 2, pp = lane & 3, hi = rg >> 1;
#pragma unroll
    for (int k = 0; k < 4; ++k) xa[k] = X1R + (((32 * rg + s) * 128 + ((h ^ ((s >> 1) & 7)) * 16)) ^ ((k ^ hi) << 5));
#pragma unroll
    for (int t = 0; t < NT; ++t)
      wa[t] = W1R + (tg[t] >> 1) * 4096 + hi * 2048 + (8 * h + q) * 128 + (((tg[t] & 1) ^ (q >> 1)) * 64) + 32 * blk + 8 * pp;
  }
};
#define SB() __builtin_amdgcn_sched_barrier(0)
template <int NT> struct Addr { u32 w[NT]; };
template <int NT> DI Addr<NT> waddr(const P1<NT>& a, u32 slot) {
  Addr<NT> r;
#pragma unroll
  for (int t = 0; t < NT; ++t) r.w[t] = a.wa[t] + slot;
  return r;
}

template <int NT, bool CS, bool RELOAD>
DI void p1_step(Frag<NT>& f, f32x16 (&acc)[NT], bf16x8 sel, f32x4& cs0, f32x4& cs2, const Addr<NT>& wa, u32 xa, int ks) {
#pragma unroll
  for (int t = 0; t < NT; ++t) {
    acc[t] = mfma32(f.w[t], f.x, acc[t]);
    if constexpr (CS) {
      if (t == 0) cs0 = __builtin_amdgcn_mfma_f32_16x16x32_bf16(sel, f.w[0], cs0, 0, 0, 0);
      if (t == 2) cs2 = __builtin_amdgcn_mfma_f32_16x16x32_bf16(sel, f.w[2], cs2, 0, 0, 0);
    }
    SB();
    if constexpr (RELOAD) {
      const u32 ad = wa.w[t] ^ (ks << 11);
      f.w[t] = ldtr(ad, ad + 512);
      if (t == NT - 1) f.x = ld128(xa);
    }
    SB();
  }
}
template <int NT, bool LAST>
DI void p1_pair(const P1<NT>& a, Frag<NT>& f0, Frag<NT>& f1, f32x16 (&acc)[NT], bf16x8 sel, f32x4& cs0, f32x4& cs2, u32 w1, u32 w2, u32 xsl, u32 xn) {
  {
    const Addr<NT> wa = waddr(a, w1);
    p1_step<NT, true, true>(f0, acc, sel, cs0, cs2, wa, a.xa[2] + xsl, 0);
    p1_step<NT, false, true>(f1, acc, sel, cs0, cs2, wa, a.xa[3] + xsl, 1);
  }
  barrier();
  {
    const Addr<NT> wa = waddr(a, w2);
    p1_step<NT, true, !LAST>(f0, acc, sel, cs0, cs2, wa, a.xa[0] + xn, 0);
    p1_step<NT, false, !LAST>(f1, acc, sel, cs0, cs2, wa, a.xa[1] + xn, 1);
  }
  if constexpr (!LAST) barrier();
}
struct Stat { u32x4 g; };
DI u32x4 stat_load(rsrc_t rws, int row0, int lane) { return __builtin_amdgcn_raw_buffer_load_b128(rws, (lane & 31) * 16, WS_ST + row0 * 16, 16); }
DI void stat_get(rsrc_t rws, int row0, int lane, u64 tag, u32x4 g, float& nmu, float& r) {
#pragma unroll 1
  for (u32 it = 0; it < SPIN_CAP; ++it) {
    if (__all(g[1] == (u32)tag && g[3] == (u32)(tag >> 32))) break;
    __builtin_amdgcn_s_sleep(1);
    g = stat_load(rws, row0, lane);
  }
  nmu = -u2f(g[0]); r = u2f(g[2]);
}
DI void stat_publish(const Params& p, rsrc_t rws, int row, int lane, u64 tag, u32x4 xv) {
  float s0 = 0.f, s1 = 0.f, q0 = 0.f, q1 = 0.f;
#pragma unroll
  for (int e = 0; e < 4; ++e) {
    const float lo = u2f(xv[e] << 16), hi = u2f(xv[e] & 0xffff0000u);
    s0 += lo; s1 += hi; q0 = fmaf(lo, lo, q0); q1 = fmaf(hi, hi, q1);
  }
  float s = s0 + s1, q = q0 + q1;
#define DPP_ADD(v, ctrl) v += u2f(__builtin_amdgcn_update_dpp(0, (int)f2u(v), ctrl, 0xf, 0xf, false))
  DPP_ADD(s, 0x128); DPP_ADD(q, 0x128); DPP_ADD(s, 0x124); DPP_ADD(q, 0x124);
  DPP_ADD(s, 0x122); DPP_ADD(q, 0x122); DPP_ADD(s, 0x121); DPP_ADD(q, 0x121);
#undef DPP_ADD
  { u32x2 a = __builtin_amdgcn_permlane16_swap(f2u(s), f2u(q), false, false);
    float m = u2f(a[0]) + u2f(a[1]);
    u32x2 b = __builtin_amdgcn_permlane16_swap(f2u(m), f2u(m), false, false);
    s = u2f(b[0]); q = u2f(b[1]); }
  xsum2(s, q);
  const float mu = s * (1.0f / D_);
  const float var = fmaf(-mu, mu, q * (1.0f / D_));
  const float r = __builtin_amdgcn_rsqf(fmax2(var, 0.f) + 1e-5f);
  if (lane == 0) {
    const u32x4 g = {f2u(mu), (u32)tag, f2u(r), (u32)(tag >> 32)};
    st128_sc1(rws, g, 0, WS_ST + row * 16);
  }
}
template <int NT> DI void p1_main(const P1<NT>& a, f32x16 (&acc)[NT], f32x4& cs0, f32x4& cs2, int lane, rsrc_t rws, int row0, u32x4& sg) {
  Frag<NT> f0, f1;
  const u32 one = ((lane & 15) == ((lane >> 4) & 1)) ? 0x3f803f80u : 0u;
  const bf16x8 sel = asfrag(one, one, one, one);
#pragma unroll
  for (int t = 0; t < NT; ++t) asm volatile("" : "+v"(acc[t]));
  barrier();
#pragma unroll
  for (int t = 0; t < NT; ++t) f0.w[t] = ldtr(a.wa[t], a.wa[t] + 512);
  f0.x = ld128(a.xa[0]);
  SB();
#pragma unroll
  for (int t = 0; t < NT; ++t) f1.w[t] = ldtr(a.wa[t] ^ 2048, (a.wa[t] ^ 2048) + 512);
  f1.x = ld128(a.xa[1]);
  SB();
  u32 wsl = 0, xsl = 0;
#pragma unroll 1
  for (int cp = 0; cp < 7; ++cp) {
    const u32 w1 = wsl == 4 * 16384 ? 0 : wsl + 16384;
    const u32 w2 = w1 == 4 * 16384 ? 0 : w1 + 16384;
    const u32 xn = (xsl + 16384) & 65535;
    p1_pair<NT, false>(a, f0, f1, acc, sel, cs0, cs2, w1, w2, xsl, xn);
    wsl = w2; xsl = xn;
  }
  sg = stat_load(rws, row0, lane);
  p1_pair<NT, true>(a, f0, f1, acc, sel, cs0, cs2, (15 % NW1) * 16384, 0, xsl, 0);
}

DI void p1_finish(f32x4 cs0, f32x4 cs2, int g0, int g2, bool add2, int lane) {
  if (lane < 16) {
    __builtin_amdgcn_ds_faddf((LAS float*)&lds32[(L_CS + g0 * 128 + lane * 4) >> 2], cs0[0], 0, 0, false);
    __builtin_amdgcn_ds_faddf((LAS float*)&lds32[(L_CS + g0 * 128 + 64 + lane * 4) >> 2], cs0[1], 0, 0, false);
    if (add2) {
      __builtin_amdgcn_ds_faddf((LAS float*)&lds32[(L_CS + g2 * 128 + lane * 4) >> 2], cs2[0], 0, 0, false);
      __builtin_amdgcn_ds_faddf((LAS float*)&lds32[(L_CS + g2 * 128 + 64 + lane * 4) >> 2], cs2[1], 0, 0, false);
    }
  }
  wait_lgkm0();
  barrier();
}
DI void cs_get(float (&c)[16], int gt, int lane) {
  const int h = lane >> 5;
#pragma unroll
  for (int g = 0; g < 4; ++g) {
    f32x4 v = ldf4(L_CS + gt * 128 + (8 * g + 4 * h) * 4);
    c[4 * g] = v[0]; c[4 * g + 1] = v[1]; c[4 * g + 2] = v[2]; c[4 * g + 3] = v[3];
  }
}

DI void silu_tile(const f32x16& acc, const float (&cs)[16], float nmu, float nrl, u32x4 (&o)[2]) {
  float y[16];
#pragma unroll
  for (int e = 0; e < 16; ++e) {
    const float t = fmaf(nmu, cs[e], acc[e]);
    y[e] = t * rcp(1.0f + ex2(t * nrl));
  }
#pragma unroll
  for (int u = 0; u < 2; ++u)
#pragma unroll
    for (int e = 0; e < 4; ++e) o[u][e] = pk2(y[8 * u + 2 * e], y[8 * u + 2 * e + 1]);
}

DI f32x16 phi_logits(const f32x16& acc, const float (&cs)[16], float nmu, bf16x8 a0, bf16x8 a1) {
  float t[16];
#pragma unroll
  for (int e = 0; e < 16; ++e) t[e] = fmaf(nmu, cs[e], acc[e]);
  bf16x8 b0 = asfrag(pk2(t[0], t[1]), pk2(t[2], t[3]), pk2(t[4], t[5]), pk2(t[6], t[7]));
  bf16x8 b1 = asfrag(pk2(t[8], t[9]), pk2(t[10], t[11]), pk2(t[12], t[13]), pk2(t[14], t[15]));
  f32x16 z = {};
  z = mfma32(a0, b0, z);
  z = mfma32(a1, b1, z);
  return z;
}
DI void phi_exp(const f32x16& pl, int hh, float rl, float (&ep)[8], float (&en)[8], float& zp, float& zn) {
  const int o = 8 * hh;
  float mx = fmax2(fmax2(fmax2(fmax2(fmax2(pl[o], pl[o + 1]), pl[o + 2]), fmax2(fmax2(pl[o + 3], pl[o + 4]), pl[o + 5])), pl[o + 6]), pl[o + 7]);
  float mn = fmin2(fmin2(fmin2(fmin2(fmin2(pl[o], pl[o + 1]), pl[o + 2]), fmin2(fmin2(pl[o + 3], pl[o + 4]), pl[o + 5])), pl[o + 6]), pl[o + 7]);
  xmaxmin(mx, mn);
  const float a = -rl * mx, b = rl * mn;
  zp = 0.f; zn = 0.f;
#pragma unroll
  for (int e = 0; e < 8; ++e) {
    ep[e] = ex2(fmaf(pl[8 * hh + e], rl, a));
    en[e] = ex2(fmaf(pl[8 * hh + e], -rl, b));
    zp += ep[e]; zn += en[e];
  }
  xsum2(zp, zn);
}

DI void load_wphi(const Params& p, int head0, int lane, bf16x8& a0, bf16x8& a1) {
  const int row = lane & 31, h = lane >> 5;
  const u16* wp = p.wphi + (head0 + (row >> 4)) * 256 + (row & 15);
  u32 v[4];
#pragma unroll
  for (int e = 0; e < 4; ++e) {
    const int j0 = 2 * e, j1 = 2 * e + 1;
    const int d0 = 8 * (j0 >> 2) + 4 * h + (j0 & 3), d1 = 8 * (j1 >> 2) + 4 * h + (j1 & 3);
    v[e] = (u32)wp[d0 * 16] | ((u32)wp[d1 * 16] << 16);
  }
  const bool lo = row < 16;
  a0 = asfrag(lo ? v[0] : 0u, lo ? v[1] : 0u, lo ? v[2] : 0u, lo ? v[3] : 0u);
  a1 = asfrag(lo ? 0u : v[0], lo ? 0u : v[1], lo ? 0u : v[2], lo ? 0u : v[3]);
}

DI void store_h(rsrc_t rws, int i, int rg, int tile, int lane, const u32x4 (&o)[2]) {
#pragma unroll
  for (int u = 0; u < 2; ++u)
    st128_sc1(rws, o[u], lane * 16, WS_H + ((4 * i + rg) * 112 + 2 * tile + u) * 1024);
}

#define HWIN 7
constexpr int HW = HWIN;
DI u32x4 srd(const void* ptr, u32 bytes) {
  const u64 a = (u64)ptr;
  u32x4 d = {(u32)a, (u32)(a >> 32) & 0xffffu, bytes, 0x00020000u};
  d[0] = __builtin_amdgcn_readfirstlane(d[0]); d[1] = __builtin_amdgcn_readfirstlane(d[1]);
  return d;
}
DI bf16x8 asm_gload(u32x4 rs, u32 voff, u32 soff) {
  bf16x8 v;
  asm volatile("buffer_load_dwordx4 %0, %1, %2, %3 offen sc1" : "=v"(v) : "v"(voff), "s"(rs), "s"(soff) : "memory");
  return v;
}
DI void asm_dma(u32x4 rs, u32 ldsa, u32 voff, u32 soff) {
  asm volatile("s_mov_b32 m0, %0\n\ts_nop 0\n\tbuffer_load_dwordx4 %1, %2, %3 offen lds" ::"s"(ldsa), "v"(voff), "s"(rs), "s"(soff) : "memory");
}
struct P2 { u32 wa[2], hv, wv, hs[2], w2s, wos, ld; u32x4 rws, rw2, rwo; };
constexpr int p2_ops(int m) { return (m + HW < 28 ? 2 : 0) + (m < 18 ? 2 : 0); }
constexpr int p2_younger(int n) {
  int c = 0;
  if (n < HW) { c = 2 * (HW - 1 - n); for (int m = 0; m < n; ++m) c += p2_ops(m); }
  else { c = (n - HW < 18) ? 2 : 0; for (int m = n - HW + 1; m < n; ++m) c += p2_ops(m); }
  return c;
}
DI u32 opq(u32 x) { asm volatile("" : "+v"(x)); return x; }
template <int N> DI void p2a_step(const P2& c, f32x16 (&ac)[4], bf16x8 (&hw)[HW][2], bf16x8 (&wf)[2][2], int q, u32 b0, u32 b1) {
  asm volatile("s_waitcnt vmcnt(%2)" : "+v"(hw[N % HW][0]), "+v"(hw[N % HW][1]) : "n"(p2_younger(N)) : "memory");
  if constexpr (N == 11) lds_spin(L_PF + 4 * q, 18);
  if constexpr (N + 1 < 28) {
    constexpr u32 sl = (N + 1) % 18, o = (sl & 7) * 8192;
    if constexpr ((sl & 7) == 0 || sl == 0) { b0 = opq(c.wa[0] + (sl >> 3) * 65536); b1 = opq(c.wa[1] + (sl >> 3) * 65536); }
    wf[(N + 1) & 1][0] = ldtr(b0 + o, b0 + o + 1024);
    wf[(N + 1) & 1][1] = ldtr(b1 + o, b1 + o + 1024);
  }
  SB();
  ac[0] = mfma32(wf[N & 1][0], hw[N % HW][0], ac[0]);
  ac[1] = mfma32(wf[N & 1][1], hw[N % HW][0], ac[1]);
  ac[2] = mfma32(wf[N & 1][0], hw[N % HW][1], ac[2]);
  ac[3] = mfma32(wf[N & 1][1], hw[N % HW][1], ac[3]);
  SB();
  if constexpr (N + HW < 28) {
    hw[N % HW][0] = asm_gload(c.rws, c.hv, c.hs[0] + (N + HW) * 1024);
    hw[N % HW][1] = asm_gload(c.rws, c.hv, c.hs[1] + (N + HW) * 1024);
  }
  if constexpr (N < 10) {
    asm_dma(c.rw2, c.ld + N * 8192, c.wv, c.w2s + (N + 18) * 16384);
    asm_dma(c.rw2, c.ld + N * 8192 + 1024, c.wv, c.w2s + (N + 18) * 16384 + 8192);
  } else if constexpr (N < 18) {
    asm_dma(c.rwo, c.ld + N * 8192, c.wv, c.wos + (N - 10) * 16384);
    asm_dma(c.rwo, c.ld + N * 8192 + 1024, c.wv, c.wos + (N - 10) * 16384 + 8192);
  }
  SB();
  if constexpr (N + 1 < 28) p2a_step<N + 1>(c, ac, hw, wf, q, b0, b1);
}
template <int N> DI void p2b_step(f32x16 (&ac)[4], bf16x8 (&ow)[8][2], u32 b0, u32 b1) {
  constexpr u32 o = N * 8192;
  asm volatile("s_waitcnt vmcnt(%2)" : "+v"(ow[N][0]), "+v"(ow[N][1]) : "n"(2 * (7 - N)) : "memory");
  const bf16x8 w0 = ldtr(b0 + o, b0 + o + 1024), w1 = ldtr(b1 + o, b1 + o + 1024);
  SB();
  ac[0] = mfma32(w0, ow[N][0], ac[0]);
  ac[1] = mfma32(w1, ow[N][0], ac[1]);
  ac[2] = mfma32(w0, ow[N][1], ac[2]);
  ac[3] = mfma32(w1, ow[N][1], ac[3]);
  SB();
  if constexpr (N < 7) p2b_step<N + 1>(ac, ow, b0, b1);
}
DI void red_send(const f32x16 (&ac)[4], int q, int lane, int slot0) {
  const u32 b = opq(slot0 * 8192 + q * 2048 + lane * 16);
#pragma unroll
  for (int l = 1; l < 4; ++l)
#pragma unroll
    for (int g = 0; g < 4; ++g) {
      const f32x4 v = {ac[l][4 * g], ac[l][4 * g + 1], ac[l][4 * g + 2], ac[l][4 * g + 3]};
      *(LAS f32x4*)(lds + b + (2 * (l - 1) + (g >> 1)) * 8192 + (g & 1) * 1024) = v;
    }
}
DI void red_recv(f32x16& a, int q, int lane, int slot0) {
#pragma unroll
  for (int l = 1; l < 4; ++l) {
    const u32 b = opq(slot0 * 8192 + (q ^ l) * 2048 + lane * 16);
#pragma unroll
    for (int g = 0; g < 4; ++g) {
      const f32x4 v = ldf4(b + (2 * (l - 1) + (g >> 1)) * 8192 + (g & 1) * 1024);
#pragma unroll
      for (int e = 0; e < 4; ++e) a[4 * g + e] += v[e];
    }
  }
}

DI u64 compute_prologue(const Params& p, rsrc_t rws, int w, int lane, int i, int j) {
  const int row = 128 * i + 8 * j + w;
  const u32x4 xv = *(const GAS u32x4*)((const char*)p.x + row * 1024 + lane * 16);
  const u64 tag = __hip_atomic_load((GAS u64*)(p.ws + WS_E), RLX) + 1;
  stat_publish(p, rws, row, lane, tag, xv);
  return tag;
}

DI void roleA_main(const Params& p, int w, int lane, int i, int j, int i2, int j2) {
  STAMP_DECL;
  const int rg = w, pr = j >> 1, isq = j & 1, odd = rg & 1;
  const int s = lane & 31, h = lane >> 5;
  const rsrc_t rws = mkrs(p.ws, WS_END);
  const u64 tag = compute_prologue(p, rws, w, lane, i, j);
  if (w == 0 && lane == 0) *(LAS u64*)(lds + L_TAG) = tag;
  const int t0 = (!isq && (pr & 1)) ? 5 : 4;
  const int tg[3] = {t0 + odd, t0 + 1 - odd, t0 + 2};
  P1<3> ad; ad.init(rg, lane, tg);
  f32x16 acc[3] = {};
  f32x4 cs0 = {}, cs2 = {};
  u32x4 sg;
  STAMP(0);
  p1_main(ad, acc, cs0, cs2, lane, rws, 128 * i + 32 * rg, sg);
  STAMP(1);
  p1_finish(cs0, cs2, tg[0], tg[2], !odd, lane);
  STAMP(2);
  if (PRIO == 0 || (PRIO == 1 && isq)) __builtin_amdgcn_s_setprio(1);
  float nmu, r;
  stat_get(rws, 128 * i + 32 * rg, lane, tag, sg, nmu, r);
  const float rl = r * LOG2E;
  const int Tb = 2 * h_unit0(pr, isq) - 4;
#pragma unroll
  for (int t = 0; t < 3; ++t) {
    float c16[16];
    u32x4 o[2];
    cs_get(c16, tg[t], lane);
    silu_tile(acc[t], c16, nmu, -rl, o);
    store_h(rws, i, rg, Tb + tg[t], lane, o);
  }
  STAMP(3);
  wait_vm0();
  if (arrive(C_H, lane) == (isq ? 7u : 3u)) set_flag(p.ws, WS_FH + (i * 16 + j) * 128, tag, lane);
  __builtin_amdgcn_s_setprio(0);
  const int q = rg, rb = q >> 1, cb = q & 1;
  const int orow = 64 * i2 + 32 * rb + s;
  const u32 ooff = orow * 1024 + (64 * j2 + 32 * cb) * 2 + h * 16;
  float nmu2, r2;
  stat_get(rws, 64 * i2 + 32 * rb, lane, tag, stat_load(rws, 64 * i2 + 32 * rb, lane), nmu2, r2);
  P2 c2;
  {
    const int blk = (lane >> 4) & 1, qq = (lane & 15) >> 2, pp = lane & 3;
    c2.wa[0] = q * 2048 + (4 * h + qq) * 128 + ((cb ^ (qq >> 1)) * 64) + 32 * blk + 8 * pp;
    c2.wa[1] = c2.wa[0] ^ 64;
    c2.hv = lane * 16;
    c2.wv = (lane >> 3) * 1024 + ((((lane & 7) >> 2) ^ ((lane >> 4) & 1)) * 64) + (lane & 3) * 16;
    c2.rws = srd(p.ws, WS_END); c2.rw2 = srd(p.w2, F_ * D_ * 2); c2.rwo = srd(p.wo, D_ * D_ * 2);
    c2.hs[0] = WS_H + ((2 * i2 + rb) * 112 + 28 * q) * 1024; c2.hs[1] = WS_H + ((2 * i2 + (rb ^ 1)) * 112 + 28 * q) * 1024;
    c2.w2s = 16 * 28 * q * 1024 + j2 * 128;
    c2.wos = 128 * q * 1024 + j2 * 128;
    c2.ld = ldsaddr(q * 2048);
  }
  f32x16 ac[4] = {};
  bf16x8 hw[HW][2], wf[2][2];
  STAMP(6);
  lds_spin(L_PF + 4 * q, 12);
  lds_spin(L_CNT + 4 * C_HR, 1);
  STAMP(7);
#pragma unroll
  for (int n = 0; n < HW; ++n) { hw[n][0] = asm_gload(c2.rws, c2.hv, c2.hs[0] + n * 1024); hw[n][1] = asm_gload(c2.rws, c2.hv, c2.hs[1] + n * 1024); }
  wf[0][0] = ldtr(c2.wa[0], c2.wa[0] + 1024); wf[0][1] = ldtr(c2.wa[1], c2.wa[1] + 1024);
  p2a_step<0>(c2, ac, hw, wf, q, c2.wa[0], c2.wa[1]);
  STAMP(8);
  red_send(ac, q, lane, 0);
  arrive(C_RG, lane);
  u32x4 xr[2];
#pragma unroll
  for (int u = 0; u < 2; ++u) xr[u] = *(const GAS u32x4*)((const char*)p.x + ooff + 32 * u);
  lds_spin(L_CNT + 4 * C_RG, 4);
  red_recv(ac[0], q, lane, 0);
#pragma unroll
  for (int g = 0; g < 4; ++g) {
    f32x4 v;
#pragma unroll
    for (int e = 0; e < 4; ++e) { const float gp = ac[0][4 * g + e] * r2; v[e] = gp * rcp(1.0f + ex2(-LOG2E * gp)); }
    *(LAS f32x4*)(lds + (6 + (g >> 1)) * 8192 + q * 2048 + (g & 1) * 1024 + lane * 16) = v;
  }
#pragma unroll
  for (int u = 0; u < 2; ++u) *(LAS u32x4*)(lds + 8 * 8192 + q * 2048 + u * 1024 + lane * 16) = xr[u];
  const u32 os0 = WS_O + ((2 * i2 + rb) * 32 + 8 * q) * 1024, os1 = WS_O + ((2 * i2 + (rb ^ 1)) * 32 + 8 * q) * 1024;
  lds_spin(L_CNT + 4 * C_OR, 1);
  STAMP(9);
  bf16x8 ow[8][2];
#pragma unroll
  for (int n = 0; n < 8; ++n) { ow[n][0] = asm_gload(c2.rws, c2.hv, os0 + n * 1024); ow[n][1] = asm_gload(c2.rws, c2.hv, os1 + n * 1024); }
#pragma unroll
  for (int l = 0; l < 4; ++l) ac[l] = f32x16{};
  p2b_step<0>(ac, ow, opq(c2.wa[0] + 10 * 8192), opq(c2.wa[1] + 10 * 8192));
  red_send(ac, q, lane, 10);
  arrive(C_RS, lane);
  lds_spin(L_CNT + 4 * C_RS, 4);
  red_recv(ac[0], q, lane, 10);
  const f32x16 as = ac[0];
  float G[16];
#pragma unroll
  for (int g = 0; g < 4; ++g) {
    const f32x4 v = ldf4((6 + (g >> 1)) * 8192 + q * 2048 + (g & 1) * 1024 + lane * 16);
    G[4 * g] = v[0]; G[4 * g + 1] = v[1]; G[4 * g + 2] = v[2]; G[4 * g + 3] = v[3];
  }
#pragma unroll
  for (int u = 0; u < 2; ++u) xr[u] = *(LAS u32x4*)(lds + 8 * 8192 + q * 2048 + u * 1024 + lane * 16);
  STAMP(10);
#pragma unroll
  for (int u = 0; u < 2; ++u) {
    u32x2 ra = __builtin_amdgcn_permlane32_swap(xr[u][0], xr[u][2], false, false);
    u32x2 rb_ = __builtin_amdgcn_permlane32_swap(xr[u][1], xr[u][3], false, false);
    u32 pk[2][2];
#pragma unroll
    for (int g = 0; g < 2; ++g) {
      const u32 x01 = ra[g], x23 = rb_[g];
      const int e = 8 * u + 4 * g;
      const float y0 = fmaf(as[e], G[e], u2f(x01 << 16)), y1 = fmaf(as[e + 1], G[e + 1], u2f(x01 & 0xffff0000u));
      const float y2 = fmaf(as[e + 2], G[e + 2], u2f(x23 << 16)), y3 = fmaf(as[e + 3], G[e + 3], u2f(x23 & 0xffff0000u));
      pk[g][0] = pk2(y0, y1); pk[g][1] = pk2(y2, y3);
    }
    u32x2 s0 = __builtin_amdgcn_permlane32_swap(pk[0][0], pk[1][0], false, false);
    u32x2 s1 = __builtin_amdgcn_permlane32_swap(pk[0][1], pk[1][1], false, false);
    u32x4 v = {s0[0], s1[0], s0[1], s1[1]};
    *(GAS u32x4*)((char*)p.out + ooff + 32 * u) = v;
  }
  {
    const GAS u64* pt = (const GAS u64*)(p.ws + (24u << 20));
    u64 t = 0;
    for (int q = 0; q < PG / 64 + 1; ++q) { const int id = q * 64 + lane; if (id < PG) { u64 v = __hip_atomic_load((GAS u64*)pt + id, RLX); t = v > t ? v : t; } }
    for (int o = 32; o >= 1; o >>= 1) { u64 v = __shfl_xor(t, o); t = v > t ? v : t; }
    while (__builtin_amdgcn_s_memrealtime() < t + 10000) __builtin_amdgcn_s_sleep(2);
  }
  STAMP(11);
  STAMP_DUMP;
}

DI void roleB_kv(const Params& p, int w, int lane, int i, int j) {
  STAMP_DECL;
  const int rg = w - 4, pr = j >> 1, odd = rg & 1;
  const int s = lane & 31, h = lane >> 5;
  bf16x8 a0[2], a1[2];
  load_wphi(p, 4 * pr + 2 * odd, lane, a0[0], a1[0]);
  load_wphi(p, 4 * pr + 2 - 2 * odd, lane, a0[1], a1[1]);
  const int tg[4] = {odd, 1 - odd, 2 + odd, 3 - odd};
  const rsrc_t rws = mkrs(p.ws, WS_END);
  const u64 tag = compute_prologue(p, rws, w, lane, i, j);
  P1<4> ad; ad.init(rg, lane, tg);
  f32x16 acc[4] = {};
  f32x4 cs0 = {}, cs2 = {};
  u32x4 sg;
  STAMP(0);
  p1_main(ad, acc, cs0, cs2, lane, rws, 128 * i + 32 * rg, sg);
  STAMP(1);
  p1_finish(cs0, cs2, tg[0], tg[2], true, lane);
  STAMP(2);
  if (PRIO >= 1) __builtin_amdgcn_s_setprio(1);
  float nmu, r;
  stat_get(rws, 128 * i + 32 * rg, lane, tag, sg, nmu, r);
  const float rl = r * LOG2E;
  const u32 ib = (32 * rg + s) * 64 + ((h ^ ((s >> 1) & 7)) * 8);
  const u32 ia[4] = {ib, ib ^ 16, ib ^ 32, ib ^ 48};
#pragma unroll
  for (int t = 0; t < 2; ++t) {
    float c16[16];
    cs_get(c16, tg[t], lane);
    const f32x16 pl = phi_logits(acc[t], c16, nmu, a0[t], a1[t]);
#pragma unroll
    for (int hh = 0; hh < 2; ++hh) {
      float ep_[8], en_[8], zp, zn;
      phi_exp(pl, hh, rl, ep_, en_, zp, zn);
      const float ip = rcp(zp), in = rcp(zn);
      const u32 base = L_PHI + (2 * tg[t] + hh) * 8192;
#pragma unroll
      for (int g = 0; g < 2; ++g) {
        u32x2 vp = {pk2(ep_[4 * g] * ip, ep_[4 * g + 1] * ip), pk2(ep_[4 * g + 2] * ip, ep_[4 * g + 3] * ip)};
        u32x2 vn = {pk2(en_[4 * g] * in, en_[4 * g + 1] * in), pk2(en_[4 * g + 2] * in, en_[4 * g + 3] * in)};
        *(LAS u32x2*)(lds + base + ia[g]) = vp;
        *(LAS u32x2*)(lds + base + ia[2 + g]) = vn;
      }
    }
  }
#pragma unroll
  for (int t = 0; t < 2; ++t) {
    float c16[16];
    cs_get(c16, tg[2 + t], lane);
    const u32 base = L_V + (tg[2 + t] - 2) * 8192;
#pragma unroll
    for (int g = 0; g < 4; ++g) {
      float v[4];
#pragma unroll
      for (int e = 0; e < 4; ++e) v[e] = fmaf(nmu, c16[4 * g + e], acc[2 + t][4 * g + e]) * r;
      u32x2 vv = {pk2(v[0], v[1]), pk2(v[2], v[3])};
      *(LAS u32x2*)(lds + base + ia[g]) = vv;
    }
  }
  arrive(C_PHI, lane);
  STAMP(3);
  lds_spin(L_CNT + 4 * C_PHI, 4);
  {
    const int blk = (lane >> 4) & 1, q = (lane & 15) >> 2, pp = lane & 3;
    const int r0 = 8 * h + q, r1 = r0 + 4;
    const u32 pa0 = L_PHI + rg * 8192 + r0 * 64 + (((4 * blk + pp) ^ ((r0 >> 1) & 7)) * 8);
    const u32 pa1 = L_PHI + rg * 8192 + r1 * 64 + (((4 * blk + pp) ^ ((r1 >> 1) & 7)) * 8);
    const u32 vb = L_V + (rg >> 1) * 8192;
    const u32 pv0 = vb + r0 * 64 + (((4 * (rg & 1) + pp) ^ ((r0 >> 1) & 7)) * 8);
    const u32 pv1 = vb + r1 * 64 + (((4 * (rg & 1) + pp) ^ ((r1 >> 1) & 7)) * 8);
    const bf16x8 ones = asfrag(0x3f803f80u, 0x3f803f80u, 0x3f803f80u, 0x3f803f80u);
    f32x16 d = {};
#pragma unroll
    for (int u = 0; u < 8; ++u) {
      const bf16x8 vv = ldtr(pv0 + u * 1024, pv1 + u * 1024);
      d = mfma32(ldtr(pa0 + u * 1024, pa1 + u * 1024), blk ? ones : vv, d);
    }
    asm volatile("" :: "v"(d));
    arrive(C_IMG, lane);
    if (s <= 16) {
#pragma unroll
      for (int u = 0; u < 2; ++u) {
        u32x4 v = {pk2(d[8 * u], d[8 * u + 1]), pk2(d[8 * u + 2], d[8 * u + 3]), pk2(d[8 * u + 4], d[8 * u + 5]), pk2(d[8 * u + 6], d[8 * u + 7])};
        st128_sc1(rws, v, (2 * s + h) * 16, WS_KV + (((pr * 16 + i) * 4 + rg) * 2 + u) * 544);
      }
    }
  }
  STAMP(4);
  wait_vm0();
  if (arrive(C_KV, lane) == 3) set_flag(p.ws, WS_FK + (pr * 16 + i) * 128, tag, lane);
  STAMP(5);
  STAMP_DUMP;
}

DI void roleB_q(const Params& p, int w, int lane, int i, int j) {
  STAMP_DECL;
  const int rg = w - 4, pr = j >> 1, odd = rg & 1;
  const int s = lane & 31, h = lane >> 5;
  bf16x8 a0[2], a1[2];
  load_wphi(p, 4 * pr + 2 * odd, lane, a0[0], a1[0]);
  load_wphi(p, 4 * pr + 2 - 2 * odd, lane, a0[1], a1[1]);
  const int tg[3] = {odd, 1 - odd, 7};
  const rsrc_t rws = mkrs(p.ws, WS_END);
  const u64 tag = compute_prologue(p, rws, w, lane, i, j);
  P1<3> ad; ad.init(rg, lane, tg);
  f32x16 acc[3] = {};
  f32x4 cs0 = {}, cs2 = {};
  u32x4 sg;
  STAMP(0);
  p1_main(ad, acc, cs0, cs2, lane, rws, 128 * i + 32 * rg, sg);
  STAMP(1);
  p1_finish(cs0, cs2, tg[0], tg[2], !odd, lane);
  STAMP(2);
  float nmu, r;
  stat_get(rws, 128 * i + 32 * rg, lane, tag, sg, nmu, r);
  const float rl = r * LOG2E;
  {
    float c16[16];
    u32x4 o[2];
    cs_get(c16, 7, lane);
    silu_tile(acc[2], c16, nmu, -rl, o);
    store_h(rws, i, rg, 2 * h_unit0(pr, 1) + 3, lane, o);
  }
  STAMP(3);
  bf16x8 bq[4][2];
  float zeps[4];
#pragma unroll
  for (int t = 0; t < 2; ++t) {
    float c16[16];
    cs_get(c16, tg[t], lane);
    const f32x16 pl = phi_logits(acc[t], c16, nmu, a0[t], a1[t]);
#pragma unroll
    for (int hh = 0; hh < 2; ++hh) {
      float ep_[8], en_[8], zp, zn;
      phi_exp(pl, hh, rl, ep_, en_, zp, zn);
      const float rho = zp * rcp(zn);
      bq[2 * t + hh][0] = asfrag(pk2(ep_[0], ep_[1]), pk2(ep_[2], ep_[3]), pk2(ep_[4], ep_[5]), pk2(ep_[6], ep_[7]));
      bq[2 * t + hh][1] = asfrag(pk2(en_[0] * rho, en_[1] * rho), pk2(en_[2] * rho, en_[3] * rho), pk2(en_[4] * rho, en_[5] * rho), pk2(en_[6] * rho, en_[7] * rho));
      zeps[2 * t + hh] = zp * 1e-6f;
    }
    if (t == 0) {
      wait_vm0();
      if (arrive(C_H, lane) == 7) set_flag(p.ws, WS_FH + (i * 16 + j) * 128, tag, lane);
    }
  }
  STAMP(4);
  poll16(p.ws, WS_FK + pr * 16 * 128, 128, tag, lane, 15);
  STAMP(5);
  {
    u32x4 pv[2][8];
    u32x2 pz[2];
#pragma unroll
    for (int u = 0; u < 2; ++u) {
      const u32 pbase = WS_KV + ((pr * 16 * 4 + rg) * 2 + u) * 544;
#pragma unroll
      for (int m = 0; m < 8; ++m)
        pv[u][m] = __builtin_amdgcn_raw_buffer_load_b128(rws, ((lane >> 5) * 8 * 544) + (lane & 31) * 16, pbase + 2 * m * 8 * 544, 16);
      pz[u] = __builtin_amdgcn_raw_buffer_load_b64(rws, (lane >> 2) * 8 * 544 + 512 + (lane & 3) * 8, pbase, 16);
    }
    u32 ps[4];
#pragma unroll
    for (int e = 0; e < 4; ++e) ps[e] = (s == 2 * e ? 0x00003f80u : 0u) | (s == 2 * e + 1 ? 0x3f800000u : 0u);
    const bf16x8 psel = asfrag(ps[0], ps[1], ps[2], ps[3]);
#pragma unroll
    for (int u = 0; u < 2; ++u) {
      f32x16 sm = {};
#pragma unroll
      for (int m = 0; m < 8; ++m) sm = mfma32(psel, __builtin_bit_cast(bf16x8, pv[u][m]), sm);
      float zz[4] = {u2f(pz[u][0] << 16), u2f(pz[u][0] & 0xffff0000u), u2f(pz[u][1] << 16), u2f(pz[u][1] & 0xffff0000u)};
#pragma unroll
      for (int e = 0; e < 4; ++e) {
        float v = zz[e];
        v += u2f(__builtin_amdgcn_update_dpp(0, (int)f2u(v), 0x128, 0xf, 0xf, false));
        v += u2f(__builtin_amdgcn_update_dpp(0, (int)f2u(v), 0x124, 0xf, 0xf, false));
        u32x2 t = __builtin_amdgcn_permlane16_swap(f2u(v), f2u(v), false, false);
        v = u2f(t[0]) + u2f(t[1]);
        zz[e] = xsum(v);
      }
      const u32 kb = L_KVA + (rg * 2 + u) * 544;
      { u32x2 v = {pk2(sm[0], sm[1]), pk2(sm[2], sm[3])}; *(LAS u32x2*)(lds + kb + s * 16 + h * 8) = v; }
      if (lane < 4) { u32x2 v = {pk2(zz[0], zz[1]), pk2(zz[2], zz[3])}; *(LAS u32x2*)(lds + kb + 512 + lane * 8) = v; }
    }
  }
  arrive(C_CH, lane);
  lds_spin(L_CNT + 4 * C_CH, 4);
  STAMP(6);
#pragma unroll
  for (int hh = 0; hh < 4; ++hh) {
    const u32 ao = (s < 16 ? s : 16) * 32 + h * 16;
    f32x16 d = {};
    const int ah = hh ^ (2 * odd);
    d = mfma32(ld128(L_KVA + (ah * 2) * 544 + ao), bq[hh][0], d);
    d = mfma32(ld128(L_KVA + (ah * 2 + 1) * 544 + ao), bq[hh][1], d);
    const float inv = rcp(d[8] + zeps[hh]);
    u32x4 v = {pk2(d[0] * inv, d[1] * inv), pk2(d[2] * inv, d[3] * inv), pk2(d[4] * inv, d[5] * inv), pk2(d[6] * inv, d[7] * inv)};
    st128_sc1(rws, v, lane * 16, WS_O + ((4 * i + rg) * 32 + 4 * pr + ah) * 1024);
  }
  STAMP(7);
  wait_vm0();
  if (arrive(C_O, lane) == 3) set_flag(p.ws, WS_FO + (i * 8 + pr) * 128, tag, lane);
  STAMP(8);
  STAMP_DUMP;
}

__global__ void __launch_bounds__(768, 3) fused_hedgehog_ffn(Params p) {
  const int tid = threadIdx.x, lane = tid & 63;
  const int w = __builtin_amdgcn_readfirstlane(tid >> 6);
  const int b = blockIdx.x, g = b & 7, m = b >> 3;
#define MAP 2
  const int i = MAP == 0 ? 4 * (g >> 1) + (m & 3) : MAP == 1 ? 2 * g + (m & 1) : MAP == 2 ? 8 * (g >> 2) + (m & 7) : (b >> 4);
  const int j = MAP == 0 ? 8 * (g & 1) + (m >> 2) : MAP == 1 ? (m >> 1) : MAP == 2 ? 4 * (g & 3) + (m >> 3) : (b & 15);
  if (w == 4) {
    *(LAS u32*)(lds + L_CNT + 4 * (lane & 31)) = 0;
#pragma unroll
    for (int q = 0; q < 4; ++q) *(LAS u32*)(lds + L_CS + 4 * (lane + 64 * q)) = 0;
    wait_lgkm0();
  }
#define MAP2 1
  const int i2 = MAP2 == 0 ? 2 * i + (j & 1) : 8 * (g >> 1) + (m & 7), j2 = MAP2 == 0 ? j >> 1 : 4 * (g & 1) + (m >> 3);
  if (w >= 8) loader_main(p, w, lane, i, j, i2, j2);
  else if (w < 4) roleA_main(p, w, lane, i, j, i2, j2);
  else if (j & 1) roleB_q(p, w, lane, i, j);
  else roleB_kv(p, w, lane, i, j);
}

__global__ void __launch_bounds__(PT) probe_kernel(char* ws) {
  const u64 t = __builtin_amdgcn_s_memrealtime();
  if (threadIdx.x == PT - 1) __hip_atomic_store((GAS u64*)(ws + (24u << 20)) + blockIdx.x, t, RLX);
}
extern "C" void kernel_launch(void* const* d_in, const int* in_sizes, int n_in, void* d_out, int out_size, void* d_ws, size_t ws_size,
                              hipStream_t stream) {
  Params p;
  p.x = (const u16*)d_in[0]; p.wqkv = (const u16*)d_in[1]; p.wphi = (const u16*)d_in[2]; p.wo = (const u16*)d_in[3];
  p.w1 = (const u16*)d_in[5]; p.w2 = (const u16*)d_in[6]; p.out = (u16*)d_out; p.ws = (char*)d_ws;
  probe_kernel<<<PG, PT, 0, stream>>>((char*)d_ws + 4096 * 0);
  fused_hedgehog_ffn<<<256, 768, 0, stream>>>(p);
}
```

```cpp
#include <hip/hip_runtime.h>

typedef unsigned short u16;
typedef unsigned u32;
typedef unsigned long long u64;
typedef short bf16x8 __attribute__((ext_vector_type(8)));
typedef short s16x4 __attribute__((ext_vector_type(4)));
typedef __bf16 bf2 __attribute__((ext_vector_type(2)));
typedef float f32x16 __attribute__((ext_vector_type(16)));
typedef float f32x4 __attribute__((ext_vector_type(4)));
typedef u32 u32x4 __attribute__((ext_vector_type(4)));
typedef u32 u32x2 __attribute__((ext_vector_type(2)));
#define LAS __attribute__((address_space(3)))
#define GAS __attribute__((address_space(1)))
#define DI __device__ __forceinline__
#define RLX __ATOMIC_RELAXED, __HIP_MEMORY_SCOPE_AGENT
#define RLXW __ATOMIC_RELAXED, __HIP_MEMORY_SCOPE_WORKGROUP

#define THROTTLE 0
#define PRIO 3
#define TIMING 0

constexpr int S_ = 2048, D_ = 512, F_ = 1792;
constexpr float LOG2E = 1.4426950408889634f;

constexpr u32 WS_E = 0;
constexpr u32 WS_FH = 4096;
constexpr u32 WS_FK = WS_FH + 32768;
constexpr u32 WS_FO = WS_FK + 32768;
constexpr u32 WS_ST = 102400;
constexpr u32 WS_KV = 12u << 20;
constexpr u32 WS_H = 1u << 20;
constexpr u32 WS_O = WS_H + 64u * 112u * 1024u;
constexpr u32 WS_END = WS_KV + 128u * 8u * 1024u;
constexpr u32 WS_TM = 16u << 20;

constexpr u32 W1R = 0, NW1 = 5;
constexpr u32 X1R = 81920, NX1 = 4;
constexpr u32 L_PHI = 98304, L_V = 131072;
constexpr u32 L_KVA = 147456;
constexpr u32 MISC = 157696;
constexpr u32 L_CS = MISC + 1024;
constexpr u32 L_CNT = MISC + 2048, L_TAG = MISC;
constexpr u32 L_PF = L_CNT + 64;
enum { C_H = 0, C_PHI = 1, C_KV = 2, C_CH = 3, C_O = 4, C_HS = 5, C_OR = 6, C_IMG = 7, C_RG = 8, C_RS = 9, C_KI = 10, C_KL = 11 };
constexpr u32 SPIN_CAP = 1u << 22;

__shared__ __attribute__((aligned(1024))) u32 lds32[40960];
#define lds ((char*)lds32)

DI f32x16 mfma32(bf16x8 a, bf16x8 b, f32x16 c) { return __builtin_amdgcn_mfma_f32_32x32x16_bf16(a, b, c, 0, 0, 0); }
DI u32 pk2(float a, float b) { bf2 v = {(__bf16)a, (__bf16)b}; return __builtin_bit_cast(u32, v); }
DI float dot2(u32 a, u32 b, float c) { return __builtin_amdgcn_fdot2_f32_bf16(__builtin_bit_cast(bf2, a), __builtin_bit_cast(bf2, b), c, false); }
DI float ex2(float x) { return __builtin_amdgcn_exp2f(x); }
DI float rcp(float x) { return __builtin_amdgcn_rcpf(x); }
DI float fmax2(float a, float b) { return __builtin_elementwise_maximum(a, b); }
DI float fmin2(float a, float b) { return __builtin_elementwise_minimum(a, b); }
DI u32 f2u(float x) { return __builtin_bit_cast(u32, x); }
DI float u2f(u32 x) { return __builtin_bit_cast(float, x); }
DI bf16x8 ld128(u32 off) { return *(LAS bf16x8*)(lds + off); }
DI f32x4 ldf4(u32 off) { return *(LAS f32x4*)(lds + off); }
DI float ldf(u32 off) { return *(LAS float*)(lds + off); }
DI void stf(u32 off, float v) { *(LAS float*)(lds + off) = v; }
DI bf16x8 ldtr(u32 off0, u32 off1) {
  s16x4 a = __builtin_amdgcn_ds_read_tr16_b64_v4i16((LAS s16x4*)(lds + off0));
  s16x4 b = __builtin_amdgcn_ds_read_tr16_b64_v4i16((LAS s16x4*)(lds + off1));
  return __builtin_shufflevector(a, b, 0, 1, 2, 3, 4, 5, 6, 7);
}
DI bf16x8 asfrag(u32 a, u32 b, u32 c, u32 d) { u32x4 v = {a, b, c, d}; return __builtin_bit_cast(bf16x8, v); }
DI void barrier() { asm volatile("s_barrier" ::: "memory"); }
DI void wait_lgkm0() { asm volatile("s_waitcnt lgkmcnt(0)" ::: "memory"); }
DI void wait_vm0() { asm volatile("s_waitcnt vmcnt(0)" ::: "memory"); }
template <int N> DI void wait_vm() { asm volatile("s_waitcnt vmcnt(%0)" ::"n"(N) : "memory"); }
DI float xsum(float x) { u32x2 r = __builtin_amdgcn_permlane32_swap(f2u(x), f2u(x), false, false); return u2f(r[0]) + u2f(r[1]); }
DI void xmaxmin(float& mx, float& mn) {
  u32x2 r = __builtin_amdgcn_permlane32_swap(f2u(mx), f2u(-mn), false, false);
  float m = fmax2(u2f(r[0]), u2f(r[1]));
  u32x2 q = __builtin_amdgcn_permlane32_swap(f2u(m), f2u(m), false, false);
  mx = u2f(q[0]); mn = -u2f(q[1]);
}
DI void xsum2(float& a, float& b) {
  u32x2 r = __builtin_amdgcn_permlane32_swap(f2u(a), f2u(b), false, false);
  float m = u2f(r[0]) + u2f(r[1]);
  u32x2 q = __builtin_amdgcn_permlane32_swap(f2u(m), f2u(m), false, false);
  a = u2f(q[0]); b = u2f(q[1]);
}
DI void lds_st(u32 off, u32 v) { __hip_atomic_store((LAS u32*)&lds32[off >> 2], v, RLXW); }
DI u32 ldsaddr(u32 off) { return (u32)(size_t)(LAS char*)lds + off; }
DI u32x4 asm_ld128(u32 off) {
  u32x4 v;
  asm volatile("ds_read_b128 %0, %1\n\ts_waitcnt lgkmcnt(0)" : "=&v"(v) : "v"(ldsaddr(off)) : "memory");
  return v;
}
DI void asm_st32(u32 off, u32 val) { asm volatile("ds_write_b32 %0, %1" ::"v"(ldsaddr(off)), "v"(val) : "memory"); }
DI u32 asm_ld32(u32 off) {
  u32 v;
  asm volatile("ds_read_b32 %0, %1\n\ts_waitcnt lgkmcnt(0)" : "=&v"(v) : "v"(ldsaddr(off)) : "memory");
  return v;
}
DI void lds_spin(u32 off, u32 target) {
#pragma unroll 1
  for (u32 it = 0; it < SPIN_CAP; ++it) {
    if (__builtin_amdgcn_readfirstlane(asm_ld32(off)) >= target) break;
    __builtin_amdgcn_s_sleep(1);
  }
}
DI u32 lds_add_rtn(u32 off) { return __hip_atomic_fetch_add((LAS u32*)&lds32[off >> 2], 1u, RLXW); }
DI u32 lds_ld(u32 off) { return __hip_atomic_load((LAS u32*)&lds32[off >> 2], RLXW); }
DI u32 arrive(int cnt, int lane) {
  asm volatile("" ::: "memory");
  u32 old = 0;
  if (lane == 0) old = lds_add_rtn(L_CNT + 4 * cnt);
  old = __builtin_amdgcn_readfirstlane(old);
  asm volatile("" ::: "memory");
  return old;
}
DI void poll16(const char* ws, u32 base, u32 stride, u64 tag, int lane, int mask) {
  GAS u64* p = (GAS u64*)(ws + base + (u32)(lane & mask) * stride);
#pragma unroll 1
  for (u32 it = 0; it < SPIN_CAP; ++it) {
    u64 v = __hip_atomic_load(p, RLX);
    if (__all(v == tag)) break;
    __builtin_amdgcn_s_sleep(1);
  }
  asm volatile("" ::: "memory");
}
DI void set_flag(char* ws, u32 off, u64 tag, int lane) {
  if (lane == 0) __hip_atomic_store((GAS u64*)(ws + off), tag, RLX);
}

#define STAMP(k) do { } while (0)
#define STAMP_DECL do { } while (0)
#define STAMP_DUMP do { } while (0)

struct Params { const u16* x; const u16* wqkv; const u16* wphi; const u16* wo; const u16* w1; const u16* w2; u16* out; char* ws; };

typedef __amdgpu_buffer_rsrc_t rsrc_t;
DI rsrc_t mkrs(const void* p, u32 bytes) { return __builtin_amdgcn_make_buffer_rsrc((void*)p, 0, bytes, 0x00020000); }
DI void st128_sc1(rsrc_t rs, u32x4 v, u32 voff, u32 soff) { __builtin_amdgcn_raw_buffer_store_b128(v, rs, voff + soff, 0, 16); }
template <int AUX> DI void dma16(rsrc_t rs, u32 ldsoff, u32 voff, u32 soff) {
  __builtin_amdgcn_raw_ptr_buffer_load_lds(rs, (LAS void*)(lds + ldsoff), 16, voff, soff, 0, AUX);
}

DI u32x4 srd(const void* ptr, u32 bytes) {
  const u64 a = (u64)ptr;
  u32x4 d = {(u32)a, (u32)(a >> 32) & 0xffffu, bytes, 0x00020000u};
  d[0] = __builtin_amdgcn_readfirstlane(d[0]); d[1] = __builtin_amdgcn_readfirstlane(d[1]);
  return d;
}
DI int h_unit(int p, int isq, int u) {
  const int b = (7 * p) >> 1;
  if (p & 1) return u == 3 ? b : u == 1 ? b + 2 : isq ? b + 3 : b + 1;
  return u == 3 ? b + 3 : u == 1 ? b : isq ? b + 1 : b + 2;
}
DI int h_tile(int p, int isq, int g) { return 2 * h_unit(p, isq, g >> 1) + (g & 1); }

constexpr int l1_w(int k, int nw) { return k >= 1 && k <= 11 ? nw : 0; }
constexpr int l1_x(int k) { return k == 1 ? 4 : (k >= 2 && k <= 9) ? 2 : 0; }
constexpr int l1_s(int k) { return k >= 4 && k <= 15 ? 2 : 0; }
constexpr int l1_allow(int c, int nw) {
  if (c == 2) return 4 + nw + l1_w(1, nw) + l1_x(1) - 4;
  if (c == 3) return l1_w(1, nw) + l1_x(1) + l1_w(2, nw) + l1_x(2) + l1_s(2);
  if (c >= 15) return l1_s(13) + l1_s(14);
  return (c + 1 <= 15 ? l1_x(c - 3) + l1_s(c - 3) : 0) + l1_w(c - 2, nw) + l1_x(c - 2) + l1_s(c - 2) + l1_w(c - 1, nw) + l1_x(c - 1) + l1_s(c - 1);
}
template <int C, class FW, class FX> DI void p1_loader_iter(bool isq, FW& issue_w, FX& issue_x, u32x4 (&st)[24], u32x4 rw2, u32 wv, u32 wsrc) {
  if (isq) wait_vm<l1_allow(C, 4)>(); else wait_vm<l1_allow(C, 3)>();
  barrier();
  if constexpr (C <= 11) issue_w(C + 4, ((C - 1) % 5) * 16384);
  if constexpr (C <= 9) issue_x((C + 6) >> 1, C & 1);
  if constexpr (l1_s(C) > 0) {
#pragma unroll
    for (int u = 0; u < 2; ++u) {
      constexpr int n = C - 4;
      asm volatile("buffer_load_dwordx4 %0, %1, %2, %3 offen" : "=v"(st[2 * n + u]) : "v"(wv), "s"(rw2), "s"(wsrc + n * 16384 + u * 8192) : "memory");
    }
  }
  if constexpr (C < 15) p1_loader_iter<C + 1>(isq, issue_w, issue_x, st, rw2, wv, wsrc);
}

DI void loader_main(const Params& p, int w, int lane, int i, int j, int i2, int j2) {
  constexpr int PROLE = 300;
  STAMP_DECL;
  const int wl = w - 8, pr = j >> 1, isq = j & 1;
  const rsrc_t rx = mkrs(p.x, S_ * D_ * 2), rq = mkrs(p.wqkv, D_ * 1536 * 2), r1 = mkrs(p.w1, D_ * F_ * 2);
  const u32 gsw = ((((lane & 7) >> 2) ^ ((lane >> 4) & 1)) * 64) + (lane & 3) * 16;
  const u32 xv = (lane >> 3) * 1024 + (((lane & 7) ^ ((4 * (wl & 1) + (lane >> 4)) & 7)) * 16);
  const u32 qv = (lane >> 3) * 3072 + gsw, hv1 = (lane >> 3) * 3584 + gsw;
  const u32 xs = (128 * i + 8 * wl) * 1024;
  const u32 s0 = (isq ? pr * 128 : 1024 + pr * 128) + 8 * wl * 3072, s1 = 2048 + pr * 128 + 8 * wl * 3072;
  const u32 h1 = h_unit(pr, isq, 1) * 128 + 8 * wl * 3584, h2 = h_unit(pr, isq, 2) * 128 + 8 * wl * 3584, h3 = h_unit(pr, isq, 3) * 128 + 8 * wl * 3584;
  auto issue_w = [&](int c, u32 slot) {
    const u32 d = slot + wl * 1024;
    dma16<0>(rq, d, qv, s0 + c * (32 * 3072));
    if (isq) dma16<0>(r1, d + 4096, hv1, h1 + c * (32 * 3584)); else dma16<0>(rq, d + 4096, qv, s1 + c * (32 * 3072));
    dma16<0>(r1, d + 8192, hv1, h2 + c * (32 * 3584));
    if (isq) dma16<0>(r1, d + 12288, hv1, h3 + c * (32 * 3584));
  };
  auto issue_x = [&](int P, int half) {
    const u32 d = X1R + (P & 3) * 16384 + (wl + 8 * half) * 1024;
    dma16<0>(rx, d, xv, xs + half * 65536 + P * 128);
    dma16<0>(rx, d + 4096, xv, xs + half * 65536 + 32768 + P * 128);
  };
  STAMP(0);
  issue_x(0, 0); issue_x(0, 1);
  issue_w(0, 0); issue_w(1, 16384);
  issue_x(1, 0); issue_x(1, 1);
  issue_w(2, 2 * 16384);
  STAMP(1);
  if (isq) wait_vm<8>(); else wait_vm<7>();
  barrier();
  issue_w(3, 3 * 16384);
  issue_x(2, 0); issue_x(2, 1);
  issue_w(4, 4 * 16384);
  if (isq) wait_vm<12>(); else wait_vm<10>();
  barrier();
  issue_w(5, 0);
  issue_x(3, 0); issue_x(3, 1);
  const u32x4 rw2 = srd(p.w2, F_ * D_ * 2);
  const u32 wv = (lane >> 3) * 1024 + gsw, wsrc = 16 * 28 * wl * 1024 + j2 * 128;
  u32x4 st[24];
  p1_loader_iter<2>(isq, issue_w, issue_x, st, rw2, wv, wsrc);
  STAMP(2);
  barrier();
  STAMP(3);
  asm volatile("s_waitcnt vmcnt(0)" ::: "memory");
#pragma unroll
  for (int e = 0; e < 24; ++e) asm volatile("" : "+v"(st[e]));
  const u32 da = ldsaddr(wl * 2048 + lane * 16);
#pragma unroll
  for (int e = 0; e < 24; ++e)
    asm volatile("ds_write_b128 %0, %1 offset:%c2" ::"v"(da + (e >> 4) * 65536), "v"(st[e]), "i"(((e >> 1) & 7) * 8192 + (e & 1) * 1024) : "memory");
  wait_lgkm0();
  asm_st32(L_PF + 4 * wl, 12);
  if (!isq) lds_spin(L_CNT + 4 * C_IMG, 4);
  lds_spin(L_CNT + 4 * C_HS, isq ? 8 : 4);
  {
    const rsrc_t rw2b = mkrs(p.w2, F_ * D_ * 2);
#pragma unroll 1
    for (int n = 12; n < 18; ++n) {
      dma16<0>(rw2b, n * 8192 + wl * 2048, wv, wsrc + n * 16384);
      dma16<0>(rw2b, n * 8192 + wl * 2048 + 1024, wv, wsrc + n * 16384 + 8192);
    }
  }
  wait_vm0();
  asm_st32(L_PF + 4 * wl, 18);
  STAMP(4);
  if (wl == 0) {
    const u32x4 tg = asm_ld128(L_TAG);
    const u64 tag = (u64)tg[0] | ((u64)tg[1] << 32);
    poll16(p.ws, WS_FO + (i2 >> 1) * 8 * 128, 128, tag, lane, 7);
    asm_st32(L_CNT + 4 * C_OR, 1);
    STAMP(6);
    if (blockIdx.x == 0) {
      poll16(p.ws, WS_FO, 128, tag, lane, 63);
      poll16(p.ws, WS_FO + 64 * 128, 128, tag, lane, 63);
      if (lane == 0) __hip_atomic_store((GAS u64*)(p.ws + WS_E), tag, RLX);
    }
  }
  STAMP_DUMP;
}

template <int NT> struct Frag { bf16x8 x; bf16x8 w[NT]; };

template <int NT> struct P1 {
  u32 xa[4];
  u32 wa[NT];
  DI void init(int rg, int lane, const int (&tg)[NT]) {
    const int s = lane & 31, h = lane >> 5, blk = (lane >> 4) & 1, q = (lane & 15) >> 2, pp = lane & 3, hi = rg >> 1;
#pragma unroll
    for (int k = 0; k < 4; ++k) xa[k] = X1R + (((32 * rg + s) * 128 + ((h ^ ((s >> 1) & 7)) * 16)) ^ ((k ^ hi) << 5));
#pragma unroll
    for (int t = 0; t < NT; ++t)
      wa[t] = W1R + (tg[t] >> 1) * 4096 + hi * 2048 + (8 * h + q) * 128 + (((tg[t] & 1) ^ (q >> 1)) * 64) + 32 * blk + 8 * pp;
  }
};
#define SB() __builtin_amdgcn_sched_barrier(0)
template <int NT> struct Addr { u32 w[NT]; };
template <int NT> DI Addr<NT> waddr(const P1<NT>& a, u32 slot) {
  Addr<NT> r;
#pragma unroll
  for (int t = 0; t < NT; ++t) r.w[t] = a.wa[t] + slot;
  return r;
}

template <int NT, bool CS, bool RELOAD>
DI void p1_step(Frag<NT>& f, f32x16 (&acc)[NT], bf16x8 sel, f32x4& cs0, f32x4& cs2, const Addr<NT>& wa, u32 xa, int ks) {
#pragma unroll
  for (int t = 0; t < NT; ++t) {
    acc[t] = mfma32(f.w[t], f.x, acc[t]);
    if constexpr (CS) {
      if (t == 0) cs0 = __builtin_amdgcn_mfma_f32_16x16x32_bf16(sel, f.w[0], cs0, 0, 0, 0);
      if (t == 2) cs2 = __builtin_amdgcn_mfma_f32_16x16x32_bf16(sel, f.w[2], cs2, 0, 0, 0);
    }
    SB();
    if constexpr (RELOAD) {
      const u32 ad = wa.w[t] ^ (ks << 11);
      f.w[t] = ldtr(ad, ad + 512);
      if (t == NT - 1) f.x = ld128(xa);
    }
    SB();
  }
}
template <int NT, bool LAST>
DI void p1_pair(const P1<NT>& a, Frag<NT>& f0, Frag<NT>& f1, f32x16 (&acc)[NT], bf16x8 sel, f32x4& cs0, f32x4& cs2, u32 w1, u32 w2, u32 xsl, u32 xn) {
  {
    const Addr<NT> wa = waddr(a, w1);
    p1_step<NT, true, true>(f0, acc, sel, cs0, cs2, wa, a.xa[2] + xsl, 0);
    p1_step<NT, false, true>(f1, acc, sel, cs0, cs2, wa, a.xa[3] + xsl, 1);
  }
  barrier();
  {
    const Addr<NT> wa = waddr(a, w2);
    p1_step<NT, true, !LAST>(f0, acc, sel, cs0, cs2, wa, a.xa[0] + xn, 0);
    p1_step<NT, false, !LAST>(f1, acc, sel, cs0, cs2, wa, a.xa[1] + xn, 1);
  }
  if constexpr (!LAST) barrier();
}
struct Stat { u32x4 g; };
DI u32x4 stat_load(rsrc_t rws, int row0, int lane) { return __builtin_amdgcn_raw_buffer_load_b128(rws, (lane & 31) * 16, WS_ST + row0 * 16, 16); }
DI void stat_get(rsrc_t rws, int row0, int lane, u64 tag, u32x4 g, float& nmu, float& r) {
#pragma unroll 1
  for (u32 it = 0; it < SPIN_CAP; ++it) {
    if (__all(g[1] == (u32)tag && g[3] == (u32)(tag >> 32))) break;
    __builtin_amdgcn_s_sleep(1);
    g = stat_load(rws, row0, lane);
  }
  nmu = -u2f(g[0]); r = u2f(g[2]);
}
DI void stat_publish(const Params& p, rsrc_t rws, int row, int lane, u64 tag, u32x4 xv) {
  float s0 = 0.f, s1 = 0.f, q0 = 0.f, q1 = 0.f;
#pragma unroll
  for (int e = 0; e < 4; ++e) {
    const float lo = u2f(xv[e] << 16), hi = u2f(xv[e] & 0xffff0000u);
    s0 += lo; s1 += hi; q0 = fmaf(lo, lo, q0); q1 = fmaf(hi, hi, q1);
  }
  float s = s0 + s1, q = q0 + q1;
#define DPP_ADD(v, ctrl) v += u2f(__builtin_amdgcn_update_dpp(0, (int)f2u(v), ctrl, 0xf, 0xf, false))
  DPP_ADD(s, 0x128); DPP_ADD(q, 0x128); DPP_ADD(s, 0x124); DPP_ADD(q, 0x124);
  DPP_ADD(s, 0x122); DPP_ADD(q, 0x122); DPP_ADD(s, 0x121); DPP_ADD(q, 0x121);
#undef DPP_ADD
  { u32x2 a = __builtin_amdgcn_permlane16_swap(f2u(s), f2u(q), false, false);
    float m = u2f(a[0]) + u2f(a[1]);
    u32x2 b = __builtin_amdgcn_permlane16_swap(f2u(m), f2u(m), false, false);
    s = u2f(b[0]); q = u2f(b[1]); }
  xsum2(s, q);
  const float mu = s * (1.0f / D_);
  const float var = fmaf(-mu, mu, q * (1.0f / D_));
  const float r = __builtin_amdgcn_rsqf(fmax2(var, 0.f) + 1e-5f);
  if (lane == 0) {
    const u32x4 g = {f2u(mu), (u32)tag, f2u(r), (u32)(tag >> 32)};
    st128_sc1(rws, g, 0, WS_ST + row * 16);
  }
}
template <int NT> DI void p1_main(const P1<NT>& a, f32x16 (&acc)[NT], f32x4& cs0, f32x4& cs2, int lane, rsrc_t rws, int row0, u32x4& sg, int row2, u32x4& sg2) {
  Frag<NT> f0, f1;
  const u32 one = ((lane & 15) == ((lane >> 4) & 1)) ? 0x3f803f80u : 0u;
  const bf16x8 sel = asfrag(one, one, one, one);
#pragma unroll
  for (int t = 0; t < NT; ++t) asm volatile("" : "+v"(acc[t]));
  barrier();
#pragma unroll
  for (int t = 0; t < NT; ++t) f0.w[t] = ldtr(a.wa[t], a.wa[t] + 512);
  f0.x = ld128(a.xa[0]);
  SB();
#pragma unroll
  for (int t = 0; t < NT; ++t) f1.w[t] = ldtr(a.wa[t] ^ 2048, (a.wa[t] ^ 2048) + 512);
  f1.x = ld128(a.xa[1]);
  SB();
  u32 wsl = 0, xsl = 0;
#pragma unroll 1
  for (int cp = 0; cp < 7; ++cp) {
    const u32 w1 = wsl == 4 * 16384 ? 0 : wsl + 16384;
    const u32 w2 = w1 == 4 * 16384 ? 0 : w1 + 16384;
    const u32 xn = (xsl + 16384) & 65535;
    p1_pair<NT, false>(a, f0, f1, acc, sel, cs0, cs2, w1, w2, xsl, xn);
    wsl = w2; xsl = xn;
  }
  sg = stat_load(rws, row0, lane);
  if (row2 >= 0) sg2 = stat_load(rws, row2, lane);
  p1_pair<NT, true>(a, f0, f1, acc, sel, cs0, cs2, (15 % NW1) * 16384, 0, xsl, 0);
}

DI void cs_add(f32x4 c, int g, int lane) {
  __builtin_amdgcn_ds_faddf((LAS float*)&lds32[(L_CS + g * 128 + lane * 4) >> 2], c[0], 0, 0, false);
  __builtin_amdgcn_ds_faddf((LAS float*)&lds32[(L_CS + g * 128 + 64 + lane * 4) >> 2], c[1], 0, 0, false);
}
DI void p1_finish(f32x4 cs0, f32x4 cs2, int g0, int g2, bool add2, int lane) {
  if (lane < 16) {
    cs_add(cs0, g0, lane);
    if (add2) cs_add(cs2, g2, lane);
  }
  wait_lgkm0();
  barrier();
}
DI void cs_get(f32x4 (&c)[4], int gt, int lane) {
  const int h = lane >> 5;
#pragma unroll
  for (int g = 0; g < 4; ++g) c[g] = ldf4(L_CS + gt * 128 + (8 * g + 4 * h) * 4);
}

typedef float f2 __attribute__((ext_vector_type(2)));
DI f2 mk2(float a, float b) { f2 v = {a, b}; return v; }
DI f2 sp(float a) { f2 v = {a, a}; return v; }
DI f2 fma2(f2 a, f2 b, f2 c) { return __builtin_elementwise_fma(a, b, c); }
DI f2 pr2(const f32x16& v, int e) { return mk2(v[e], v[e + 1]); }
DI f2 cs2v(const f32x4 (&c)[4], int e) { return mk2(c[e >> 1][2 * (e & 1)], c[e >> 1][2 * (e & 1) + 1]); }
DI u32 pk2v(f2 v) { return pk2(v[0], v[1]); }
DI f2 ex2v(f2 v) { return mk2(ex2(v[0]), ex2(v[1])); }
DI void silu_tile(const f32x16& acc, const f32x4 (&cs)[4], float nmu, float nrl, u32x4 (&o)[2]) {
  f2 t[8], e[8];
#pragma unroll
  for (int q = 0; q < 8; ++q) t[q] = fma2(sp(nmu), cs2v(cs, q), pr2(acc, 2 * q));
#pragma unroll
  for (int q = 0; q < 8; ++q) e[q] = ex2v(t[q] * sp(nrl)) + sp(1.0f);
#pragma unroll
  for (int q = 0; q < 8; ++q) e[q] = t[q] * mk2(rcp(e[q][0]), rcp(e[q][1]));
#pragma unroll
  for (int u = 0; u < 2; ++u)
#pragma unroll
    for (int q = 0; q < 4; ++q) o[u][q] = pk2v(e[4 * u + q]);
}
DI f32x16 phi_logits(const f32x16& acc, const f32x4 (&cs)[4], float nmu, bf16x8 a0, bf16x8 a1) {
  u32 t[8];
#pragma unroll
  for (int e = 0; e < 8; ++e) t[e] = pk2v(fma2(sp(nmu), cs2v(cs, e), pr2(acc, 2 * e)));
  f32x16 z = {};
  z = mfma32(a0, asfrag(t[0], t[1], t[2], t[3]), z);
  z = mfma32(a1, asfrag(t[4], t[5], t[6], t[7]), z);
  return z;
}
DI void xmaxmin_pair(float& m0, float& n0, float& m1, float& n1) {
  u32x2 r0 = __builtin_amdgcn_permlane32_swap(f2u(m0), f2u(-n0), false, false);
  u32x2 r1 = __builtin_amdgcn_permlane32_swap(f2u(m1), f2u(-n1), false, false);
  float a = fmax2(u2f(r0[0]), u2f(r0[1])), b = fmax2(u2f(r1[0]), u2f(r1[1]));
  u32x2 q0 = __builtin_amdgcn_permlane32_swap(f2u(a), f2u(a), false, false);
  u32x2 q1 = __builtin_amdgcn_permlane32_swap(f2u(b), f2u(b), false, false);
  m0 = u2f(q0[0]); n0 = -u2f(q0[1]); m1 = u2f(q1[0]); n1 = -u2f(q1[1]);
}
DI void xsum_pair(float& p0, float& n0, float& p1, float& n1) {
  u32x2 r0 = __builtin_amdgcn_permlane32_swap(f2u(p0), f2u(n0), false, false);
  u32x2 r1 = __builtin_amdgcn_permlane32_swap(f2u(p1), f2u(n1), false, false);
  float a = u2f(r0[0]) + u2f(r0[1]), b = u2f(r1[0]) + u2f(r1[1]);
  u32x2 q0 = __builtin_amdgcn_permlane32_swap(f2u(a), f2u(a), false, false);
  u32x2 q1 = __builtin_amdgcn_permlane32_swap(f2u(b), f2u(b), false, false);
  p0 = u2f(q0[0]); n0 = u2f(q0[1]); p1 = u2f(q1[0]); n1 = u2f(q1[1]);
}
DI float max8(const f32x16& p, int o) { return fmax2(fmax2(fmax2(fmax2(fmax2(p[o], p[o + 1]), p[o + 2]), fmax2(fmax2(p[o + 3], p[o + 4]), p[o + 5])), p[o + 6]), p[o + 7]); }
DI float min8(const f32x16& p, int o) { return fmin2(fmin2(fmin2(fmin2(fmin2(p[o], p[o + 1]), p[o + 2]), fmin2(fmin2(p[o + 3], p[o + 4]), p[o + 5])), p[o + 6]), p[o + 7]); }
DI float sum4v(const f2* e) { const f2 s = (e[0] + e[1]) + (e[2] + e[3]); return s[0] + s[1]; }
DI void phi_exp(const f32x16& pl, float rl, f2 (&ep)[8], f2 (&en)[8], float (&zp)[2], float (&zn)[2]) {
  float mx0 = max8(pl, 0), mn0 = min8(pl, 0), mx1 = max8(pl, 8), mn1 = min8(pl, 8);
  xmaxmin_pair(mx0, mn0, mx1, mn1);
  const float a[2] = {-rl * mx0, -rl * mx1}, b[2] = {rl * mn0, rl * mn1};
#pragma unroll
  for (int e = 0; e < 8; ++e) {
    ep[e] = ex2v(fma2(pr2(pl, 2 * e), sp(rl), sp(a[e >> 2])));
    en[e] = ex2v(fma2(pr2(pl, 2 * e), sp(-rl), sp(b[e >> 2])));
  }
  zp[0] = sum4v(ep); zn[0] = sum4v(en); zp[1] = sum4v(ep + 4); zn[1] = sum4v(en + 4);
  xsum_pair(zp[0], zn[0], zp[1], zn[1]);
}

DI void load_wphi(const Params& p, int head0, int lane, bf16x8& a0, bf16x8& a1) {
  const int row = lane & 31, h = lane >> 5;
  const u16* wp = p.wphi + (head0 + (row >> 4)) * 256 + (row & 15);
  u32 v[4];
#pragma unroll
  for (int e = 0; e < 4; ++e) {
    const int j0 = 2 * e, j1 = 2 * e + 1;
    const int d0 = 8 * (j0 >> 2) + 4 * h + (j0 & 3), d1 = 8 * (j1 >> 2) + 4 * h + (j1 & 3);
    v[e] = (u32)wp[d0 * 16] | ((u32)wp[d1 * 16] << 16);
  }
  const bool lo = row < 16;
  a0 = asfrag(lo ? v[0] : 0u, lo ? v[1] : 0u, lo ? v[2] : 0u, lo ? v[3] : 0u);
  a1 = asfrag(lo ? 0u : v[0], lo ? 0u : v[1], lo ? 0u : v[2], lo ? 0u : v[3]);
}

DI void store_h(rsrc_t rws, int i, int rg, int tile, int lane, const u32x4 (&o)[2]) {
#pragma unroll
  for (int u = 0; u < 2; ++u)
    st128_sc1(rws, o[u], lane * 16, WS_H + ((4 * i + rg) * 112 + 2 * tile + u) * 1024);
}

DI u32 opq(u32 x) { asm volatile("" : "+v"(x)); return x; }
DI void k_epilogue(const f32x16& acc, int gt, int pair, bf16x8 a0, bf16x8 a1, float nmu, float rl, const u32 (&ia)[4], int lane) {
  f32x4 c4[4];
  cs_get(c4, gt, lane);
  const f32x16 pl = phi_logits(acc, c4, nmu, a0, a1);
  f2 ep[8], en[8]; float zp[2], zn[2];
  phi_exp(pl, rl, ep, en, zp, zn);
  const u32 b0 = opq(L_PHI + 2 * pair * 8192 + ia[0]), b1 = b0 ^ 16, b2 = b0 ^ 32, b3 = b0 ^ 48;
#pragma unroll
  for (int hh = 0; hh < 2; ++hh) {
    const f2 ip = sp(rcp(zp[hh])), in = sp(rcp(zn[hh]));
    const f2* e = ep + 4 * hh; const f2* n = en + 4 * hh;
    const u32x2 p0 = {pk2v(e[0] * ip), pk2v(e[1] * ip)}, p1 = {pk2v(e[2] * ip), pk2v(e[3] * ip)};
    const u32x2 n0 = {pk2v(n[0] * in), pk2v(n[1] * in)}, n1 = {pk2v(n[2] * in), pk2v(n[3] * in)};
    *(LAS u32x2*)(lds + b0 + hh * 8192) = p0;
    *(LAS u32x2*)(lds + b1 + hh * 8192) = p1;
    *(LAS u32x2*)(lds + b2 + hh * 8192) = n0;
    *(LAS u32x2*)(lds + b3 + hh * 8192) = n1;
  }
}
DI void image_addr(u32 (&ia)[4], int rg, int lane) {
  const int s = lane & 31, h = lane >> 5;
  const u32 ib = (32 * rg + s) * 64 + ((h ^ ((s >> 1) & 7)) * 8);
  ia[0] = ib; ia[1] = ib ^ 16; ia[2] = ib ^ 32; ia[3] = ib ^ 48;
}

#define HWIN 7
constexpr int HW = HWIN;
DI bf16x8 asm_gload(u32x4 rs, u32 voff, u32 soff) {
  bf16x8 v;
  asm volatile("buffer_load_dwordx4 %0, %1, %2, %3 offen sc1" : "=v"(v) : "v"(voff), "s"(rs), "s"(soff) : "memory");
  return v;
}
DI void asm_dma(u32x4 rs, u32 ldsa, u32 voff, u32 soff) {
  asm volatile("s_mov_b32 m0, %0\n\ts_nop 0\n\tbuffer_load_dwordx4 %1, %2, %3 offen lds" ::"s"(ldsa), "v"(voff), "s"(rs), "s"(soff) : "memory");
}
DI bf16x8 asm_gload0(u32x4 rs, u32 voff, u32 off) {
  bf16x8 v;
  asm volatile("buffer_load_dwordx4 %0, %1, %2, 0 offen offset:%c3" : "=v"(v) : "v"(voff), "s"(rs), "i"(off) : "memory");
  return v;
}
struct P2 { u32 wa[2], hv, wv, hs[2], w2s, wos, ld; int rot; u32x4 rws, rw2, rwo; };
DI u32 p2_ks(int n, int rot) { return n + rot >= 28 ? n + rot - 28 : n + rot; }
constexpr int p2_ops(int m) { return (m + HW < 28 ? 2 : 0) + (m < 18 ? 2 : 0); }
constexpr int p2_younger(int n) {
  int c = 0;
  if (n < HW) { c = 2 * (HW - 1 - n); for (int m = 0; m < n; ++m) c += p2_ops(m); }
  else { c = (n - HW < 18) ? 2 : 0; for (int m = n - HW + 1; m < n; ++m) c += p2_ops(m); }
  return c;
}
template <int N> DI void p2a_step(const P2& c, f32x16 (&ac)[4], bf16x8 (&hw)[HW][2], bf16x8 (&wf)[2][2], int q, u32 b0, u32 b1) {
  asm volatile("s_waitcnt vmcnt(%2)" : "+v"(hw[N % HW][0]), "+v"(hw[N % HW][1]) : "n"(p2_younger(N)) : "memory");
  if constexpr (N == 11) lds_spin(L_PF + 4 * q, 18);
  if constexpr (N + 1 < 28) {
    constexpr u32 sl = (N + 1) % 18, o = (sl & 7) * 8192;
    if constexpr ((sl & 7) == 0 || sl == 0) { b0 = opq(c.wa[0] + (sl >> 3) * 65536); b1 = opq(c.wa[1] + (sl >> 3) * 65536); }
    wf[(N + 1) & 1][0] = ldtr(b0 + o, b0 + o + 1024);
    wf[(N + 1) & 1][1] = ldtr(b1 + o, b1 + o + 1024);
  }
  SB();
  ac[0] = mfma32(wf[N & 1][0], hw[N % HW][0], ac[0]);
  ac[1] = mfma32(wf[N & 1][1], hw[N % HW][0], ac[1]);
  ac[2] = mfma32(wf[N & 1][0], hw[N % HW][1], ac[2]);
  ac[3] = mfma32(wf[N & 1][1], hw[N % HW][1], ac[3]);
  SB();
  if constexpr (N + HW < 28) {
    const u32 o = p2_ks(N + HW, c.rot) * 1024;
    hw[N % HW][0] = asm_gload(c.rws, c.hv, c.hs[0] + o);
    hw[N % HW][1] = asm_gload(c.rws, c.hv, c.hs[1] + o);
  }
  if constexpr (N < 10) {
    const u32 o = c.w2s + p2_ks(N + 18, c.rot) * 16384;
    asm_dma(c.rw2, c.ld + N * 8192, c.wv, o);
    asm_dma(c.rw2, c.ld + N * 8192 + 1024, c.wv, o + 8192);
  } else if constexpr (N < 18) {
    asm_dma(c.rwo, c.ld + N * 8192, c.wv, c.wos + (N - 10) * 16384);
    asm_dma(c.rwo, c.ld + N * 8192 + 1024, c.wv, c.wos + (N - 10) * 16384 + 8192);
  }
  SB();
  if constexpr (N + 1 < 28) p2a_step<N + 1>(c, ac, hw, wf, q, b0, b1);
}
template <int N> DI void p2b_step(f32x16 (&ac)[4], bf16x8 (&ow)[8][2], bf16x8 (&wf)[2][2], u32 b0, u32 b1) {
  if constexpr (N < 7) {
    constexpr u32 o = (N + 1) * 8192;
    wf[(N + 1) & 1][0] = ldtr(b0 + o, b0 + o + 1024);
    wf[(N + 1) & 1][1] = ldtr(b1 + o, b1 + o + 1024);
  }
  asm volatile("s_waitcnt vmcnt(%2)" : "+v"(ow[N][0]), "+v"(ow[N][1]) : "n"(2 * (7 - N)) : "memory");
  SB();
  ac[0] = mfma32(wf[N & 1][0], ow[N][0], ac[0]);
  ac[1] = mfma32(wf[N & 1][1], ow[N][0], ac[1]);
  ac[2] = mfma32(wf[N & 1][0], ow[N][1], ac[2]);
  ac[3] = mfma32(wf[N & 1][1], ow[N][1], ac[3]);
  SB();
  if constexpr (N < 7) p2b_step<N + 1>(ac, ow, wf, b0, b1);
}
DI void red_send(const f32x16 (&ac)[4], int q, int lane, int slot0) {
  const u32 b = opq(slot0 * 8192 + q * 2048 + lane * 16);
#pragma unroll
  for (int l = 1; l < 4; ++l)
#pragma unroll
    for (int g = 0; g < 4; ++g) {
      const f32x4 v = {ac[l][4 * g], ac[l][4 * g + 1], ac[l][4 * g + 2], ac[l][4 * g + 3]};
      *(LAS f32x4*)(lds + b + (2 * (l - 1) + (g >> 1)) * 8192 + (g & 1) * 1024) = v;
    }
}
DI void red_recv(f32x16& a, int q, int lane, int slot0) {
#pragma unroll
  for (int l = 1; l < 4; ++l) {
    const u32 b = opq(slot0 * 8192 + (q ^ l) * 2048 + lane * 16);
#pragma unroll
    for (int g = 0; g < 4; ++g) {
      const f32x4 v = ldf4(b + (2 * (l - 1) + (g >> 1)) * 8192 + (g & 1) * 1024);
#pragma unroll
      for (int e = 0; e < 4; ++e) a[4 * g + e] += v[e];
    }
  }
}

#define PF_DATA 3
#define PF_CODE 280
DI u64 compute_prologue(const Params& p, rsrc_t rws, int w, int lane, int i, int j, u64 pc) {
  const int row = 128 * i + 8 * j + w;
  const u32x4 xv = *(const GAS u32x4*)((const char*)p.x + row * 1024 + lane * 16);
  const u64 tag = __hip_atomic_load((GAS u64*)(p.ws + WS_E), RLX) + 1;
  u32 pf1 = 0;
  if (w >= 4) {
    const int b = blockIdx.x;
    u32 t = (b * 4 + (w - 4)) * 43 + lane;
    const char* a = nullptr;
    if (lane < 43) {
      if (t < 7680) a = (const char*)p.wqkv + 192 * 3072 + t * 128;
      else if ((t -= 7680) < 8960) a = (const char*)p.w1 + 192 * 3584 + t * 128;
      else if ((t -= 8960) < 8192) a = (const char*)p.x + (t >> 2) * 1024 + 512 + (t & 3) * 128;
      else if ((t -= 8192) < 14336) a = (const char*)p.w2 + t * 128;
      else if ((t -= 14336) < 4096) a = (const char*)p.wo + t * 128;
    } else if (lane < 46 && w == 4) a = p.ws + WS_FH + (lane - 43) * 32768 + b * 128;
    if (a) pf1 = *(const volatile GAS u32*)a;
  }
  u32 pf0 = 0;
  if (w == 1) {
    const u32 line = (blockIdx.x >> 3) * 9 + lane;
    if (lane < 9 && line < PF_CODE) pf0 = *(const volatile GAS u32*)((pc & ~127ull) + line * 128);
  }
  stat_publish(p, rws, row, lane, tag, xv);
  asm volatile("" :: "v"(pf0), "v"(pf1));
  return tag;
}

DI void roleA_main(const Params& p, int w, int lane, int i, int j, int i2, int j2, u64 pc) {
  constexpr int PROLE = 0;
  STAMP_DECL;
  const int rg = w, pr = j >> 1, isq = j & 1, odd = rg & 1;
  const int s = lane & 31, h = lane >> 5;
  const rsrc_t rws = mkrs(p.ws, WS_END);
  const u64 tag = compute_prologue(p, rws, w, lane, i, j, pc);
  if (w == 0 && lane == 0) *(LAS u64*)(lds + L_TAG) = tag;
  const int hb = isq ? 2 : 4;
  const int tg[3] = {hb + odd, hb + 1 - odd, isq ? 6 + (pr & 1) : 1};
  bf16x8 a0, a1;
  load_wphi(p, 4 * pr + 2, lane, a0, a1);
  P1<3> ad; ad.init(rg, lane, tg);
  f32x16 acc[3] = {};
  f32x4 cs0 = {}, cs2 = {};
  u32x4 sg, sg2;
  STAMP(0);
  p1_main(ad, acc, cs0, cs2, lane, rws, 128 * i + 32 * rg, sg, 64 * i2 + 32 * (rg >> 1), sg2);
  STAMP(1);
  p1_finish(cs0, cs2, tg[0], tg[2], !odd, lane);
  STAMP(2);
  if (isq && (PRIO & 1)) __builtin_amdgcn_s_setprio(1);
  float nmu, r;
  stat_get(rws, 128 * i + 32 * rg, lane, tag, sg, nmu, r);
  float nmu2, r2;
  stat_get(rws, 64 * i2 + 32 * (rg >> 1), lane, tag, sg2, nmu2, r2);
  const float rl = r * LOG2E;
  if (!isq) {
    u32 ia[4];
    image_addr(ia, rg, lane);
    k_epilogue(acc[2], 1, 1, a0, a1, nmu, rl, ia, lane);
    arrive(C_PHI, lane);
  }
  STAMP(3);
#pragma unroll
  for (int t = 0; t < 2; ++t) {
    f32x4 c16[4];
    u32x4 o[2];
    cs_get(c16, tg[t], lane);
    silu_tile(acc[t], c16, nmu, -rl, o);
    store_h(rws, i, rg, h_tile(pr, isq, tg[t]), lane, o);
  }
  if (isq) {
    f32x4 c16[4];
    u32x4 o[2];
    cs_get(c16, tg[2], lane);
    silu_tile(acc[2], c16, nmu, -rl, o);
    store_h(rws, i, rg, h_tile(pr, 1, tg[2]), lane, o);
  }
  arrive(C_HS, lane);
  STAMP(4);
  wait_vm0();
  if (arrive(C_H, lane) == (isq ? 7u : 3u)) set_flag(p.ws, WS_FH + (i * 16 + j) * 128, tag, lane);
  __builtin_amdgcn_s_setprio(0);
  STAMP(12);
  const int q = rg, rb = q >> 1, cb = q & 1;
  const int orow = 64 * i2 + 32 * rb + s;
  const u32 ooff = orow * 1024 + (64 * j2 + 32 * cb) * 2 + h * 16;
  P2 c2;
  {
    const int blk = (lane >> 4) & 1, qq = (lane & 15) >> 2, pp = lane & 3;
    c2.wa[0] = q * 2048 + (4 * h + qq) * 128 + ((cb ^ (qq >> 1)) * 64) + 32 * blk + 8 * pp;
    c2.wa[1] = c2.wa[0] ^ 64;
    c2.hv = lane * 16;
    c2.wv = (lane >> 3) * 1024 + ((((lane & 7) >> 2) ^ ((lane >> 4) & 1)) * 64) + (lane & 3) * 16;
    c2.rws = srd(p.ws, WS_END); c2.rw2 = srd(p.w2, F_ * D_ * 2); c2.rwo = srd(p.wo, D_ * D_ * 2);
    c2.hs[0] = WS_H + ((2 * i2 + rb) * 112 + 28 * q) * 1024; c2.hs[1] = WS_H + ((2 * i2 + (rb ^ 1)) * 112 + 28 * q) * 1024;
    c2.w2s = 16 * 28 * q * 1024 + j2 * 128;
    c2.wos = 128 * q * 1024 + j2 * 128;
    c2.ld = ldsaddr(q * 2048);
  }
  c2.rot = 0;
  f32x16 ac[4] = {};
  bf16x8 hw[HW][2], wf[2][2];
  STAMP(6);
  lds_spin(L_PF + 4 * q, 12);
  STAMP(5);
  poll16(p.ws, WS_FH + ((i2 >> 1) * 16 + 4 * q) * 128, 128, tag, lane, 3);
  if (THROTTLE && isq) {
    const u32 ki = __builtin_amdgcn_readfirstlane(asm_ld32(L_CNT + 4 * C_KI));
    if (ki) lds_spin(L_CNT + 4 * C_KL, ki);
  }
  STAMP(7);
#pragma unroll
  for (int n = 0; n < HW; ++n) { const u32 o = p2_ks(n, c2.rot) * 1024; hw[n][0] = asm_gload(c2.rws, c2.hv, c2.hs[0] + o); hw[n][1] = asm_gload(c2.rws, c2.hv, c2.hs[1] + o); }
  wf[0][0] = ldtr(c2.wa[0], c2.wa[0] + 1024); wf[0][1] = ldtr(c2.wa[1], c2.wa[1] + 1024);
  p2a_step<0>(c2, ac, hw, wf, q, c2.wa[0], c2.wa[1]);
  STAMP(8);
  red_send(ac, q, lane, 0);
  arrive(C_RG, lane);
  const u32 os0 = WS_O + ((2 * i2 + rb) * 32 + 8 * q) * 1024, os1 = WS_O + ((2 * i2 + (rb ^ 1)) * 32 + 8 * q) * 1024;
  const u32x4 rxs = srd(p.x, S_ * D_ * 2);
  u32x4 xr[2];
  xr[0] = __builtin_bit_cast(u32x4, asm_gload0(rxs, ooff, 0)); xr[1] = __builtin_bit_cast(u32x4, asm_gload0(rxs, ooff, 32));
  lds_spin(L_CNT + 4 * C_OR, 1);
  STAMP(9);
  bf16x8 ow[8][2];
#pragma unroll
  for (int n = 0; n < 8; ++n) { ow[n][0] = asm_gload(c2.rws, c2.hv, os0 + n * 1024); ow[n][1] = asm_gload(c2.rws, c2.hv, os1 + n * 1024); }
  lds_spin(L_CNT + 4 * C_RG, 4);
  red_recv(ac[0], q, lane, 0);
#pragma unroll
  for (int g = 0; g < 4; ++g) {
    f32x4 v;
#pragma unroll
    for (int e = 0; e < 4; ++e) { const float gp = ac[0][4 * g + e] * r2; v[e] = gp * rcp(1.0f + ex2(-LOG2E * gp)); }
    *(LAS f32x4*)(lds + (6 + (g >> 1)) * 8192 + q * 2048 + (g & 1) * 1024 + lane * 16) = v;
  }
#pragma unroll
  for (int l = 0; l < 4; ++l) ac[l] = f32x16{};
  {
    const u32 b0 = opq(c2.wa[0] + 10 * 8192), b1 = opq(c2.wa[1] + 10 * 8192);
    wf[0][0] = ldtr(b0, b0 + 1024); wf[0][1] = ldtr(b1, b1 + 1024);
    p2b_step<0>(ac, ow, wf, b0, b1);
  }
  asm volatile("" : "+v"(xr[0]), "+v"(xr[1]));
  red_send(ac, q, lane, 10);
  arrive(C_RS, lane);
  lds_spin(L_CNT + 4 * C_RS, 4);
  red_recv(ac[0], q, lane, 10);
  const f32x16 as = ac[0];
  float G[16];
#pragma unroll
  for (int g = 0; g < 4; ++g) {
    const f32x4 v = ldf4((6 + (g >> 1)) * 8192 + q * 2048 + (g & 1) * 1024 + lane * 16);
    G[4 * g] = v[0]; G[4 * g + 1] = v[1]; G[4 * g + 2] = v[2]; G[4 * g + 3] = v[3];
  }
  STAMP(10);
#pragma unroll
  for (int u = 0; u < 2; ++u) {
    u32x2 ra = __builtin_amdgcn_permlane32_swap(xr[u][0], xr[u][2], false, false);
    u32x2 rb_ = __builtin_amdgcn_permlane32_swap(xr[u][1], xr[u][3], false, false);
    u32 pk[2][2];
#pragma unroll
    for (int g = 0; g < 2; ++g) {
      const u32 x01 = ra[g], x23 = rb_[g];
      const int e = 8 * u + 4 * g;
      const float y0 = fmaf(as[e], G[e], u2f(x01 << 16)), y1 = fmaf(as[e + 1], G[e + 1], u2f(x01 & 0xffff0000u));
      const float y2 = fmaf(as[e + 2], G[e + 2], u2f(x23 << 16)), y3 = fmaf(as[e + 3], G[e + 3], u2f(x23 & 0xffff0000u));
      pk[g][0] = pk2(y0, y1); pk[g][1] = pk2(y2, y3);
    }
    u32x2 s0 = __builtin_amdgcn_permlane32_swap(pk[0][0], pk[1][0], false, false);
    u32x2 s1 = __builtin_amdgcn_permlane32_swap(pk[0][1], pk[1][1], false, false);
    u32x4 v = {s0[0], s1[0], s0[1], s1[1]};
    *(GAS u32x4*)((char*)p.out + ooff + 32 * u) = v;
  }
  STAMP(11);
  STAMP_DUMP;
}

DI void roleB_kv(const Params& p, int w, int lane, int i, int j, u64 pc) {
  constexpr int PROLE = 200;
  STAMP_DECL;
  const int rg = w - 4, pr = j >> 1, odd = rg & 1;
  const int s = lane & 31, h = lane >> 5;
  bf16x8 a0, a1;
  load_wphi(p, 4 * pr, lane, a0, a1);
  const int tg[3] = {2 + odd, 3 - odd, 0};
  const rsrc_t rws = mkrs(p.ws, WS_END);
  const u64 tag = compute_prologue(p, rws, w, lane, i, j, pc);
  P1<3> ad; ad.init(rg, lane, tg);
  f32x16 acc[3] = {};
  f32x4 cs0 = {}, cs2 = {};
  u32x4 sg, sg2;
  STAMP(0);
  p1_main(ad, acc, cs0, cs2, lane, rws, 128 * i + 32 * rg, sg, -1, sg2);
  STAMP(1);
  p1_finish(cs0, cs2, tg[0], 0, !odd, lane);
  STAMP(2);
  if (PRIO & 2) __builtin_amdgcn_s_setprio(1);
  float nmu, r;
  stat_get(rws, 128 * i + 32 * rg, lane, tag, sg, nmu, r);
  const float rl = r * LOG2E;
  u32 ia[4];
  image_addr(ia, rg, lane);
  k_epilogue(acc[2], 0, 0, a0, a1, nmu, rl, ia, lane);
#pragma unroll
  for (int t = 0; t < 2; ++t) {
    f32x4 c16[4];
    cs_get(c16, tg[t], lane);
    const u32 base = L_V + (tg[t] - 2) * 8192;
#pragma unroll
    for (int g = 0; g < 4; ++g) {
      const f2 v0 = fma2(sp(nmu), cs2v(c16, 2 * g), pr2(acc[t], 4 * g)) * sp(r), v1 = fma2(sp(nmu), cs2v(c16, 2 * g + 1), pr2(acc[t], 4 * g + 2)) * sp(r);
      const u32x2 vv = {pk2v(v0), pk2v(v1)};
      *(LAS u32x2*)(lds + base + ia[g]) = vv;
    }
  }
  arrive(C_PHI, lane);
  STAMP(3);
  lds_spin(L_CNT + 4 * C_PHI, 8);
  {
    const int blk = (lane >> 4) & 1, q = (lane & 15) >> 2, pp = lane & 3;
    const int r0 = 8 * h + q, r1 = r0 + 4;
    const u32 pa0 = L_PHI + rg * 8192 + r0 * 64 + (((4 * blk + pp) ^ ((r0 >> 1) & 7)) * 8);
    const u32 pa1 = L_PHI + rg * 8192 + r1 * 64 + (((4 * blk + pp) ^ ((r1 >> 1) & 7)) * 8);
    const u32 vb = L_V + (rg >> 1) * 8192;
    const u32 pv0 = vb + r0 * 64 + (((4 * (rg & 1) + pp) ^ ((r0 >> 1) & 7)) * 8);
    const u32 pv1 = vb + r1 * 64 + (((4 * (rg & 1) + pp) ^ ((r1 >> 1) & 7)) * 8);
    const bf16x8 ones = asfrag(0x3f803f80u, 0x3f803f80u, 0x3f803f80u, 0x3f803f80u);
    f32x16 d = {};
#pragma unroll
    for (int u = 0; u < 8; ++u) {
      const bf16x8 vv = ldtr(pv0 + u * 1024, pv1 + u * 1024);
      d = mfma32(ldtr(pa0 + u * 1024, pa1 + u * 1024), blk ? ones : vv, d);
    }
    asm volatile("" :: "v"(d));
    arrive(C_IMG, lane);
#pragma unroll
    for (int u = 0; u < 2; ++u) {
      u32x4 v = {pk2(d[8 * u], d[8 * u + 1]), pk2(d[8 * u + 2], d[8 * u + 3]), pk2(d[8 * u + 4], d[8 * u + 5]), pk2(d[8 * u + 6], d[8 * u + 7])};
      st128_sc1(rws, v, (2 * s + h) * 16, WS_KV + (((pr * 16 + i) * 4 + rg) * 2 + u) * 1024);
    }
  }
  STAMP(4);
  wait_vm0();
  if (arrive(C_KV, lane) == 3) set_flag(p.ws, WS_FK + (pr * 16 + i) * 128, tag, lane);
  STAMP(5);
  STAMP_DUMP;
}

DI void roleB_q(const Params& p, int w, int lane, int i, int j, u64 pc) {
  constexpr int PROLE = 100;
  STAMP_DECL;
  const int rg = w - 4, pr = j >> 1, odd = rg & 1;
  const int s = lane & 31, h = lane >> 5;
  bf16x8 a0[2], a1[2];
  load_wphi(p, 4 * pr + 2 * odd, lane, a0[0], a1[0]);
  load_wphi(p, 4 * pr + 2 - 2 * odd, lane, a0[1], a1[1]);
  const int tg[4] = {odd, 1 - odd, 4 + odd, 5 - odd};
  const rsrc_t rws = mkrs(p.ws, WS_END);
  const u64 tag = compute_prologue(p, rws, w, lane, i, j, pc);
  P1<4> ad; ad.init(rg, lane, tg);
  f32x16 acc[4] = {};
  f32x4 cs0 = {}, cs2 = {};
  u32x4 sg, sg2;
  STAMP(0);
  p1_main(ad, acc, cs0, cs2, lane, rws, 128 * i + 32 * rg, sg, -1, sg2);
  STAMP(1);
  p1_finish(cs0, cs2, tg[0], tg[2], true, lane);
  STAMP(2);
  float nmu, r;
  stat_get(rws, 128 * i + 32 * rg, lane, tag, sg, nmu, r);
  const float rl = r * LOG2E;
#pragma unroll
  for (int t = 2; t < 4; ++t) {
    f32x4 c16[4];
    u32x4 o[2];
    cs_get(c16, tg[t], lane);
    silu_tile(acc[t], c16, nmu, -rl, o);
    store_h(rws, i, rg, h_tile(pr, 1, tg[t]), lane, o);
  }
  arrive(C_HS, lane);
  STAMP(3);
  bf16x8 bq[4][2];
  float zeps[4];
  u64 kf = 0;
#pragma unroll
  for (int t = 0; t < 2; ++t) {
    f32x4 c16[4];
    cs_get(c16, tg[t], lane);
    const f32x16 pl = phi_logits(acc[t], c16, nmu, a0[t], a1[t]);
    f2 ep[8], en[8]; float zp[2], zn[2];
    phi_exp(pl, rl, ep, en, zp, zn);
#pragma unroll
    for (int hh = 0; hh < 2; ++hh) {
      const f2 rho = sp(zp[hh] * rcp(zn[hh]));
      const f2* e = ep + 4 * hh; const f2* n = en + 4 * hh;
      bq[2 * t + hh][0] = asfrag(pk2v(e[0]), pk2v(e[1]), pk2v(e[2]), pk2v(e[3]));
      bq[2 * t + hh][1] = asfrag(pk2v(n[0] * rho), pk2v(n[1] * rho), pk2v(n[2] * rho), pk2v(n[3] * rho));
      zeps[2 * t + hh] = zp[hh] * 1e-6f;
    }
    if (t == 0) {
      wait_vm0();
      if (arrive(C_H, lane) == 7) set_flag(p.ws, WS_FH + (i * 16 + j) * 128, tag, lane);
      if (rg == 0) asm volatile("global_load_dwordx2 %0, %1, off sc1" : "=v"(kf) : "v"(p.ws + WS_FK + (pr * 16 + (lane & 15)) * 128) : "memory");
    }
  }
  STAMP(4);
  if (rg == 0) {
    asm volatile("s_waitcnt vmcnt(0)" : "+v"(kf) :: "memory");
    if (!__all(kf == tag)) poll16(p.ws, WS_FK + pr * 16 * 128, 128, tag, lane, 15);
    asm_st32(L_CNT + 4 * C_KI, 4);
  } else lds_spin(L_CNT + 4 * C_KI, 4);
  __builtin_amdgcn_s_setprio(2);
  STAMP(5);
  {
    u32x4 pv[2][8];
    u32x2 pz[2];
#pragma unroll
    for (int u = 0; u < 2; ++u) {
      const u32 pbase = WS_KV + ((pr * 16 * 4 + rg) * 2 + u) * 1024;
#pragma unroll
      for (int m = 0; m < 8; ++m)
        pv[u][m] = __builtin_amdgcn_raw_buffer_load_b128(rws, ((lane >> 5) * 8 * 1024) + (lane & 31) * 16, pbase + 2 * m * 8 * 1024, 16);
      pz[u] = __builtin_amdgcn_raw_buffer_load_b64(rws, (lane >> 2) * 8 * 1024 + 512 + (lane & 3) * 8, pbase, 16);
    }
    asm volatile("" :: "v"(pv[1][7]), "v"(pz[1]));
    arrive(C_KL, lane);
    u32 ps[4];
#pragma unroll
    for (int e = 0; e < 4; ++e) ps[e] = (s == 2 * e ? 0x00003f80u : 0u) | (s == 2 * e + 1 ? 0x3f800000u : 0u);
    const bf16x8 psel = asfrag(ps[0], ps[1], ps[2], ps[3]);
#pragma unroll
    for (int u = 0; u < 2; ++u) {
      f32x16 sm = {};
#pragma unroll
      for (int m = 0; m < 8; ++m) sm = mfma32(psel, __builtin_bit_cast(bf16x8, pv[u][m]), sm);
      float zz[4] = {u2f(pz[u][0] << 16), u2f(pz[u][0] & 0xffff0000u), u2f(pz[u][1] << 16), u2f(pz[u][1] & 0xffff0000u)};
#pragma unroll
      for (int e = 0; e < 4; ++e) {
        float v = zz[e];
        v += u2f(__builtin_amdgcn_update_dpp(0, (int)f2u(v), 0x128, 0xf, 0xf, false));
        v += u2f(__builtin_amdgcn_update_dpp(0, (int)f2u(v), 0x124, 0xf, 0xf, false));
        u32x2 t = __builtin_amdgcn_permlane16_swap(f2u(v), f2u(v), false, false);
        v = u2f(t[0]) + u2f(t[1]);
        zz[e] = xsum(v);
      }
      const u32 kb = L_KVA + (rg * 2 + u) * 544;
      { u32x2 v = {pk2(sm[0], sm[1]), pk2(sm[2], sm[3])}; *(LAS u32x2*)(lds + kb + s * 16 + h * 8) = v; }
      if (lane < 4) { u32x2 v = {pk2(zz[0], zz[1]), pk2(zz[2], zz[3])}; *(LAS u32x2*)(lds + kb + 512 + lane * 8) = v; }
    }
  }
  arrive(C_CH, lane);
  lds_spin(L_CNT + 4 * C_CH, 4);
  STAMP(6);
#pragma unroll
  for (int hh = 0; hh < 4; ++hh) {
    const u32 ao = (s < 16 ? s : 16) * 32 + h * 16;
    f32x16 d = {};
    const int ah = hh ^ (2 * odd);
    d = mfma32(ld128(L_KVA + (ah * 2) * 544 + ao), bq[hh][0], d);
    d = mfma32(ld128(L_KVA + (ah * 2 + 1) * 544 + ao), bq[hh][1], d);
    const float inv = rcp(d[8] + zeps[hh]);
    u32x4 v = {pk2(d[0] * inv, d[1] * inv), pk2(d[2] * inv, d[3] * inv), pk2(d[4] * inv, d[5] * inv), pk2(d[6] * inv, d[7] * inv)};
    st128_sc1(rws, v, lane * 16, WS_O + ((4 * i + rg) * 32 + 4 * pr + ah) * 1024);
  }
  STAMP(7);
  wait_vm0();
  if (arrive(C_O, lane) == 3) set_flag(p.ws, WS_FO + (i * 8 + pr) * 128, tag, lane);
  STAMP(8);
  STAMP_DUMP;
}

__global__ void __launch_bounds__(768, 3) fused_hedgehog_ffn(Params p) {
  u64 pc;
  asm volatile("s_getpc_b64 %0" : "=s"(pc));
  const int tid = threadIdx.x, lane = tid & 63;
  const int w = __builtin_amdgcn_readfirstlane(tid >> 6);
  const int b = blockIdx.x, g = b & 7, m = b >> 3;
#define MAP 2
  const int i = MAP == 0 ? 4 * (g >> 1) + (m & 3) : MAP == 1 ? 2 * g + (m & 1) : MAP == 2 ? 8 * (g >> 2) + (m & 7) : (b >> 4);
  const int j = MAP == 0 ? 8 * (g & 1) + (m >> 2) : MAP == 1 ? (m >> 1) : MAP == 2 ? 4 * (g & 3) + (m >> 3) : (b & 15);
  if (w == 4) {
    *(LAS u32*)(lds + L_CNT + 4 * (lane & 31)) = 0;
#pragma unroll
    for (int q = 0; q < 4; ++q) *(LAS u32*)(lds + L_CS + 4 * (lane + 64 * q)) = 0;
    wait_lgkm0();
  }
#define MAP2 1
  const int i2 = MAP2 == 0 ? 2 * i + (j & 1) : 8 * (g >> 1) + (m & 7), j2 = MAP2 == 0 ? j >> 1 : 4 * (g & 1) + (m >> 3);
  if (w >= 8) loader_main(p, w, lane, i, j, i2, j2);
  else if (w < 4) roleA_main(p, w, lane, i, j, i2, j2, pc);
  else if (j & 1) roleB_q(p, w, lane, i, j, pc);
  else roleB_kv(p, w, lane, i, j, pc);
}

extern "C" void kernel_launch(void* const* d_in, const int* in_sizes, int n_in, void* d_out, int out_size, void* d_ws, size_t ws_size,
                              hipStream_t stream) {
  Params p;
  p.x = (const u16*)d_in[0]; p.wqkv = (const u16*)d_in[1]; p.wphi = (const u16*)d_in[2]; p.wo = (const u16*)d_in[3];
  p.w1 = (const u16*)d_in[5]; p.w2 = (const u16*)d_in[6]; p.out = (u16*)d_out; p.ws = (char*)d_ws;
  fused_hedgehog_ffn<<<256, 768, 0, stream>>>(p);
}
```
